```python
import torch

def _softmax(x, axis=-1):
    return torch.softmax(x, dim=axis)
def _gelu(x):
    return torch.nn.functional.gelu(x, approximate="tanh")
def _silu(x):
    return torch.nn.functional.silu(x)
def _sigmoid(x):
    return torch.sigmoid(x)
def _layernorm(x, axis=-1, eps=1e-5):
    m = x.mean(dim=axis, keepdim=True)
    v = x.var(dim=axis, keepdim=True, unbiased=False)
    return (x - m) / torch.sqrt(v + eps)
def _scatter_add(tgt, idx, src, axis=0):
    return tgt.clone().index_add_(axis, idx, src)
def _segment_sum(src, seg, n):
    out = torch.zeros((n,) + src.shape[1:], dtype=src.dtype)
    return out.index_add_(0, seg, src)
def _pad(x, pw):
    flat = tuple(v for pair in reversed(pw) for v in pair)
    return torch.nn.functional.pad(x, flat)

kernel_name = 'concat_heads__ssm_conv_s4__grouped_kv_mqa__prompt'

def setup_inputs():

    _w = (lambda a: (lambda t: t.to(torch.bfloat16) if t.is_floating_point() else t)(torch.as_tensor(a)))
    def _A_setup():
        B, L, D, N = 1, 2048, 512, 128
        A_re = -(lambda _x: torch.exp(torch.as_tensor(_x)))((lambda *s: torch.randn(*s, dtype=torch.bfloat16) * 3.2)(D, N))
        A_im = torch.pi * (lambda *s: torch.randn(*s, dtype=torch.bfloat16) * 0.05)(D, N)
        B_re = (lambda *s: torch.randn(*s, dtype=torch.bfloat16) * 0.1)(D, N)
        B_im = (lambda *s: torch.randn(*s, dtype=torch.bfloat16) * 0.1)(D, N)
        C_re = (lambda *s: torch.randn(*s, dtype=torch.bfloat16) * 0.1)(D, N)
        C_im = (lambda *s: torch.randn(*s, dtype=torch.bfloat16) * 0.1)(D, N)
        log_dt = (lambda *s: torch.randn(*s, dtype=torch.bfloat16) * 0.05)(D) * 0.5 - 4.0
        Dskip = (lambda *s: torch.randn(*s, dtype=torch.bfloat16) * 0.05)(D)
        return {
            "u": _w((lambda *s: torch.randn(*s, dtype=torch.bfloat16) * 0.05)(B, L, D)),
            "A_re": _w(A_re),
            "A_im": _w(A_im),
            "B_re": _w(B_re),
            "B_im": _w(B_im),
            "C_re": _w(C_re),
            "C_im": _w(C_im),
            "log_dt": _w(log_dt),
            "Dskip": _w(Dskip),
        }
    _A_in = _A_setup()

    def _B_setup():
        B, S, D, H = 1, 2048, 512, 32
        Dh = D // H
        return {
            "x": _w((lambda *s: torch.randn(*s, dtype=torch.bfloat16) * 0.05)(B, S, D)),
            "wq": _w((lambda *s: torch.randn(*s, dtype=torch.bfloat16) * 0.8)(D, H * Dh)),
            "wk": _w((lambda *s: torch.randn(*s, dtype=torch.bfloat16) * 0.8)(D, Dh)),
            "wv": _w((lambda *s: torch.randn(*s, dtype=torch.bfloat16) * 0.05)(D, Dh)),
            "wo": _w((lambda *s: torch.randn(*s, dtype=torch.bfloat16) * 0.05)(H * Dh, D)),
        }
    _B_in = _B_setup()

    return {
        "A_u": _A_in["u"],
        "A_A_re": _A_in["A_re"],
        "A_A_im": _A_in["A_im"],
        "A_B_re": _A_in["B_re"],
        "A_B_im": _A_in["B_im"],
        "A_C_re": _A_in["C_re"],
        "A_C_im": _A_in["C_im"],
        "A_log_dt": _A_in["log_dt"],
        "A_Dskip": _A_in["Dskip"],
        "B_x": _B_in["x"],
        "B_wq": _B_in["wq"],
        "B_wk": _B_in["wk"],
        "B_wv": _B_in["wv"],
        "B_wo": _B_in["wo"],
    }

def reference(A_u, A_A_re, A_A_im, A_B_re, A_B_im, A_C_re, A_C_im, A_log_dt, A_Dskip, B_x, B_wq, B_wk, B_wv, B_wo):
    def _A_ref(u, A_re, A_im, B_re, B_im, C_re, C_im, log_dt, Dskip):
        Bsz, L, D = u.shape
        dt = torch.exp(log_dt)[:, None]
        A = A_re + 1j * A_im
        Bc = B_re + 1j * B_im
        Cc = C_re + 1j * C_im
        logdA = dt * A
        dB = dt * Bc
        l = (lambda x, dt: x.to(dt))(torch.arange(L), A_re.dtype)
        P = torch.exp(logdA[:, :, None] * l[None, None, :])
        K = torch.einsum("dn,dnl,dn->dl", Cc, P, dB).real
        L2 = 2 * L
        Kp = _pad(K, ((0, 0), (0, L)))
        up = (lambda x, *d: x.permute(*d))(u, 0, 2, 1)
        up = _pad(up, ((0, 0), (0, 0), (0, L)))
        Kf = (lambda x, axes=(-2,-1): torch.fft.rfft2(x.float(), dim=axes))(Kp[:, None, :], axes=(-2, -1))
        Uf = (lambda x, axes=(-2,-1): torch.fft.rfft2(x.float(), dim=axes))(up[:, :, None, :], axes=(-2, -1))
        Yf = Uf * Kf[None, :, :, :]
        y = (lambda x, s, axes=(-2,-1): torch.fft.irfft2(x, s=s, dim=axes).to(torch.bfloat16))(Yf, (1, L2), axes=(-2, -1))[:, :, 0, :L]
        y = (lambda x, *d: x.permute(*d))(y, 0, 2, 1) + u * Dskip[None, None, :]
        return y

    def _B_ref(x, wq, wk, wv, wo):
        B, S, D = x.shape
        H = 32
        Dh = D // H
        q = (lambda x, *d: x.permute(*d))((x @ wq).reshape(B, S, H, Dh), 0, 2, 1, 3)
        k = x @ wk
        v = x @ wv
        scores = torch.einsum("bhqd,bkd->bhqk", q, k) * (Dh ** -0.5)
        p = _softmax(scores, axis=-1)
        o = torch.einsum("bhqk,bkd->bhqd", p, v)
        return (lambda x, *d: x.permute(*d))(o, 0, 2, 1, 3).reshape(B, S, H * Dh) @ wo

    a = _A_ref(A_u, A_A_re, A_A_im, A_B_re, A_B_im, A_C_re, A_C_im, A_log_dt, A_Dskip)
    b = _B_ref(B_x, B_wq, B_wk, B_wv, B_wo)
    return torch.concatenate([a, b], axis=-1)
```

```cpp
#include <hip/hip_runtime.h>
#include <hip/hip_bf16.h>
typedef unsigned short u16;
__device__ __forceinline__ float bf2f(u16 x){ return __uint_as_float(((unsigned)x)<<16); }
__device__ __forceinline__ u16 f2bf(float f){ __hip_bfloat16 h = __float2bfloat16(f); return *(u16*)&h; }
__device__ __forceinline__ float rbf(float f){ return bf2f(f2bf(f)); }

__global__ void gemm_naive(const u16* A, const u16* B, u16* C, int M, int N, int K, int ldc){
  int n = blockIdx.x*blockDim.x+threadIdx.x; int m = blockIdx.y;
  if(n>=N) return;
  float s=0; for(int k=0;k<K;k++) s += bf2f(A[m*K+k])*bf2f(B[k*N+n]);
  C[m*ldc+n]=f2bf(s);
}
__global__ void kgen(const u16* Are,const u16* Aim,const u16* Bre,const u16* Bim,const u16* Cre,const u16* Cim,const u16* logdt,float* Kout){
  int d = blockIdx.y; int l = blockIdx.x*blockDim.x+threadIdx.x;
  float dt = rbf(expf(bf2f(logdt[d])));
  float lf = rbf((float)l);
  float s=0;
  for(int n=0;n<128;n++){
    float ar = dt*bf2f(Are[d*128+n]), ai = dt*bf2f(Aim[d*128+n]);
    float br = dt*bf2f(Bre[d*128+n]), bi = dt*bf2f(Bim[d*128+n]);
    float cr = bf2f(Cre[d*128+n]), ci = bf2f(Cim[d*128+n]);
    float e = expf(ar*lf); float sn,cs; sincosf(ai*lf,&sn,&cs);
    float pr=e*cs, pi=e*sn;
    float xr = cr*br-ci*bi, xi = cr*bi+ci*br;
    s += xr*pr - xi*pi;
  }
  Kout[d*2048+l]=s;
}
__global__ void conv(const u16* u,const float* Kk,const u16* Dskip,u16* out){
  int d = blockIdx.x*blockDim.x+threadIdx.x; int l = blockIdx.y;
  float s=0; for(int j=0;j<=l;j++) s += Kk[d*2048+j]*bf2f(u[(l-j)*512+d]);
  float y = rbf(s) + rbf(bf2f(u[l*512+d])*bf2f(Dskip[d]));
  out[l*1024+d]=f2bf(y);
}
__global__ void attn(const u16* q,const u16* k,const u16* v,u16* o){
  int idx = blockIdx.x*blockDim.x+threadIdx.x;
  int row = idx/32, h = idx%32;
  float qq[16]; for(int i=0;i<16;i++) qq[i]=bf2f(q[row*512+h*16+i]);
  float m=-1e30f;
  for(int j=0;j<2048;j++){ float s=0; for(int i=0;i<16;i++) s+=qq[i]*bf2f(k[j*16+i]); s*=0.25f; m=fmaxf(m,s);}
  float acc[16]; for(int i=0;i<16;i++) acc[i]=0; float l=0;
  for(int j=0;j<2048;j++){ float s=0; for(int i=0;i<16;i++) s+=qq[i]*bf2f(k[j*16+i]); s*=0.25f; float p=expf(s-m); l+=p; for(int i=0;i<16;i++) acc[i]+=p*bf2f(v[j*16+i]); }
  for(int i=0;i<16;i++) o[row*512+h*16+i]=f2bf(acc[i]/l);
}
extern "C" void kernel_launch(void* const* d_in, const int* in_sizes, int n_in, void* d_out, int out_size, void* d_ws, size_t ws_size, hipStream_t stream){
  const u16* u=(const u16*)d_in[0];
  const u16 *Are=(const u16*)d_in[1],*Aim=(const u16*)d_in[2],*Bre=(const u16*)d_in[3],*Bim=(const u16*)d_in[4],*Cre=(const u16*)d_in[5],*Cim=(const u16*)d_in[6],*logdt=(const u16*)d_in[7],*Dsk=(const u16*)d_in[8];
  const u16 *x=(const u16*)d_in[9],*wq=(const u16*)d_in[10],*wk=(const u16*)d_in[11],*wv=(const u16*)d_in[12],*wo=(const u16*)d_in[13];
  u16* out=(u16*)d_out;
  char* ws=(char*)d_ws;
  u16* q=(u16*)ws; u16* k=(u16*)(ws+(2<<20)); u16* v=(u16*)(ws+(3<<20)); u16* o=(u16*)(ws+(4<<20)); float* Kk=(float*)(ws+(8<<20));
  gemm_naive<<<dim3(2,2048),256,0,stream>>>(x,wq,q,2048,512,512,512);
  gemm_naive<<<dim3(1,2048),64,0,stream>>>(x,wk,k,2048,16,512,16);
  gemm_naive<<<dim3(1,2048),64,0,stream>>>(x,wv,v,2048,16,512,16);
  kgen<<<dim3(8,512),256,0,stream>>>(Are,Aim,Bre,Bim,Cre,Cim,logdt,Kk);
  conv<<<dim3(2,2048),256,0,stream>>>(u,Kk,Dsk,out);
  attn<<<256,256,0,stream>>>(q,k,v,o);
  gemm_naive<<<dim3(2,2048),256,0,stream>>>(o,wo,out+512,2048,512,512,1024);
}
```

```cpp
#include <hip/hip_runtime.h>
#include <hip/hip_bf16.h>
#pragma clang diagnostic ignored "-Wint-to-pointer-cast"

typedef unsigned short u16;
typedef unsigned long long u64;
typedef __attribute__((ext_vector_type(8))) short bf16x8;
typedef __attribute__((ext_vector_type(4))) short s16x4;
typedef __attribute__((ext_vector_type(16))) float f32x16;
typedef __attribute__((ext_vector_type(4))) float f32x4;
typedef __attribute__((ext_vector_type(2))) float f32x2;
typedef __attribute__((ext_vector_type(4))) unsigned u32x4;
typedef __attribute__((ext_vector_type(2))) unsigned u32x2;
typedef __attribute__((ext_vector_type(2))) __bf16 bf16x2;
#define LAS __attribute__((address_space(3)))
#define LP(T, off) ((LAS T*)(unsigned)(off))
#define SBAR() __builtin_amdgcn_sched_barrier(0)
#define PIN(x) asm volatile("" : "+v"(x))
#define MFMA32(a, b, c) __builtin_amdgcn_mfma_f32_32x32x16_bf16(a, b, c, 0, 0, 0)
#define MFMA16(a, b, c) __builtin_amdgcn_mfma_f32_16x16x32_bf16(a, b, c, 0, 0, 0)
#define AS8(x) __builtin_bit_cast(bf16x8, x)
#define EX2(v) __builtin_amdgcn_exp2f(v)
#define BAR() asm volatile("s_waitcnt lgkmcnt(0)\n\ts_barrier" ::: "memory")
#define WAITV(n) asm volatile("s_waitcnt vmcnt(" #n ")" ::: "memory")

#define STAMP 0
#define TS(i) do {} while (0)
#define TSR(i) do {} while (0)

constexpr unsigned WS_EPOCH = 0, WS_FLAG = 1024, WS_KMAX = 4096, WS_K = 8192, WS_V = WS_K + 65536, WS_O = 262144, WS_DBG = 4u << 20;
constexpr unsigned R0 = 0, R1 = 65536, R2 = 131072, LDS_BYTES = 163840;
constexpr unsigned KA = R2 + 512, KA_STRIDE = 4608;
constexpr unsigned MISC = R2 + 9728;
constexpr unsigned ONES = R2 + 10240;
constexpr unsigned CB = R2 + 12288;
constexpr unsigned UC = R1 + 32768, UC_STRIDE = 4160;
constexpr unsigned YP = R1;
constexpr float QSCALE = 0.25f * 1.4426950408889634f;
constexpr float SAFE_BOUND = 40.f;

__device__ __forceinline__ unsigned cvtpk(float lo, float hi) { f32x2 v = {lo, hi}; return __builtin_bit_cast(unsigned, __builtin_convertvector(v, bf16x2)); }
__device__ __forceinline__ float bflo(unsigned x) { return __uint_as_float(x << 16); }
__device__ __forceinline__ float bfhi(unsigned x) { return __uint_as_float(x & 0xffff0000u); }
template <int C> __device__ __forceinline__ float dpp(float v) { return __int_as_float(__builtin_amdgcn_update_dpp(0, __float_as_int(v), C, 0xf, 0xf, true)); }
__device__ __forceinline__ float x16(float v, float& o) { auto r = __builtin_amdgcn_permlane16_swap(__float_as_uint(v), __float_as_uint(v), false, false); o = __uint_as_float(r[1]); return __uint_as_float(r[0]); }
__device__ __forceinline__ float x32(float v, float& o) { auto r = __builtin_amdgcn_permlane32_swap(__float_as_uint(v), __float_as_uint(v), false, false); o = __uint_as_float(r[1]); return __uint_as_float(r[0]); }
__device__ __forceinline__ float row_sum(float v) { v += dpp<0x128>(v); v += dpp<0x124>(v); v += dpp<0x122>(v); v += dpp<0x121>(v); return v; }
__device__ __forceinline__ float row_max(float v) { v = fmaxf(v, dpp<0x128>(v)); v = fmaxf(v, dpp<0x124>(v)); v = fmaxf(v, dpp<0x122>(v)); v = fmaxf(v, dpp<0x121>(v)); return v; }
__device__ __forceinline__ float col_sum(float v) { float o; v = x16(v, o); v += o; v = x32(v, o); return v + o; }
__device__ __forceinline__ float col_max(float v) { float o; v = x16(v, o); v = fmaxf(v, o); v = x32(v, o); return fmaxf(v, o); }

template <int OFF> __device__ __forceinline__ void dma16(const void* base, unsigned voff, unsigned ldst) {
  asm volatile("s_mov_b32 m0, %2\n\ts_nop 0\n\tglobal_load_lds_dwordx4 %0, %1 offset:%3" :: "v"(voff), "s"(base), "s"(ldst), "i"(OFF) : "memory");
}
__device__ __forceinline__ void dma16x2(const void* base, unsigned voff, unsigned ldst) {
  asm volatile("s_mov_b32 m0, %2\n\ts_nop 0\n\tglobal_load_lds_dwordx4 %0, %1\n\tglobal_load_lds_dwordx4 %0, %1 offset:1024" :: "v"(voff), "s"(base), "s"(ldst) : "memory");
}
__device__ __forceinline__ void dma16x4v(const void* base, unsigned v0, unsigned v1, unsigned v2, unsigned v3, unsigned ldst) {
  asm volatile("s_mov_b32 m0, %5\n\ts_nop 0\n\tglobal_load_lds_dwordx4 %0, %4\n\tglobal_load_lds_dwordx4 %1, %4 offset:1024\n\t"
               "global_load_lds_dwordx4 %2, %4 offset:2048\n\tglobal_load_lds_dwordx4 %3, %4 offset:3072" :: "v"(v0), "v"(v1), "v"(v2), "v"(v3), "s"(base), "s"(ldst) : "memory");
}
__device__ __forceinline__ void dma16x4_sc1(const void* base, unsigned voff, unsigned ldst) {
  asm volatile("s_mov_b32 m0, %2\n\ts_nop 0\n\tglobal_load_lds_dwordx4 %0, %1 sc1\n\tglobal_load_lds_dwordx4 %0, %1 offset:1024 sc1\n\t"
               "global_load_lds_dwordx4 %0, %1 offset:2048 sc1\n\tglobal_load_lds_dwordx4 %0, %1 offset:3072 sc1" :: "v"(voff), "s"(base), "s"(ldst) : "memory");
}
__device__ __forceinline__ void dma16x2_sc1(const void* base, unsigned voff, unsigned ldst) {
  asm volatile("s_mov_b32 m0, %2\n\ts_nop 0\n\tglobal_load_lds_dwordx4 %0, %1 sc1\n\tglobal_load_lds_dwordx4 %0, %1 offset:1024 sc1" :: "v"(voff), "s"(base), "s"(ldst) : "memory");
}
__device__ __forceinline__ void dma_wtile(const u16* wb, unsigned vo, unsigned ldst) {
  dma16x4v(wb, vo, vo + 7168, vo + 2 * 7168, vo + 3 * 7168, ldst);
  dma16x4v(wb + 16384, vo, vo + 7168, vo + 2 * 7168, vo + 3 * 7168, ldst + 4096);
}
template <int OFF> __device__ __forceinline__ void gld16(u32x4& d, const void* base, unsigned voff) { asm volatile("global_load_dwordx4 %0, %1, %2 offset:%3" : "=v"(d) : "v"(voff), "s"(base), "i"(OFF) : "memory"); }
template <int OFF> __device__ __forceinline__ void gld16_sc1(u32x4& d, const void* base, unsigned voff) { asm volatile("global_load_dwordx4 %0, %1, %2 offset:%3 sc1" : "=v"(d) : "v"(voff), "s"(base), "i"(OFF) : "memory"); }
template <int OFF> __device__ __forceinline__ void gld4(unsigned& d, const void* base, unsigned voff) { asm volatile("global_load_dword %0, %1, %2 offset:%3" : "=v"(d) : "v"(voff), "s"(base), "i"(OFF) : "memory"); }
__device__ __forceinline__ void gld2(unsigned& d, const void* base, unsigned voff) { asm volatile("global_load_ushort %0, %1, %2" : "=v"(d) : "v"(voff), "s"(base) : "memory"); }
__device__ __forceinline__ void gld8_sc1(u32x2& d, const void* base, unsigned voff) { asm volatile("global_load_dwordx2 %0, %1, %2 sc1" : "=v"(d) : "v"(voff), "s"(base) : "memory"); }
__device__ __forceinline__ void gst16_sc1(void* base, unsigned voff, u32x4 d) { asm volatile("global_store_dwordx4 %0, %1, %2 sc1\n\ts_nop 1" :: "v"(voff), "v"(d), "s"(base) : "memory"); }
__device__ __forceinline__ void gst8_sc1(void* base, unsigned voff, u32x2 d) { asm volatile("global_store_dwordx2 %0, %1, %2 sc1" :: "v"(voff), "v"(d), "s"(base) : "memory"); }
__device__ __forceinline__ void gst4_sc1(void* base, unsigned voff, unsigned d) { asm volatile("global_store_dword %0, %1, %2 sc1" :: "v"(voff), "v"(d), "s"(base) : "memory"); }
__device__ __forceinline__ s16x4 trd(unsigned a) { return __builtin_bit_cast(s16x4, __builtin_amdgcn_ds_read_tr16_b64_v4i16(LP(s16x4, a))); }
__device__ __forceinline__ bf16x8 trd2(unsigned a0, unsigned a1) { s16x4 x = trd(a0), y = trd(a1); return (bf16x8){x[0], x[1], x[2], x[3], y[0], y[1], y[2], y[3]}; }

template <bool FR> __device__ __forceinline__ f32x2 zpow(f32x2 ab, float e) {
  f32x2 t = ab * e;
  float m = EX2(t.x);
  float a = FR ? __builtin_amdgcn_fractf(t.y) : t.y;
  f32x2 cs = {__builtin_amdgcn_cosf(a), __builtin_amdgcn_sinf(a)};
  return cs * m;
}

__global__ void __launch_bounds__(512, 2)
fused_kernel(const u16* __restrict__ u, const u16* __restrict__ Are, const u16* __restrict__ Aim, const u16* __restrict__ Bre, const u16* __restrict__ Bim,
             const u16* __restrict__ Cre, const u16* __restrict__ Cim, const u16* __restrict__ logdt, const u16* __restrict__ Dsk,
             const u16* __restrict__ x, const u16* __restrict__ wq, const u16* __restrict__ wk, const u16* __restrict__ wv, const u16* __restrict__ wo,
             u16* __restrict__ out, char* __restrict__ ws) {
  extern __shared__ __attribute__((aligned(16))) char lds_dyn[];
  const int tid = threadIdx.x, lane = tid & 63;
  const int w = __builtin_amdgcn_readfirstlane(tid >> 6);
  const int bid = blockIdx.x, xcd = bid & 7, loc = bid >> 3;
  const int rg = xcd * 4 + (loc >> 3), hg = loc & 7, row0 = rg * 64, ch0 = xcd * 64 + loc * 2;
  const int r16 = lane & 15, g = lane >> 4, r32 = lane & 31, h = lane >> 5;
  TSR(0); TS(1);

  const unsigned slice = R2 + 4096 * w;
  u32x2 ep; gld8_sc1(ep, ws + WS_EPOCH, 0);
  const unsigned oslot = WS_O + (((rg * 2 + ((w >> 1) & 1)) * 32) + 4 * hg + 2 * (w & 1) + (w >> 2)) * 1024 + 16 * lane;
  gst16_sc1(ws, oslot, (u32x4){~0u, ~0u, ~0u, ~0u});
  u32x4 xk0, xk1;
  { const unsigned vo = (row0 + 8 * hg + (r16 & 7)) * 1024 + 128 * w + 16 * g;
    gld16<0>(xk0, x, vo); gld16<64>(xk1, x, vo); }
  dma16x2(wk + 1024 * w, 16 * lane, slice); dma16x2(wv + 1024 * w, 16 * lane, slice + 2048);
  unsigned p_ar, p_ai, p_br, p_bi, p_cr, p_ci, p_dt;
  { const unsigned t = tid & 255, d = ch0 + (t >> 7), vo = (d * 128 + (t & 127)) * 2;
    gld2(p_ar, Are, vo); gld2(p_ai, Aim, vo); gld2(p_br, Bre, vo); gld2(p_bi, Bim, vo); gld2(p_cr, Cre, vo); gld2(p_ci, Cim, vo); gld2(p_dt, logdt, d * 2); }
  const unsigned wvo = (lane >> 3) * 1024 + 16 * ((lane & 7) ^ (((lane >> 4) & 1) << 2));
  auto big_dma = [&]() {
    const u16* xb = x + (row0 + 8 * w) * 512; const unsigned xl = R0 + 8192 * w; const unsigned sw = (8 * w) & 15;
    const unsigned xv = 16 * (lane ^ sw);
    dma16x4v(xb, xv, xv ^ 16, xv ^ 32, xv ^ 48, xl); dma16x4v(xb + 2048, xv ^ 64, xv ^ 80, xv ^ 96, xv ^ 112, xl + 4096);
    dma_wtile(wq + (64 * w) * 512 + 64 * hg, wvo, R1 + 8192 * w);
  };
  if (w != 7) { big_dma(); WAITV(23); } else WAITV(7);
  asm volatile("" : "+v"(xk0), "+v"(xk1), "+v"(ep) :: "memory");
  TS(2);
  {
    const unsigned ta = slice + 256 * g + 8 * r16;
    bf16x8 wk0 = trd2(ta, ta + 128), wk1 = trd2(ta + 1024, ta + 1152), wv0 = trd2(ta + 2048, ta + 2176), wv1 = trd2(ta + 3072, ta + 3200);
    f32x4 dk = {0, 0, 0, 0}, dv = {0, 0, 0, 0};
    dk = MFMA16(wk0, AS8(xk0), dk); dv = MFMA16(AS8(xk0), wv0, dv);
    dk = MFMA16(wk1, AS8(xk1), dk); dv = MFMA16(AS8(xk1), wv1, dv);
    PIN(dk); PIN(dv);
    *LP(f32x4, slice + 16 * lane) = dk; *LP(f32x4, slice + 1024 + 16 * lane) = dv;
  }
  if (w != 7) WAITV(16); else WAITV(0);
  asm volatile("" : "+v"(p_ar), "+v"(p_ai), "+v"(p_br), "+v"(p_bi), "+v"(p_cr), "+v"(p_ci), "+v"(p_dt) :: "memory");
  if (w < 4) {
    const float dt = bflo(cvtpk(expf(bflo(p_dt)), 0.f));
    const float al = (dt * bflo(p_ar)) * 1.4426950408889634f;
    float be = (dt * bflo(p_ai)) * 0.15915494309189535f; be -= __builtin_rintf(be);
    const float br = dt * bflo(p_br), bi = dt * bflo(p_bi), cr = bflo(p_cr), ci = bflo(p_ci);
    const float xr = cr * br - ci * bi, xi = cr * bi + ci * br;
    *LP(f32x4, slice + 2048 + 16 * lane) = (f32x4){al, be, xr, xi};
    const float s = col_sum(row_sum(xr));
    if (lane == 0) *LP(float, slice + 3072) = s;
  }
  BAR();
  TS(3);
  const u64 tag = (((u64)ep[1] << 32) | ep[0]) + 1;
  if (w == 7) {
    unsigned rb = R2 + 16 * lane; PIN(rb);
    f32x4 dk = *LP(f32x4, rb), dv = *LP(f32x4, rb + 1024);
#pragma unroll
    for (int s = 1; s < 8; ++s) { dk += *LP(f32x4, rb + 4096 * s); dv += *LP(f32x4, rb + 4096 * s + 1024); }
    const int kk0 = row0 + 8 * hg, t = kk0 >> 5, c = (kk0 >> 3) & 3;
    const u32x2 kp = {cvtpk(dk[0], dk[1]), cvtpk(dk[2], dk[3])}, vp = {cvtpk(dv[0], dv[1]), cvtpk(dv[2], dv[3])};
    if (r16 < 8) gst8_sc1(ws, WS_K + t * 1024 + ((g & 1) * 32 + 8 * c + r16) * 16 + 8 * (g >> 1), kp);
    if (g < 2) gst8_sc1(ws, WS_V + t * 1024 + 512 * (c >> 1) + 256 * g + 16 * r16 + 8 * (c & 1), vp);
    const float k2 = row_max(col_sum(dk[0] * dk[0] + dk[1] * dk[1] + dk[2] * dk[2] + dk[3] * dk[3]));
    if (lane == 0) gst4_sc1(ws, WS_KMAX + 4 * bid, __float_as_uint(k2));
    WAITV(0);
    if (lane == 0) gst8_sc1(ws, WS_FLAG + 8 * bid, (u32x2){(unsigned)tag, (unsigned)(tag >> 32)});
    big_dma();
  }
  TS(4);
  f32x4 kacc[6];
  {
    const float eB = (float)(16 * r16), e1 = (float)(r16 + 1);
    const float e2 = (float)(r16 < 8 ? 258 + 2 * r16 : 516 + 256 * ((r16 - 8) >> 2) + 4 * (r16 & 3));
    const float e3 = (float)(1032 + 256 * ((r16 & 7) >> 1) + 8 * (r16 & 1));
    const unsigned pa = R2 + 4096 * (w >> 2) + 2048 + 16 * ((16 * w + 4 * g) & 63);
    u32x4 fb[2], f1[2], f2[2], f3;
#pragma unroll
    for (int c = 0; c < 2; ++c)
#pragma unroll
      for (int i = 0; i < 4; ++i) {
        const f32x4 P = *LP(f32x4, pa + 8192 * c + 16 * i);
        const f32x2 ab = {P[0], P[1]};
        const f32x2 wv_ = zpow<false>(ab, eB);
        fb[c][i] = cvtpk(P[2] * wv_.x - P[3] * wv_.y, P[2] * wv_.y + P[3] * wv_.x);
        const f32x2 a1 = zpow<false>(ab, e1); f1[c][i] = cvtpk(a1.x, -a1.y);
        const f32x2 a2 = zpow<true>(ab, e2); f2[c][i] = cvtpk(a2.x, -a2.y);
      }
#pragma unroll
    for (int i = 0; i < 4; ++i) {
      const f32x2 ab = *LP(f32x2, pa + 8192 * (r16 >> 3) + 16 * i);
      const f32x2 a3 = zpow<true>(ab, e3); f3[i] = cvtpk(a3.x, -a3.y);
    }
    const f32x4 z4 = {0, 0, 0, 0};
#pragma unroll
    for (int c = 0; c < 2; ++c) {
      kacc[3 * c + 0] = MFMA16(AS8(f1[c]), AS8(fb[c]), z4);
      kacc[3 * c + 1] = MFMA16(AS8(f2[c]), AS8(fb[c]), z4);
      kacc[3 * c + 2] = MFMA16(AS8(f3), AS8(fb[c]), z4);
    }
#pragma unroll
    for (int i = 0; i < 6; ++i) PIN(kacc[i]);
  }
  const float k0part = *LP(float, R2 + 4096 * (2 * (w & 1)) + 3072) + *LP(float, R2 + 4096 * (2 * (w & 1) + 1) + 3072);
  TS(5);
  WAITV(0); BAR();
  TS(6);
  if (w < 4) {
    const int rh = w >> 1, hp = w & 1;
    const unsigned xa = R0 + 1024 * (32 * rh + r32) + 16 * (h ^ r16);
    const int q = r16 >> 2, p = lane & 3;
    const unsigned wa = R1 + 128 * (8 * h + q) + 16 * ((4 * hp + 2 * (g & 1) + (p >> 1)) ^ ((q >> 1) << 2)) + 8 * (p & 1);
    f32x16 acc = {0};
#pragma unroll
    for (int ks = 0; ks < 32; ++ks) {
      const bf16x8 b = *LP(bf16x8, (xa ^ ((ks & 7) << 5)) + 256 * (ks >> 3));
      const bf16x8 a = trd2(wa + 2048 * ks, wa + 2048 * ks + 512);
      acc = MFMA32(a, b, acc);
    }
    acc *= QSCALE;
    float n0 = 0, n1 = 0;
#pragma unroll
    for (int i = 0; i < 8; ++i) { n0 += acc[i] * acc[i]; n1 += acc[8 + i] * acc[8 + i]; }
    float o; n0 = x32(n0, o); n0 += o; n1 = x32(n1, o); n1 += o;
    n0 = __builtin_amdgcn_sqrtf(n0); n1 = __builtin_amdgcn_sqrtf(n1);
    const unsigned qs = R2 + 4096 * (4 + w) + 2048 + 16 * lane;
    *LP(u32x4, qs) = (u32x4){cvtpk(acc[0], acc[1]), cvtpk(acc[2], acc[3]), cvtpk(acc[4], acc[5]), cvtpk(acc[6], acc[7])};
    *LP(u32x4, qs + 1024) = (u32x4){cvtpk(acc[8], acc[9]), cvtpk(acc[10], acc[11]), cvtpk(acc[12], acc[13]), cvtpk(acc[14], acc[15])};
    *LP(f32x2, R2 + 4096 * (4 + w) + 8 * lane) = (f32x2){n0, n1};
    const float nm = col_max(row_max(fmaxf(n0, n1)));
    if (lane == 0) *LP(float, R2 + 4096 * (4 + w) + 1024) = nm;
  } else if (w == 4) {
    for (unsigned spins = 0; spins < (1u << 20); ++spins) {
      u32x4 f0, f1; gld16_sc1<0>(f0, ws + WS_FLAG, 32 * lane); gld16_sc1<16>(f1, ws + WS_FLAG, 32 * lane);
      asm volatile("s_waitcnt vmcnt(0)" : "+v"(f0), "+v"(f1) :: "memory");
      const unsigned lo = (unsigned)tag, hi = (unsigned)(tag >> 32);
      const bool ok = f0[0] == lo && f0[1] == hi && f0[2] == lo && f0[3] == hi && f1[0] == lo && f1[1] == hi && f1[2] == lo && f1[3] == hi;
      if (__all(ok)) break;
      __builtin_amdgcn_s_sleep(2);
    }
    u32x4 km; gld16_sc1<0>(km, ws + WS_KMAX, 16 * lane);
    asm volatile("s_waitcnt vmcnt(0)" : "+v"(km) :: "memory");
    float m = fmaxf(fmaxf(__uint_as_float(km[0]), __uint_as_float(km[1])), fmaxf(__uint_as_float(km[2]), __uint_as_float(km[3])));
    m = __builtin_amdgcn_sqrtf(col_max(row_max(m))) * 1.01f;
    if (lane == 0) *LP(float, R2 + 4096 * 4 + 1028) = m;
    if (bid == 0 && lane == 0) gst8_sc1(ws, WS_EPOCH, (u32x2){(unsigned)tag, (unsigned)(tag >> 32)});
  }
  TS(7);
  BAR();
  TS(8);
  {
    const unsigned o0 = (w >> 1) * 32768 + (w & 1) * 10240;
    const char* src = ws + WS_K + o0; const unsigned vo = 16 * lane;
    dma16x4_sc1(src, vo, o0); dma16x4_sc1(src + 4096, vo, o0 + 4096); dma16x2_sc1(src + 8192, vo, o0 + 8192);
  }
  const int kh = w >> 2, qt = w & 3;
  const bf16x8 qA = *LP(bf16x8, R2 + 4096 * (4 + qt) + 2048 + 16 * lane), qB = *LP(bf16x8, R2 + 4096 * (4 + qt) + 3072 + 16 * lane);
  const f32x2 qn = *LP(f32x2, R2 + 4096 * (4 + qt) + 8 * lane);
  const float kmax = *LP(float, R2 + 4096 * 4 + 1028);
  const float qmax = fmaxf(fmaxf(*LP(float, R2 + 4096 * 4 + 1024), *LP(float, R2 + 4096 * 5 + 1024)), fmaxf(*LP(float, R2 + 4096 * 6 + 1024), *LP(float, R2 + 4096 * 7 + 1024)));
  const bool unsafe = (unsigned)__builtin_amdgcn_readfirstlane(__float_as_int(qmax * kmax)) > __float_as_uint(SAFE_BOUND);
  {
    const unsigned pb = (w >> 1) * 32768 + 20480 + (w & 1) * 6144 + 16 * lane;
#pragma unroll
    for (int i = 0; i < 6; ++i) *LP(f32x4, pb + 1024 * i) = kacc[i];
  }
  BAR();
  TS(9);
  if (w < 6) {
    f32x4 v = {0, 0, 0, 0};
#pragma unroll
    for (int s = 0; s < 8; ++s) v += *LP(f32x4, (s >> 1) * 32768 + 20480 + (s & 1) * 6144 + 1024 * w + 16 * lane);
    const int ch = w >= 3, tau = w - 3 * ch;
    const unsigned kb = KA + KA_STRIDE * ch + 480;
    const unsigned c01 = cvtpk(v[0], v[1]), c23 = cvtpk(v[2], v[3]);
    const u16 b0 = (u16)c01, b1 = (u16)(c01 >> 16), b2 = (u16)c23, b3 = (u16)(c23 >> 16);
    if (tau == 0) {
      const unsigned a = kb + 2 * (4 * g + 1 + 16 * r16);
      *LP(u16, a) = b0; *LP(u16, a + 2) = b1; *LP(u16, a + 4) = b2; *LP(u16, a + 6) = b3;
      if (lane == 63) *LP(u16, a + 8) = b3;
    } else if (tau == 1) {
      const bool up = g >= 2;
      const unsigned a0 = kb + 2 * ((up ? 516 + 256 * (g - 2) : 258 + 8 * g) + 16 * r16), st = up ? 8 : 4;
      const unsigned a1 = a0 + st, a2 = a1 + st, a3 = a2 + st;
      *LP(u16, a0) = b0; *LP(u16, a2) = b2;
      *LP(u16, a1 - 2) = b1; *LP(u16, a1) = b1; *LP(u16, a1 + 2) = b1;
      *LP(u16, a3 - 2) = b3; *LP(u16, a3) = b3; *LP(u16, a3 + 2) = b3;
      if (up) {
        *LP(u16, a0 - 2) = b0; *LP(u16, a0 + 2) = b0; *LP(u16, a2 - 2) = b2; *LP(u16, a2 + 2) = b2;
        *LP(u16, a1 - 4) = b1; *LP(u16, a1 + 4) = b1; *LP(u16, a3 - 4) = b3; *LP(u16, a3 + 4) = b3;
      }
      if (lane == 31) *LP(u16, a3 + 4) = b3;
      if (lane == 63) { *LP(u16, a3 + 6) = b3; *LP(u16, a3 + 8) = b3; }
    } else if ((g >> 1) == ch) {
      const unsigned a = kb + 2 * (1032 + 512 * (g & 1) + 16 * r16);
      const unsigned d0 = cvtpk(v[0], v[0]), d1 = cvtpk(v[1], v[1]), d2 = cvtpk(v[2], v[2]), d3 = cvtpk(v[3], v[3]);
      *LP(u16, a - 6) = b0; *LP(unsigned, a - 4) = d0; *LP(u32x2, a) = (u32x2){d0, d0};
      *LP(u32x2, a + 16 - 8) = (u32x2){d1, d1}; *LP(u32x2, a + 16) = (u32x2){d1, d1}; *LP(u16, a + 16 + 8) = b1;
      *LP(u16, a + 512 - 6) = b2; *LP(unsigned, a + 512 - 4) = d2; *LP(u32x2, a + 512) = (u32x2){d2, d2};
      *LP(u32x2, a + 528 - 8) = (u32x2){d3, d3}; *LP(u32x2, a + 528) = (u32x2){d3, d3}; *LP(u16, a + 528 + 8) = b3;
    }
  } else {
    const unsigned kb = KA + KA_STRIDE * (w & 1);
    if (lane < 60) *LP(u32x2, kb + 8 * lane) = (u32x2){0, 0};
    if (lane == 60) *LP(u16, kb + 480) = (u16)cvtpk(k0part, 0.f);
    if (lane < 4) *LP(u32x4, ONES + 64 * (w & 1) + 512 * lane) = (u32x4){0x3f803f80u, 0x3f803f80u, 0x3f803f80u, 0x3f803f80u};
  }
  TS(10);
  WAITV(0); BAR();
  TS(11);
  {
    const unsigned o0 = (w >> 1) * 32768 + 20480 + (w & 1) * 6144;
    const char* src = ws + WS_K + o0; const unsigned vo = 16 * lane;
    dma16x4_sc1(src, vo, o0); dma16x2_sc1(src + 4096, vo, o0 + 4096);
  }
  unsigned ur0, ur1, ur2, ur3;
  { const unsigned vo = tid * 4096 + 2 * ch0; gld4<0>(ur0, u, vo); gld4<1024>(ur1, u, vo); gld4<2048>(ur2, u, vo); gld4<3072>(ur3, u, vo); }
  unsigned dsk; gld4<0>(dsk, Dsk, 2 * ch0);

  float shA = qn.x * kmax, shB = qn.y * kmax;
  if (unsafe) {
    WAITV(0); BAR();
    const f32x16 z = {0};
    float mA = -INFINITY, mB = -INFINITY;
    for (int t = 0; t < 64; ++t) {
      const bf16x8 kf = *LP(bf16x8, R0 + 1024 * t + 16 * lane);
      const f32x16 a = MFMA32(kf, qA, z), b = MFMA32(kf, qB, z);
#pragma unroll
      for (int i = 0; i < 16; ++i) { mA = fmaxf(mA, a[i]); mB = fmaxf(mB, b[i]); }
    }
    float o; mA = x32(mA, o); shA = fmaxf(mA, o); mB = x32(mB, o); shB = fmaxf(mB, o);
  }
  f32x16 oA = {0}, oB = {0};
  {
    f32x16 mA, mB;
#pragma unroll
    for (int i = 0; i < 16; ++i) { mA[i] = -shA; mB[i] = -shB; }
    PIN(mA); PIN(mB);
    const bool lo = (lane & 16) == 0;
    unsigned kp = R0 + kh * 32768 + 16 * lane;
    unsigned vp = lo ? R1 + kh * 32768 + 256 * h + 16 * r16 : ONES + ((r32 >= 20 && r32 < 28) ? 64 : 0);
    const unsigned vinc = lo ? 2048 : 0;
    f32x16 sA, sB = mB; u32x4 pA0, pA1, pB0 = {0, 0, 0, 0}, pB1 = {0, 0, 0, 0};
    bf16x8 kf = *LP(bf16x8, kp), kfn, v0, v1, w0, w1;
    w0 = *LP(bf16x8, vp); w1 = w0;
    sA = MFMA32(kf, qA, mA);
#pragma unroll
    for (int i = 8; i < 16; ++i) sB[i] = 0.f;
#define CV(P, j, S, i) P[j] = cvtpk(S[i], S[i + 1])
#define E4(S, i) do { S[i] = EX2(S[i]); S[i + 1] = EX2(S[i + 1]); S[i + 2] = EX2(S[i + 2]); S[i + 3] = EX2(S[i + 3]); \
                      asm volatile("" : "+v"(S[i]), "+v"(S[i + 1]), "+v"(S[i + 2]), "+v"(S[i + 3])); SBAR(); } while (0)
#define UNIT(S, P0, T, R1_, M1, M2, M3, LD) do { \
      E4(S, 0); CV(R1_, 0, T, 8); CV(R1_, 1, T, 10); CV(R1_, 2, T, 12); CV(R1_, 3, T, 14); PIN(R1_); SBAR(); \
      M1; LD; SBAR(); E4(S, 4); M2; SBAR(); E4(S, 8); M3; SBAR(); \
      CV(P0, 0, S, 0); CV(P0, 1, S, 2); CV(P0, 2, S, 4); CV(P0, 3, S, 6); PIN(P0); SBAR(); E4(S, 12); } while (0)
    for (int t = 0; t < 32; t += 2) {
      if (t == 16) { WAITV(0); BAR(); }
      UNIT(sA, pA0, sB, pB1, sB = MFMA32(kf, qB, mB), oB = MFMA32(w0, AS8(pB0), oB), oB = MFMA32(w1, AS8(pB1), oB),
           { kfn = *LP(bf16x8, kp + 1024); v0 = *LP(bf16x8, vp); v1 = *LP(bf16x8, vp + 512); });
      UNIT(sB, pB0, sA, pA1, sA = MFMA32(kfn, qA, mA), oA = MFMA32(v0, AS8(pA0), oA), oA = MFMA32(v1, AS8(pA1), oA), {});
      UNIT(sA, pA0, sB, pB1, sB = MFMA32(kfn, qB, mB), oB = MFMA32(v0, AS8(pB0), oB), oB = MFMA32(v1, AS8(pB1), oB),
           { kf = *LP(bf16x8, kp + 2048); w0 = *LP(bf16x8, vp + 1024); w1 = *LP(bf16x8, vp + 1536); });
      UNIT(sB, pB0, sA, pA1, sA = MFMA32(kf, qA, mA), oA = MFMA32(w0, AS8(pA0), oA), oA = MFMA32(w1, AS8(pA1), oA), { kp += 2048; vp += vinc; });
    }
    CV(pB1, 0, sB, 8); CV(pB1, 1, sB, 10); CV(pB1, 2, sB, 12); CV(pB1, 3, sB, 14);
    oB = MFMA32(w0, AS8(pB0), oB); oB = MFMA32(w1, AS8(pB1), oB);
  }
  TS(12);
  {
    const f32x16 snd = kh ? oA : oB;
    const unsigned cb = CB + 2304 * w;
    *LP(f32x4, cb + 16 * lane) = (f32x4){snd[0], snd[1], snd[2], snd[3]};
    *LP(f32x4, cb + 1024 + 16 * lane) = (f32x4){snd[4], snd[5], snd[6], snd[7]};
    *LP(float, cb + 2048 + 4 * lane) = snd[8];
  }
  BAR();
  TS(13);
  {
    const f32x16 kp_ = kh ? oB : oA;
    const unsigned cb = CB + 2304 * (w ^ 4);
    const f32x4 a = *LP(f32x4, cb + 16 * lane), b = *LP(f32x4, cb + 1024 + 16 * lane);
    const float rl = __builtin_amdgcn_rcpf(kp_[8] + *LP(float, cb + 2048 + 4 * lane));
    u32x4 o = {cvtpk((kp_[0] + a[0]) * rl, (kp_[1] + a[1]) * rl), cvtpk((kp_[2] + a[2]) * rl, (kp_[3] + a[3]) * rl),
               cvtpk((kp_[4] + b[0]) * rl, (kp_[5] + b[1]) * rl), cvtpk((kp_[6] + b[2]) * rl, (kp_[7] + b[3]) * rl)};
#pragma unroll
    for (int i = 0; i < 4; ++i) o[i] = min(o[i], 0xfffffffeu);
    gst16_sc1(ws, oslot, o);
  }
  dma_wtile(wo + (64 * w) * 512 + 64 * hg, wvo, R0 + 8192 * w);
  asm volatile("" : "+v"(ur0), "+v"(ur1), "+v"(ur2), "+v"(ur3), "+v"(dsk));
  {
    const unsigned ub = UC + 2 * (2045 - 4 * tid);
#pragma unroll
    for (int s = 0; s < 4; ++s) {
      const unsigned a = ub + UC_STRIDE * s + 2 * s, b = a + 4 * UC_STRIDE;
      *LP(u16, a + 6) = (u16)ur0; *LP(u16, a + 4) = (u16)ur1; *LP(u16, a + 2) = (u16)ur2; *LP(u16, a) = (u16)ur3;
      *LP(u16, b + 6) = (u16)(ur0 >> 16); *LP(u16, b + 4) = (u16)(ur1 >> 16); *LP(u16, b + 2) = (u16)(ur2 >> 16); *LP(u16, b) = (u16)(ur3 >> 16);
    }
    if (tid < 128) { const int cp = tid >> 4; *LP(u16, UC + UC_STRIDE * cp + 2 * (2049 + (cp & 3) + (tid & 15))) = 0; }
  }
  BAR();
  TS(14);
  {
    const int ch = w >> 2, cs = (w >> 1) & 1, rs = w & 1;
    unsigned ba = KA + KA_STRIDE * ch + 2 * (224 + 16 * r16 + 8 * g - 128 * rs);
    unsigned aa = UC + UC_STRIDE * (4 * ch + (r16 & 3)) + 2 * (2032 - 128 * rs - (r16 & 12) + 8 * g) - 3584;
    f32x4 y0 = {0, 0, 0, 0}, y1 = y0, y2 = y0, y3 = y0;
    for (int rr = 0; rr < 4; ++rr) {
      bf16x8 A[8], B[8];
#pragma unroll
      for (int i = 0; i < 8; ++i) {
        const s16x4 lo_ = *LP(s16x4, aa + 512 * (7 - i)), hi_ = *LP(volatile s16x4, aa + 512 * (7 - i) + 8);
        A[i] = (bf16x8){lo_[0], lo_[1], lo_[2], lo_[3], hi_[0], hi_[1], hi_[2], hi_[3]};
        B[i] = *LP(bf16x8, ba + 512 * i);
      }
      if (cs == 0) {
        y0 = MFMA16(A[0], B[0], y0);
#pragma unroll
        for (int t = 0; t <= 3; ++t) y1 = MFMA16(A[t], B[3 - t], y1);
#pragma unroll
        for (int t = 0; t <= 4; ++t) y2 = MFMA16(A[t], B[4 - t], y2);
#pragma unroll
        for (int t = 0; t <= 7; ++t) y3 = MFMA16(A[t], B[7 - t], y3);
      } else {
#pragma unroll
        for (int t = 0; t <= 1; ++t) y0 = MFMA16(A[t], B[1 - t], y0);
#pragma unroll
        for (int t = 0; t <= 2; ++t) y1 = MFMA16(A[t], B[2 - t], y1);
#pragma unroll
        for (int t = 0; t <= 5; ++t) y2 = MFMA16(A[t], B[5 - t], y2);
#pragma unroll
        for (int t = 0; t <= 6; ++t) y3 = MFMA16(A[t], B[6 - t], y3);
      }
      ba -= 64; aa -= 64;
    }
    const unsigned yb = YP + (ch * 2 + rs) * 8192 + 64 * r16 + 16 * g;
    *LP(f32x4, yb + 1024 * (cs ? 1 : 0)) = y0; *LP(f32x4, yb + 1024 * (cs ? 2 : 3)) = y1;
    *LP(f32x4, yb + 1024 * (cs ? 5 : 4)) = y2; *LP(f32x4, yb + 1024 * (cs ? 6 : 7)) = y3;
  }
  TS(15);
  WAITV(0); BAR();
  TS(16);
  const int nh = w & 1, rh = (w >> 1) & 1, k2 = w >> 2;
  u32x4 of[16];
  const char* osrc = ws + WS_O + ((rg * 2 + rh) * 32 + 16 * k2) * 1024;
#define LOAD_O() do { \
    gld16_sc1<0>(of[0], osrc, 16 * lane); gld16_sc1<1024>(of[1], osrc, 16 * lane); gld16_sc1<2048>(of[2], osrc, 16 * lane); gld16_sc1<3072>(of[3], osrc, 16 * lane); \
    gld16_sc1<0>(of[4], osrc + 4096, 16 * lane); gld16_sc1<1024>(of[5], osrc + 4096, 16 * lane); gld16_sc1<2048>(of[6], osrc + 4096, 16 * lane); gld16_sc1<3072>(of[7], osrc + 4096, 16 * lane); \
    gld16_sc1<0>(of[8], osrc + 8192, 16 * lane); gld16_sc1<1024>(of[9], osrc + 8192, 16 * lane); gld16_sc1<2048>(of[10], osrc + 8192, 16 * lane); gld16_sc1<3072>(of[11], osrc + 8192, 16 * lane); \
    gld16_sc1<0>(of[12], osrc + 12288, 16 * lane); gld16_sc1<1024>(of[13], osrc + 12288, 16 * lane); gld16_sc1<2048>(of[14], osrc + 12288, 16 * lane); gld16_sc1<3072>(of[15], osrc + 12288, 16 * lane); } while (0)
#define WAIT_O(n) asm volatile("s_waitcnt vmcnt(" #n ")" : "+v"(of[0]), "+v"(of[1]), "+v"(of[2]), "+v"(of[3]), "+v"(of[4]), "+v"(of[5]), "+v"(of[6]), "+v"(of[7]), \
    "+v"(of[8]), "+v"(of[9]), "+v"(of[10]), "+v"(of[11]), "+v"(of[12]), "+v"(of[13]), "+v"(of[14]), "+v"(of[15]) :: "memory")
  LOAD_O();
  {
    const f32x4 a0 = *LP(f32x4, YP + 16 * tid), a1 = *LP(f32x4, YP + 8192 + 16 * tid), b0 = *LP(f32x4, YP + 16384 + 16 * tid), b1 = *LP(f32x4, YP + 24576 + 16 * tid);
    const float d0 = bflo(dsk), d1 = bfhi(dsk);
    const unsigned uu[4] = {ur0, ur1, ur2, ur3};
    char* ob = (char*)out + tid * 8192 + 2 * ch0;
#pragma unroll
    for (int m = 0; m < 4; ++m) {
      const unsigned yv = cvtpk(a0[m] + a1[m], b0[m] + b1[m]), sv = cvtpk(bflo(uu[m]) * d0, bfhi(uu[m]) * d1);
      const unsigned r = cvtpk(bflo(yv) + bflo(sv), bfhi(yv) + bfhi(sv));
      asm volatile("global_store_dword %0, %1, off" :: "v"(ob + 2048 * m), "v"(r) : "memory");
    }
  }
  WAIT_O(4);
  for (unsigned spins = 0; spins < (1u << 20); ++spins) {
    unsigned mx = 0;
#pragma unroll
    for (int i = 0; i < 16; ++i) mx = max(mx, max(max(of[i][0], of[i][1]), max(of[i][2], of[i][3])));
    if (!__any(mx == 0xffffffffu)) break;
    __builtin_amdgcn_s_sleep(1);
    LOAD_O(); WAIT_O(0);
  }
  TS(17);
  {
    const int q = r16 >> 2, p = lane & 3;
    const unsigned wa = R0 + 32768 * k2 + 128 * (4 * h + q) + 16 * ((4 * nh + 2 * (g & 1) + (p >> 1)) ^ ((q >> 1) << 2)) + 8 * (p & 1);
    f32x16 acc = {0};
#pragma unroll
    for (int ks = 0; ks < 16; ++ks) acc = MFMA32(trd2(wa + 2048 * ks, wa + 2048 * ks + 1024), AS8(of[ks]), acc);
    const unsigned cb = CB + 2048 * w + 16 * lane;
    if (k2) { *LP(f32x4, cb) = (f32x4){acc[0], acc[1], acc[2], acc[3]}; *LP(f32x4, cb + 1024) = (f32x4){acc[4], acc[5], acc[6], acc[7]}; }
    else    { *LP(f32x4, cb) = (f32x4){acc[8], acc[9], acc[10], acc[11]}; *LP(f32x4, cb + 1024) = (f32x4){acc[12], acc[13], acc[14], acc[15]}; }
    BAR();
    const unsigned cr = CB + 2048 * (w ^ 4) + 16 * lane;
    f32x4 a = *LP(f32x4, cr), b = *LP(f32x4, cr + 1024);
    if (k2) { a += (f32x4){acc[8], acc[9], acc[10], acc[11]}; b += (f32x4){acc[12], acc[13], acc[14], acc[15]}; }
    else    { a += (f32x4){acc[0], acc[1], acc[2], acc[3]}; b += (f32x4){acc[4], acc[5], acc[6], acc[7]}; }
    auto s0 = __builtin_amdgcn_permlane32_swap(cvtpk(a[0], a[1]), cvtpk(b[0], b[1]), false, false);
    auto s1 = __builtin_amdgcn_permlane32_swap(cvtpk(a[2], a[3]), cvtpk(b[2], b[3]), false, false);
    *(u32x4*)(out + (row0 + 32 * rh + r32) * 1024 + 512 + 64 * hg + 32 * nh + 16 * k2 + 8 * h) = (u32x4){s0[0], s1[0], s0[1], s1[1]};
  }
  TS(18); TSR(19);
}

extern "C" void kernel_launch(void* const* d_in, const int* in_sizes, int n_in, void* d_out, int out_size, void* d_ws, size_t ws_size, hipStream_t stream) {
  (void)in_sizes; (void)n_in; (void)out_size; (void)ws_size;
  (void)hipFuncSetAttribute((const void*)fused_kernel, hipFuncAttributeMaxDynamicSharedMemorySize, LDS_BYTES);
  fused_kernel<<<256, 512, LDS_BYTES, stream>>>((const u16*)d_in[0], (const u16*)d_in[1], (const u16*)d_in[2], (const u16*)d_in[3], (const u16*)d_in[4],
      (const u16*)d_in[5], (const u16*)d_in[6], (const u16*)d_in[7], (const u16*)d_in[8], (const u16*)d_in[9], (const u16*)d_in[10], (const u16*)d_in[11],
      (const u16*)d_in[12], (const u16*)d_in[13], (u16*)d_out, (char*)d_ws);
}
```

```cpp
#include <hip/hip_runtime.h>
#include <hip/hip_bf16.h>
#pragma clang diagnostic ignored "-Wint-to-pointer-cast"

typedef unsigned short u16;
typedef unsigned long long u64;
typedef __attribute__((ext_vector_type(8))) short bf16x8;
typedef __attribute__((ext_vector_type(4))) short s16x4;
typedef __attribute__((ext_vector_type(16))) float f32x16;
typedef __attribute__((ext_vector_type(4))) float f32x4;
typedef __attribute__((ext_vector_type(2))) float f32x2;
typedef __attribute__((ext_vector_type(4))) unsigned u32x4;
typedef __attribute__((ext_vector_type(2))) unsigned u32x2;
typedef __attribute__((ext_vector_type(2))) __bf16 bf16x2;
#define LAS __attribute__((address_space(3)))
#define LP(T, off) ((LAS T*)(unsigned)(off))
#define SBAR() __builtin_amdgcn_sched_barrier(0)
#define PIN(x) asm volatile("" : "+v"(x))
#define MFMA32(a, b, c) __builtin_amdgcn_mfma_f32_32x32x16_bf16(a, b, c, 0, 0, 0)
#define MFMA16(a, b, c) __builtin_amdgcn_mfma_f32_16x16x32_bf16(a, b, c, 0, 0, 0)
#define AS8(x) __builtin_bit_cast(bf16x8, x)
#define EX2(v) __builtin_amdgcn_exp2f(v)
#define BAR() asm volatile("s_waitcnt lgkmcnt(0)\n\ts_barrier" ::: "memory")
#define WAITV(n) asm volatile("s_waitcnt vmcnt(" #n ")" ::: "memory")

#define STAMP 0
#define TS(i) do {} while (0)
#define TSR(i) do {} while (0)
#define TS_FLUSH() do {} while (0)

constexpr unsigned WS_EPOCH = 0, WS_FLAG = 1024, WS_KMAX = 4096, WS_K = 8192, WS_V = WS_K + 65536, WS_O = 1u << 18, WS_UT = WS_O + (2u << 20), WS_YT = WS_UT + (2u << 20), WS_DBG = 8u << 20;
constexpr unsigned R0 = 0, R1 = 65536, R2 = 131072, LDS_BYTES = 163840;
constexpr unsigned KA = R2, KA_STRIDE = 4608;
constexpr unsigned UC = R2 + 9216, UC_STRIDE = 4160;
constexpr unsigned ONES = R2 + 28672;
constexpr unsigned PART = 40960;
constexpr float QSCALE = 0.25f * 1.4426950408889634f;
constexpr float SAFE_BOUND = 40.f;
constexpr unsigned SENT = 0xffffffffu;
constexpr int CODE_KB = 15;

__device__ __forceinline__ unsigned cvtpk(float lo, float hi) { f32x2 v = {lo, hi}; return __builtin_bit_cast(unsigned, __builtin_convertvector(v, bf16x2)); }
__device__ __forceinline__ float bflo(unsigned x) { return __uint_as_float(x << 16); }
__device__ __forceinline__ float bfhi(unsigned x) { return __uint_as_float(x & 0xffff0000u); }
template <int C> __device__ __forceinline__ float dpp(float v) { return __int_as_float(__builtin_amdgcn_update_dpp(0, __float_as_int(v), C, 0xf, 0xf, true)); }
__device__ __forceinline__ float x16(float v, float& o) { auto r = __builtin_amdgcn_permlane16_swap(__float_as_uint(v), __float_as_uint(v), false, false); o = __uint_as_float(r[1]); return __uint_as_float(r[0]); }
__device__ __forceinline__ float x32(float v, float& o) { auto r = __builtin_amdgcn_permlane32_swap(__float_as_uint(v), __float_as_uint(v), false, false); o = __uint_as_float(r[1]); return __uint_as_float(r[0]); }
__device__ __forceinline__ float row_sum(float v) { v += dpp<0x128>(v); v += dpp<0x124>(v); v += dpp<0x122>(v); v += dpp<0x121>(v); return v; }
__device__ __forceinline__ float row_max(float v) { v = fmaxf(v, dpp<0x128>(v)); v = fmaxf(v, dpp<0x124>(v)); v = fmaxf(v, dpp<0x122>(v)); v = fmaxf(v, dpp<0x121>(v)); return v; }
__device__ __forceinline__ float col_sum(float v) { float o; v = x16(v, o); v += o; v = x32(v, o); return v + o; }
__device__ __forceinline__ float col_max(float v) { float o; v = x16(v, o); v = fmaxf(v, o); v = x32(v, o); return fmaxf(v, o); }

__device__ __forceinline__ void dma16(const void* base, unsigned voff, unsigned ldst) {
  asm volatile("s_mov_b32 m0, %2\n\ts_nop 0\n\tglobal_load_lds_dwordx4 %0, %1" :: "v"(voff), "s"(base), "s"(ldst) : "memory");
}
__device__ __forceinline__ void dma16x2(const void* base, unsigned voff, unsigned ldst) {
  asm volatile("s_mov_b32 m0, %2\n\ts_nop 0\n\tglobal_load_lds_dwordx4 %0, %1\n\tglobal_load_lds_dwordx4 %0, %1 offset:1024" :: "v"(voff), "s"(base), "s"(ldst) : "memory");
}
__device__ __forceinline__ void dma16x4v(const void* base, unsigned v0, unsigned v1, unsigned v2, unsigned v3, unsigned ldst) {
  asm volatile("s_mov_b32 m0, %5\n\ts_nop 0\n\tglobal_load_lds_dwordx4 %0, %4\n\tglobal_load_lds_dwordx4 %1, %4 offset:1024\n\t"
               "global_load_lds_dwordx4 %2, %4 offset:2048\n\tglobal_load_lds_dwordx4 %3, %4 offset:3072" :: "v"(v0), "v"(v1), "v"(v2), "v"(v3), "s"(base), "s"(ldst) : "memory");
}
__device__ __forceinline__ void dma16x4_sc1(const void* base, unsigned voff, unsigned ldst) {
  asm volatile("s_mov_b32 m0, %2\n\ts_nop 0\n\tglobal_load_lds_dwordx4 %0, %1 sc1\n\tglobal_load_lds_dwordx4 %0, %1 offset:1024 sc1\n\t"
               "global_load_lds_dwordx4 %0, %1 offset:2048 sc1\n\tglobal_load_lds_dwordx4 %0, %1 offset:3072 sc1" :: "v"(voff), "s"(base), "s"(ldst) : "memory");
}
template <int OFF> __device__ __forceinline__ void gld16(u32x4& d, const void* base, unsigned voff) { asm volatile("global_load_dwordx4 %0, %1, %2 offset:%3" : "=v"(d) : "v"(voff), "s"(base), "i"(OFF) : "memory"); }
template <int OFF> __device__ __forceinline__ void gld16_sc1(u32x4& d, const void* base, unsigned voff) { asm volatile("global_load_dwordx4 %0, %1, %2 offset:%3 sc1" : "=v"(d) : "v"(voff), "s"(base), "i"(OFF) : "memory"); }
template <int OFF> __device__ __forceinline__ void gld4_sc1(unsigned& d, const void* base, unsigned voff) { asm volatile("global_load_dword %0, %1, %2 offset:%3 sc1" : "=v"(d) : "v"(voff), "s"(base), "i"(OFF) : "memory"); }
__device__ __forceinline__ void gld2(unsigned& d, const void* base, unsigned voff) { asm volatile("global_load_ushort %0, %1, %2" : "=v"(d) : "v"(voff), "s"(base) : "memory"); }
__device__ __forceinline__ void gld8_sc1(u32x2& d, const void* base, unsigned voff) { asm volatile("global_load_dwordx2 %0, %1, %2 sc1" : "=v"(d) : "v"(voff), "s"(base) : "memory"); }
template <int OFF> __device__ __forceinline__ void gst16_sc1(void* base, unsigned voff, u32x4 d) { asm volatile("global_store_dwordx4 %0, %1, %2 offset:%3 sc1\n\ts_nop 1" :: "v"(voff), "v"(d), "s"(base), "i"(OFF) : "memory"); }
__device__ __forceinline__ void gst16(void* base, unsigned voff, u32x4 d) { asm volatile("global_store_dwordx4 %0, %1, %2\n\ts_nop 1" :: "v"(voff), "v"(d), "s"(base) : "memory"); }
__device__ __forceinline__ void gst8_sc1(void* base, unsigned voff, u32x2 d) { asm volatile("global_store_dwordx2 %0, %1, %2 sc1" :: "v"(voff), "v"(d), "s"(base) : "memory"); }
__device__ __forceinline__ void gst4_sc1(void* base, unsigned voff, unsigned d) { asm volatile("global_store_dword %0, %1, %2 sc1" :: "v"(voff), "v"(d), "s"(base) : "memory"); }
__device__ __forceinline__ s16x4 trd(unsigned a) { return __builtin_bit_cast(s16x4, __builtin_amdgcn_ds_read_tr16_b64_v4i16(LP(s16x4, a))); }
__device__ __forceinline__ bf16x8 trd2(unsigned a0, unsigned a1) { s16x4 x = trd(a0), y = trd(a1); return (bf16x8){x[0], x[1], x[2], x[3], y[0], y[1], y[2], y[3]}; }
__device__ __forceinline__ unsigned max4(u32x4 v) { return max(max(v[0], v[1]), max(v[2], v[3])); }

template <bool FR> __device__ __forceinline__ f32x2 zpow(f32x2 ab, float e) {
  f32x2 t = ab * e;
  float m = EX2(t.x);
  float a = FR ? __builtin_amdgcn_fractf(t.y) : t.y;
  f32x2 cs = {__builtin_amdgcn_cosf(a), __builtin_amdgcn_sinf(a)};
  return cs * m;
}

__global__ void __launch_bounds__(768, 3)
fused_kernel(const u16* __restrict__ u, const u16* __restrict__ Are, const u16* __restrict__ Aim, const u16* __restrict__ Bre, const u16* __restrict__ Bim,
             const u16* __restrict__ Cre, const u16* __restrict__ Cim, const u16* __restrict__ logdt, const u16* __restrict__ Dsk,
             const u16* __restrict__ x, const u16* __restrict__ wq, const u16* __restrict__ wk, const u16* __restrict__ wv, const u16* __restrict__ wo,
             u16* __restrict__ out, char* __restrict__ ws) {
  u64 pc0; asm volatile("s_getpc_b64 %0" : "=s"(pc0));
  const int tid = threadIdx.x, lane = tid & 63;
  const int w = __builtin_amdgcn_readfirstlane(tid >> 6);
  const int bid = blockIdx.x, xcd = bid & 7, loc = bid >> 3;
  const int rg = xcd * 4 + (loc >> 3), hg = loc & 7, row0 = rg * 64, ch0 = xcd * 64 + loc * 2;
  const int r16 = lane & 15, g = lane >> 4, r32 = lane & 31, h = lane >> 5;
  const unsigned l16 = 16 * lane;
  const unsigned wvo = (lane >> 3) * 1024 + 16 * ((lane & 7) ^ (((lane >> 4) & 1) << 2));
  TSR(0); TS(1);

  if (w >= 8) {
    const int a = w - 8;
    u64 tag = 0;
    if (a == 0) {
      u32x2 ep; gld8_sc1(ep, ws + WS_EPOCH, 0);
      const unsigned ust = R0 + 1024 * ((8 * hg + 56) & 63);
      const u16* ub = u + row0 * 512 + 64 * hg;
      const unsigned uvo = (lane >> 3) * 1024 + 16 * (lane & 7);
      dma16x4v(ub, uvo, uvo + 7168, uvo + 2 * 7168, uvo + 3 * 7168, ust); dma16x4v(ub + 16384, uvo, uvo + 7168, uvo + 2 * 7168, uvo + 3 * 7168, ust + 4096);
      const u32x4 sent = {SENT, SENT, SENT, SENT};
      char* os = ws + WS_O + (rg * 64 + 4 * hg) * 1024; char* ys = ws + WS_YT + ch0 * 4096;
      gst16_sc1<0>(os, l16, sent); gst16_sc1<1024>(os, l16, sent); gst16_sc1<2048>(os, l16, sent); gst16_sc1<3072>(os, l16, sent);
      gst16_sc1<0>(os + 32768, l16, sent); gst16_sc1<1024>(os + 32768, l16, sent); gst16_sc1<2048>(os + 32768, l16, sent); gst16_sc1<3072>(os + 32768, l16, sent);
      gst16_sc1<0>(ys, l16, sent); gst16_sc1<1024>(ys, l16, sent); gst16_sc1<2048>(ys, l16, sent); gst16_sc1<3072>(ys, l16, sent);
      gst16_sc1<0>(ys + 4096, l16, sent); gst16_sc1<1024>(ys + 4096, l16, sent); gst16_sc1<2048>(ys + 4096, l16, sent); gst16_sc1<3072>(ys + 4096, l16, sent);
      asm volatile("s_waitcnt vmcnt(16)" : "+v"(ep) :: "memory");
      tag = (((u64)(unsigned)__builtin_amdgcn_readfirstlane(ep[1]) << 32) | (unsigned)__builtin_amdgcn_readfirstlane(ep[0])) + 1;
      TS(2);
      unsigned ta = ust + 128 * (8 * g + (r16 >> 2)) + 8 * (lane & 3); PIN(ta);
      char* ut = ws + WS_UT + (64 * hg) * 4096 + row0 * 2;
      const unsigned so = r16 * 4096 + 16 * g;
#pragma unroll
      for (int j = 0; j < 8; ++j) {
        const bf16x8 v = trd2(ta + 4096 * (j >> 2) + 32 * (j & 3), ta + 4096 * (j >> 2) + 32 * (j & 3) + 512);
        if (j >> 2) gst16_sc1<64>(ut + 65536 * (j & 3), so, __builtin_bit_cast(u32x4, v)); else gst16_sc1<0>(ut + 65536 * (j & 3), so, __builtin_bit_cast(u32x4, v));
      }
    } else if (a == 3) {
      if (loc < CODE_KB) {
        unsigned dummy; asm volatile("global_load_dword %0, %1, %2\n\ts_waitcnt vmcnt(0)" : "=v"(dummy) : "v"(loc * 1024 + l16), "s"(pc0) : "memory");
      }
    }
    auto dma_x = [&](int i0, int i1) { _Pragma("unroll 1") for (int i = i0; i < i1; ++i) { const int r = (8 * hg + i) & 63; dma16(x + (row0 + r) * 512, 16 * (lane ^ (r & 15)), R0 + 1024 * r); } };
    auto dma_wq = [&](int i0, int i1) { _Pragma("unroll 1") for (int i = i0; i < i1; ++i) { const int p = (16 * (loc >> 3) + i) & 63; dma16(wq + p * 4096 + 64 * hg, wvo, R1 + 1024 * p); } };
    dma_x(0, a == 1 ? 8 : 0); dma_wq(0, a == 2 ? 16 : 0);
    BAR();
    TS(3);
    if (a == 0) {
      unsigned rb = R2 + l16; PIN(rb);
      f32x4 dk = *LP(f32x4, rb), dv = *LP(f32x4, rb + 1024);
#pragma unroll
      for (int s = 1; s < 8; ++s) { dk += *LP(f32x4, rb + 4096 * s); dv += *LP(f32x4, rb + 4096 * s + 1024); }
      const int kk0 = row0 + 8 * hg, t = kk0 >> 5, c = (kk0 >> 3) & 3;
      const u32x2 kp = {cvtpk(dk[0], dk[1]), cvtpk(dk[2], dk[3])}, vp = {cvtpk(dv[0], dv[1]), cvtpk(dv[2], dv[3])};
      if (r16 < 8) gst8_sc1(ws, WS_K + t * 1024 + ((g & 1) * 32 + 8 * c + r16) * 16 + 8 * (g >> 1), kp);
      if (g < 2) gst8_sc1(ws, WS_V + t * 1024 + 512 * (c >> 1) + 256 * g + 16 * r16 + 8 * (c & 1), vp);
      const float k2 = row_max(col_sum(dk[0] * dk[0] + dk[1] * dk[1] + dk[2] * dk[2] + dk[3] * dk[3]));
      if (lane == 0) gst4_sc1(ws, WS_KMAX + 4 * bid, __float_as_uint(k2));
      TS(4);
      WAITV(0);
      if (lane == 0) gst8_sc1(ws, WS_FLAG + 8 * bid, (u32x2){(unsigned)tag, (unsigned)(tag >> 32)});
    } else {
      dma_x(a == 1 ? 8 : 43, a == 1 ? 43 : a == 2 ? 0 : 64); dma_wq(a == 2 ? 16 : 43, a == 2 ? 43 : a == 1 ? 0 : 64);
      TS(4);
      WAITV(0);
    }
    TS(5);
    BAR();
    TS(6);
    const float k0 = *LP(float, R2 + 4096 * (2 * (a & 1)) + 3072) + *LP(float, R2 + 4096 * (2 * (a & 1) + 1) + 3072);
    if (a == 0) {
      _Pragma("unroll 1") for (unsigned spins = 0; spins < (1u << 20); ++spins) {
        u32x4 f0, f1; gld16_sc1<0>(f0, ws + WS_FLAG, 32 * lane); gld16_sc1<16>(f1, ws + WS_FLAG, 32 * lane);
        asm volatile("s_waitcnt vmcnt(0)" : "+v"(f0), "+v"(f1) :: "memory");
        const unsigned lo = (unsigned)tag, hi = (unsigned)(tag >> 32);
        const bool ok = f0[0] == lo && f0[1] == hi && f0[2] == lo && f0[3] == hi && f1[0] == lo && f1[1] == hi && f1[2] == lo && f1[3] == hi;
        if (__all(ok)) break;
        __builtin_amdgcn_s_sleep(1);
      }
      u32x4 km; gld16_sc1<0>(km, ws + WS_KMAX, l16);
      asm volatile("s_waitcnt vmcnt(0)" : "+v"(km) :: "memory");
      float m = fmaxf(fmaxf(__uint_as_float(km[0]), __uint_as_float(km[1])), fmaxf(__uint_as_float(km[2]), __uint_as_float(km[3])));
      m = __builtin_amdgcn_sqrtf(col_max(row_max(m))) * 1.01f;
      if (lane == 0) *LP(float, R2 + 1028) = m;
      if (bid == 0 && lane == 0) gst8_sc1(ws, WS_EPOCH, (u32x2){(unsigned)tag, (unsigned)(tag >> 32)});
      WAITV(0);
    }
    TS(7);
    BAR();
    TS(8);
    const unsigned kvo = (a & 1) * 65536 + (a >> 1) * 4096;
    dma16x4_sc1(ws + WS_K + kvo, l16, kvo);
    const int ch = a >> 1;
    u32x4 uA, uB; unsigned uAp, uBp;
    { const char* us = ws + WS_UT + (ch0 + ch) * 4096 + (a & 1) * 2048;
      gld16_sc1<0>(uA, us, l16); gld16_sc1<1024>(uB, us, l16); gld4_sc1<-4>(uAp, us, l16); gld4_sc1<1020>(uBp, us, l16); }
#pragma unroll
    for (int r = 1; r < 5; ++r) dma16x4_sc1(ws + WS_K + kvo + 8192 * r, l16, kvo + 8192 * r);
    const float qk_ = fmaxf(fmaxf(*LP(float, R2 + 1024), *LP(float, R2 + 4096 + 1024)), fmaxf(*LP(float, R2 + 8192 + 1024), *LP(float, R2 + 12288 + 1024))) * *LP(float, R2 + 1028);
    const bool unsafe = (unsigned)__builtin_amdgcn_readfirstlane(__float_as_int(qk_)) > __float_as_uint(SAFE_BOUND);
    __builtin_amdgcn_s_setprio(2);
    WAITV(20);
    BAR();
    TS(9);
    for (int task = a; task < 6; task += 4) {
      unsigned pb = PART + 1024 * task + l16; PIN(pb);
      f32x4 v = *LP(f32x4, pb);
#pragma unroll
      for (int s = 1; s < 4; ++s) v += *LP(f32x4, pb + 6144 * s);
      pb += R1; PIN(pb);
#pragma unroll
      for (int s = 0; s < 4; ++s) v += *LP(f32x4, pb + 6144 * s);
      const int c = task >= 3, tau = task - 3 * c;
      const unsigned kb = KA + KA_STRIDE * c + 2 * 2055;
      const unsigned c01 = cvtpk(v[0], v[1]), c23 = cvtpk(v[2], v[3]);
      const u16 b0 = (u16)c01, b1 = (u16)(c01 >> 16), b2 = (u16)c23, b3 = (u16)(c23 >> 16);
      if (tau == 0) {
        const unsigned p = kb - 2 * (4 * g + 4 + 16 * r16);
        *LP(u16, p + 6) = b0; *LP(u16, p + 4) = b1; *LP(u16, p + 2) = b2; *LP(u16, p) = b3;
        if (lane == 63) *LP(u16, p - 2) = b3;
      } else if (tau == 1) {
        const bool up = g >= 2;
        const unsigned a0 = kb - 2 * ((up ? 516 + 256 * (g - 2) : 258 + 8 * g) + 16 * r16) - 8, st = up ? 8 : 4;
        const unsigned a1 = a0 - st, a2 = a1 - st, a3 = a2 - st;
        *LP(u16, a0 + 8) = b0; *LP(u16, a2 + 8) = b2;
        *LP(u16, a1 + 6) = b1; *LP(u16, a1 + 8) = b1; *LP(u16, a1 + 10) = b1;
        *LP(u16, a3 + 6) = b3; *LP(u16, a3 + 8) = b3; *LP(u16, a3 + 10) = b3;
        if (up) {
          *LP(u16, a0 + 6) = b0; *LP(u16, a0 + 10) = b0; *LP(u16, a2 + 6) = b2; *LP(u16, a2 + 10) = b2;
          *LP(u16, a1 + 4) = b1; *LP(u16, a1 + 12) = b1; *LP(u16, a3 + 4) = b3; *LP(u16, a3 + 12) = b3;
        }
        if (lane == 31) *LP(u16, a3 + 4) = b3;
        if (lane == 63) { *LP(u16, a3 + 2) = b3; *LP(u16, a3) = b3; }
      } else if ((g >> 1) == c) {
        const unsigned p = kb - 2 * (1032 + 512 * (g & 1) + 16 * r16) - 536;
        const unsigned d0 = cvtpk(v[0], v[0]), d1 = cvtpk(v[1], v[1]), d2 = cvtpk(v[2], v[2]), d3 = cvtpk(v[3], v[3]);
        *LP(u32x2, p + 536 - 6) = (u32x2){d0, d0}; *LP(unsigned, p + 536 + 2) = d0; *LP(u16, p + 536 + 6) = b0;
        *LP(u32x2, p + 520 - 6) = (u32x2){d1, d1}; *LP(u32x2, p + 520 + 2) = (u32x2){d1, d1}; *LP(u16, p + 520 - 8) = b1;
        *LP(u32x2, p + 24 - 6) = (u32x2){d2, d2}; *LP(unsigned, p + 24 + 2) = d2; *LP(u16, p + 24 + 6) = b2;
        *LP(u32x2, p + 8 - 6) = (u32x2){d3, d3}; *LP(u32x2, p + 8 + 2) = (u32x2){d3, d3}; *LP(u16, p) = b3;
      }
    }
    if (a >= 2) {
      const unsigned kb = KA + KA_STRIDE * (a & 1) + 2 * 2055;
      if (lane < 60) *LP(u32x2, kb + 2 + 8 * lane) = (u32x2){0, 0};
      if (lane == 60) *LP(u16, kb) = (u16)cvtpk(k0, 0.f);
      if (lane < 4) *LP(u32x4, UC + UC_STRIDE * (2 * (a & 1) + (lane >> 1)) + 16 * (lane & 1)) = (u32x4){0, 0, 0, 0};
    }
    asm volatile("s_waitcnt vmcnt(16)" : "+v"(uA), "+v"(uB), "+v"(uAp), "+v"(uBp) :: "memory");
    {
      if (lane == 0 && (a & 1) == 0) uAp = 0;
      const unsigned ub = UC + UC_STRIDE * 2 * ch + 32 + (a & 1) * 2048 + l16;
      *LP(u32x4, ub) = uA; *LP(u32x4, ub + 1024) = uB;
      *LP(u32x4, ub + UC_STRIDE) = (u32x4){__builtin_amdgcn_alignbit(uA[0], uAp, 16), __builtin_amdgcn_alignbit(uA[1], uA[0], 16), __builtin_amdgcn_alignbit(uA[2], uA[1], 16), __builtin_amdgcn_alignbit(uA[3], uA[2], 16)};
      *LP(u32x4, ub + UC_STRIDE + 1024) = (u32x4){__builtin_amdgcn_alignbit(uB[0], uBp, 16), __builtin_amdgcn_alignbit(uB[1], uB[0], 16), __builtin_amdgcn_alignbit(uB[2], uB[1], 16), __builtin_amdgcn_alignbit(uB[3], uB[2], 16)};
    }
    WAITV(0);
    TS(10);
    BAR();
#pragma unroll
    for (int r = 5; r < 8; ++r) dma16x4_sc1(ws + WS_K + kvo + 8192 * r, l16, kvo + 8192 * r);
    if (unsafe) { WAITV(0); BAR(); }
    {
      const int cs = a & 1;
      unsigned ba = KA + KA_STRIDE * ch + 2 * (2040 - 16 * r16 + 8 * g) - 3584;
      unsigned aa = UC + UC_STRIDE * (2 * ch + 1 - (r16 & 1)) + 2 * (2 + (r16 & 14) + 8 * g);
      f32x4 y0 = {0, 0, 0, 0}, y1 = y0, y2 = y0, y3 = y0;
      for (int rr = 0; rr < 8; ++rr) {
        bf16x8 A[8], B[8];
#pragma unroll
        for (int i = 0; i < 8; ++i) {
          const LAS unsigned* p = LP(unsigned, aa + 1024 * (i >> 1)) + 128 * (i & 1);
          A[i] = AS8(((u32x4){p[0], p[1], p[2], p[3]}));
          B[i] = LP(bf16x8, ba)[32 * (7 - i)];
        }
        if (cs == 0) {
          y0 = MFMA16(A[0], B[0], y0);
#pragma unroll
          for (int t = 0; t <= 3; ++t) y1 = MFMA16(A[t], B[3 - t], y1);
#pragma unroll
          for (int t = 0; t <= 4; ++t) y2 = MFMA16(A[t], B[4 - t], y2);
#pragma unroll
          for (int t = 0; t <= 7; ++t) y3 = MFMA16(A[t], B[7 - t], y3);
        } else {
#pragma unroll
          for (int t = 0; t <= 1; ++t) y0 = MFMA16(A[t], B[1 - t], y0);
#pragma unroll
          for (int t = 0; t <= 2; ++t) y1 = MFMA16(A[t], B[2 - t], y1);
#pragma unroll
          for (int t = 0; t <= 5; ++t) y2 = MFMA16(A[t], B[5 - t], y2);
#pragma unroll
          for (int t = 0; t <= 6; ++t) y3 = MFMA16(A[t], B[6 - t], y3);
        }
        ba += 64; aa += 64;
      }
      char* yb = ws + WS_YT + (ch0 + ch) * 4096; const unsigned yo = 32 * r16 + 8 * g;
      gst8_sc1(yb, yo + 512 * (cs ? 1 : 0), (u32x2){min(cvtpk(y0[0], y0[1]), SENT - 1), min(cvtpk(y0[2], y0[3]), SENT - 1)});
      gst8_sc1(yb, yo + 512 * (cs ? 2 : 3), (u32x2){min(cvtpk(y1[0], y1[1]), SENT - 1), min(cvtpk(y1[2], y1[3]), SENT - 1)});
      gst8_sc1(yb, yo + 512 * (cs ? 5 : 4), (u32x2){min(cvtpk(y2[0], y2[1]), SENT - 1), min(cvtpk(y2[2], y2[3]), SENT - 1)});
      gst8_sc1(yb, yo + 512 * (cs ? 6 : 7), (u32x2){min(cvtpk(y3[0], y3[1]), SENT - 1), min(cvtpk(y3[2], y3[3]), SENT - 1)});
    }
    TS(11);
    WAITV(0);
    BAR();
    TS(12);
#pragma unroll
    for (int j = 0; j < 4; ++j)
      dma16x4v(wo + (128 * a + 32 * j) * 512 + 64 * hg, wvo, wvo + 7168, wvo + 2 * 7168, wvo + 3 * 7168, (a >> 1) * 65536 + (a & 1) * 16384 + 4096 * j);
    {
      u32x4 u0, u1, d0, d1, ya, yb;
      const unsigned uo = (row0 + 16 * a + r16) * 1024 + 128 * hg + 16 * g;
      gld16<0>(u0, u, uo); gld16<64>(u1, u, uo); gld16<0>(d0, Dsk, 128 * hg + 16 * g); gld16<64>(d1, Dsk, 128 * hg + 16 * g);
      const char* ys = ws + WS_YT + (64 * hg) * 4096 + (row0 + 16 * a) * 2; const unsigned yo = (lane >> 1) * 4096 + 16 * (lane & 1);
      _Pragma("unroll 1") for (unsigned spins = 0; spins < (1u << 20); ++spins) {
        gld16_sc1<0>(ya, ys, yo); gld16_sc1<0>(yb, ys + 32 * 4096, yo);
        asm volatile("s_waitcnt vmcnt(0)" : "+v"(ya), "+v"(yb) :: "memory");
        if (!__any(max(max4(ya), max4(yb)) == SENT)) break;
        __builtin_amdgcn_s_sleep(4);
      }
      asm volatile("" : "+v"(u0), "+v"(u1), "+v"(d0), "+v"(d1) :: "memory");
      const unsigned sc = R2 + 2048 * a;
      *LP(u32x4, sc + l16) = ya; *LP(u32x4, sc + 1024 + l16) = yb;
      const unsigned ta = sc + 256 * g + 8 * r16;
      const u32x4 t0 = __builtin_bit_cast(u32x4, trd2(ta, ta + 128)), t1 = __builtin_bit_cast(u32x4, trd2(ta + 1024, ta + 1152));
      u32x4 o0, o1;
#pragma unroll
      for (int i = 0; i < 4; ++i) {
        const unsigned s0 = cvtpk(bflo(u0[i]) * bflo(d0[i]), bfhi(u0[i]) * bfhi(d0[i])), s1 = cvtpk(bflo(u1[i]) * bflo(d1[i]), bfhi(u1[i]) * bfhi(d1[i]));
        o0[i] = cvtpk(bflo(t0[i]) + bflo(s0), bfhi(t0[i]) + bfhi(s0)); o1[i] = cvtpk(bflo(t1[i]) + bflo(s1), bfhi(t1[i]) + bfhi(s1));
      }
      const unsigned oo = (row0 + 16 * a + r16) * 2048 + 128 * hg + 16 * g;
      gst16(out, oo, o0); gst16(out, oo + 64, o1);
    }
    TS(13);
    WAITV(0);
    TS_FLUSH();
    return;
  }

  const unsigned slice = R2 + 4096 * w;
  u32x4 xk0, xk1;
  { const unsigned vo = (row0 + 8 * hg + (r16 & 7)) * 1024 + 128 * w + 16 * g;
    gld16<0>(xk0, x, vo); gld16<64>(xk1, x, vo); }
  dma16x2(wk + 1024 * w, l16, slice); dma16x2(wv + 1024 * w, l16, slice + 2048);
  unsigned p_ar, p_ai, p_br, p_bi, p_cr, p_ci, p_dt;
  { const unsigned t = tid & 255, d = ch0 + (t >> 7), vo = (d * 128 + (t & 127)) * 2;
    gld2(p_ar, Are, vo); gld2(p_ai, Aim, vo); gld2(p_br, Bre, vo); gld2(p_bi, Bim, vo); gld2(p_cr, Cre, vo); gld2(p_ci, Cim, vo); gld2(p_dt, logdt, d * 2); }
  asm volatile("s_waitcnt vmcnt(7)" : "+v"(xk0), "+v"(xk1) :: "memory");
  TS(2);
  {
    const unsigned ta = slice + 256 * g + 8 * r16;
    bf16x8 wk0 = trd2(ta, ta + 128), wk1 = trd2(ta + 1024, ta + 1152), wv0 = trd2(ta + 2048, ta + 2176), wv1 = trd2(ta + 3072, ta + 3200);
    f32x4 dk = {0, 0, 0, 0}, dv = {0, 0, 0, 0};
    dk = MFMA16(wk0, AS8(xk0), dk); dv = MFMA16(AS8(xk0), wv0, dv);
    dk = MFMA16(wk1, AS8(xk1), dk); dv = MFMA16(AS8(xk1), wv1, dv);
    PIN(dk); PIN(dv);
    *LP(f32x4, slice + l16) = dk; *LP(f32x4, slice + 1024 + l16) = dv;
  }
  asm volatile("s_waitcnt vmcnt(0)" : "+v"(p_ar), "+v"(p_ai), "+v"(p_br), "+v"(p_bi), "+v"(p_cr), "+v"(p_ci), "+v"(p_dt) :: "memory");
  if (w < 4) {
    const float dt = bflo(cvtpk(expf(bflo(p_dt)), 0.f));
    const float al = (dt * bflo(p_ar)) * 1.4426950408889634f;
    float be = (dt * bflo(p_ai)) * 0.15915494309189535f; be -= __builtin_rintf(be);
    const float br = dt * bflo(p_br), bi = dt * bflo(p_bi), cr = bflo(p_cr), ci = bflo(p_ci);
    const float xr = cr * br - ci * bi, xi = cr * bi + ci * br;
    *LP(f32x4, slice + 2048 + l16) = (f32x4){al, be, xr, xi};
    const float s = col_sum(row_sum(xr));
    if (lane == 0) *LP(float, slice + 3072) = s;
  }
  BAR();
  TS(3);
  f32x4 kacc[6];
  {
    const float eB = (float)(16 * r16), e1 = (float)(r16 + 1);
    const float e2 = (float)(r16 < 8 ? 258 + 2 * r16 : 516 + 256 * ((r16 - 8) >> 2) + 4 * (r16 & 3));
    const float e3 = (float)(1032 + 256 * ((r16 & 7) >> 1) + 8 * (r16 & 1));
    unsigned pa = R2 + 4096 * (w >> 2) + 2048 + 16 * ((16 * w + 4 * g) & 63); PIN(pa);
    u32x4 fb[2], f1[2], f2[2], f3;
#pragma unroll
    for (int c = 0; c < 2; ++c)
#pragma unroll
      for (int i = 0; i < 4; ++i) {
        const f32x4 P = *LP(f32x4, pa + 8192 * c + 16 * i);
        const f32x2 ab = {P[0], P[1]};
        const f32x2 wv_ = zpow<false>(ab, eB);
        fb[c][i] = cvtpk(P[2] * wv_.x - P[3] * wv_.y, P[2] * wv_.y + P[3] * wv_.x);
        const f32x2 a1 = zpow<false>(ab, e1); f1[c][i] = cvtpk(a1.x, -a1.y);
        const f32x2 a2 = zpow<true>(ab, e2); f2[c][i] = cvtpk(a2.x, -a2.y);
      }
    pa += 8192 * (r16 >> 3);
#pragma unroll
    for (int i = 0; i < 4; ++i) {
      const f32x2 ab = *LP(f32x2, pa + 16 * i);
      const f32x2 a3 = zpow<true>(ab, e3); f3[i] = cvtpk(a3.x, -a3.y);
    }
    const f32x4 z4 = {0, 0, 0, 0};
#pragma unroll
    for (int c = 0; c < 2; ++c) {
      kacc[3 * c + 0] = MFMA16(AS8(f1[c]), AS8(fb[c]), z4);
      kacc[3 * c + 1] = MFMA16(AS8(f2[c]), AS8(fb[c]), z4);
      kacc[3 * c + 2] = MFMA16(AS8(f3), AS8(fb[c]), z4);
    }
#pragma unroll
    for (int i = 0; i < 6; ++i) PIN(kacc[i]);
  }
  TS(5);
  BAR();
  TS(6);
  if (w < 4) {
    const int rh = w >> 1, hp = w & 1;
    const unsigned xa = R0 + 1024 * (32 * rh + r32) + 16 * (h ^ r16);
    const int q = r16 >> 2, p = lane & 3;
    unsigned wa = R1 + 128 * (8 * h + q) + 16 * ((4 * hp + 2 * (g & 1) + (p >> 1)) ^ ((q >> 1) << 2)) + 8 * (p & 1); PIN(wa);
    f32x16 acc = {0};
#pragma unroll
    for (int ks = 0; ks < 32; ++ks) {
      const bf16x8 b = *LP(bf16x8, (xa ^ ((ks & 7) << 5)) + 256 * (ks >> 3));
      const bf16x8 a = trd2(wa + 2048 * ks, wa + 2048 * ks + 512);
      acc = MFMA32(a, b, acc);
    }
    acc *= QSCALE;
    float n0 = 0, n1 = 0;
#pragma unroll
    for (int i = 0; i < 8; ++i) { n0 += acc[i] * acc[i]; n1 += acc[8 + i] * acc[8 + i]; }
    float o; n0 = x32(n0, o); n0 += o; n1 = x32(n1, o); n1 += o;
    n0 = __builtin_amdgcn_sqrtf(n0); n1 = __builtin_amdgcn_sqrtf(n1);
    const unsigned qs = R2 + 4096 * (4 + w);
    *LP(u32x4, qs + 2048 + l16) = (u32x4){cvtpk(acc[0], acc[1]), cvtpk(acc[2], acc[3]), cvtpk(acc[4], acc[5]), cvtpk(acc[6], acc[7])};
    *LP(u32x4, qs + 3072 + l16) = (u32x4){cvtpk(acc[8], acc[9]), cvtpk(acc[10], acc[11]), cvtpk(acc[12], acc[13]), cvtpk(acc[14], acc[15])};
    *LP(float, slice + 4 * lane) = n0; *LP(float, slice + 256 + 4 * lane) = n1;
    const float nm = col_max(row_max(fmaxf(n0, n1)));
    if (lane == 0) *LP(float, slice + 1024) = nm;
  } else if (w == 4 && lane < 12) {
    const unsigned j = lane >> 2, o1 = j ? 0u : 0x3f803f80u;
    *LP(u32x4, ONES + 512 * (lane & 3) + (j == 2 ? 64 : 16 * j)) = (u32x4){o1, o1, o1, o1};
  }
  TS(7);
  BAR();
  TS(8);
  const int qt = w & 3, hs = w >> 2;
  const bf16x8 qf = *LP(bf16x8, R2 + 4096 * (4 + qt) + 2048 + 1024 * hs + l16);
  const float kmax = *LP(float, R2 + 1028);
  float sh = *LP(float, R2 + 4096 * qt + 256 * hs + 4 * lane) * kmax;
  const float qmax = fmaxf(fmaxf(*LP(float, R2 + 1024), *LP(float, R2 + 4096 + 1024)), fmaxf(*LP(float, R2 + 8192 + 1024), *LP(float, R2 + 12288 + 1024)));
  const bool unsafe = (unsigned)__builtin_amdgcn_readfirstlane(__float_as_int(qmax * kmax)) > __float_as_uint(SAFE_BOUND);
  {
    const unsigned pb = (w >> 2) * R1 + PART + (w & 3) * 6144 + l16;
#pragma unroll
    for (int i = 0; i < 6; ++i) *LP(f32x4, pb + 1024 * i) = kacc[i];
  }
  BAR();
  TS(9);
  if (unsafe) {
    BAR(); BAR();
    const f32x16 z = {0};
    float m = -INFINITY;
    for (int t = 0; t < 64; ++t) {
      const f32x16 s = MFMA32(*LP(bf16x8, R0 + 1024 * t + l16), qf, z);
#pragma unroll
      for (int i = 0; i < 16; ++i) m = fmaxf(m, s[i]);
    }
    float o; m = x32(m, o); sh = fmaxf(m, o);
  }
  f32x16 oacc = {0};
  {
    f32x16 mneg;
#pragma unroll
    for (int i = 0; i < 16; ++i) mneg[i] = -sh;
    PIN(mneg);
    const bool lo = (lane & 16) == 0;
    unsigned kp = R0 + l16;
    unsigned vp = lo ? R1 + 256 * h + 16 * r16 : ONES + (r32 == 16 ? 0 : (r32 >= 20 && r32 < 28) ? 64 : 16);
    const unsigned vinc = lo ? 2048 : 0;
    const int tb = unsafe ? -1 : 4;
    f32x16 sA, sB; u32x4 pA0, pA1, pB0 = {0, 0, 0, 0}, pB1;
    bf16x8 kf = *LP(bf16x8, kp), v0, v1;
    sA = MFMA32(kf, qf, mneg);
    kf = *LP(bf16x8, kp + 1024);
    v0 = *LP(bf16x8, vp); v1 = v0;
#pragma unroll
    for (int i = 0; i < 16; ++i) sB[i] = 0.f;
#define CV(P, j, S, i) P[j] = cvtpk(S[i], S[i + 1])
#define E4(S, i) do { S[i] = EX2(S[i]); S[i + 1] = EX2(S[i + 1]); S[i + 2] = EX2(S[i + 2]); S[i + 3] = EX2(S[i + 3]); \
                      asm volatile("" : "+v"(S[i]), "+v"(S[i + 1]), "+v"(S[i + 2]), "+v"(S[i + 3])); SBAR(); } while (0)
#define UNIT(S, P0, T, Q0, Q1, KOFF, VOFF) do { \
      E4(S, 0); CV(Q1, 0, T, 8); CV(Q1, 1, T, 10); CV(Q1, 2, T, 12); CV(Q1, 3, T, 14); PIN(Q1); SBAR(); \
      T = MFMA32(kf, qf, mneg); kf = *LP(bf16x8, kp + KOFF); SBAR(); E4(S, 4); \
      oacc = MFMA32(v0, AS8(Q0), oacc); v0 = *LP(bf16x8, vp + VOFF); SBAR(); E4(S, 8); \
      oacc = MFMA32(v1, AS8(Q1), oacc); v1 = *LP(bf16x8, vp + VOFF + 512); SBAR(); \
      CV(P0, 0, S, 0); CV(P0, 1, S, 2); CV(P0, 2, S, 4); CV(P0, 3, S, 6); PIN(P0); SBAR(); E4(S, 12); } while (0)
    for (int t = 0; t < 64; t += 4) {
      if (t == tb || t == 32) BAR();
      UNIT(sA, pA0, sB, pB0, pB1, 2048, 0);
      UNIT(sB, pB0, sA, pA0, pA1, 3072, 1024);
      vp += vinc;
      UNIT(sA, pA0, sB, pB0, pB1, 4096, 0);
      UNIT(sB, pB0, sA, pA0, pA1, 5120, 1024);
      kp += 4096; vp += vinc;
    }
    CV(pB1, 0, sB, 8); CV(pB1, 1, sB, 10); CV(pB1, 2, sB, 12); CV(pB1, 3, sB, 14);
    oacc = MFMA32(v0, AS8(pB0), oacc); oacc = MFMA32(v1, AS8(pB1), oacc);
  }
  TS(10);
  {
    const float rl = __builtin_amdgcn_rcpf(oacc[8]);
    float o; const float r0 = x32(rl, o);
    u32x4 of_ = {cvtpk(oacc[0] * r0, oacc[1] * r0), cvtpk(oacc[2] * r0, oacc[3] * r0), cvtpk(oacc[4] * r0, oacc[5] * r0), cvtpk(oacc[6] * r0, oacc[7] * r0)};
#pragma unroll
    for (int i = 0; i < 4; ++i) of_[i] = min(of_[i], SENT - 1);
    gst16_sc1<0>(ws + WS_O + ((rg * 2 + (qt >> 1)) * 32 + 4 * hg + 2 * (qt & 1) + hs) * 1024, l16, of_);
  }
  BAR();
  TS(11);
  {
    const int rh = w >> 2, kq = w & 3;
    const char* osrc = ws + WS_O + ((rg * 2 + rh) * 32 + 8 * kq) * 1024;
    _Pragma("unroll 1") for (unsigned spins = 0; spins < (1u << 20); ++spins) {
      unsigned t; gld4_sc1<1020>(t, osrc, (lane & 7) * 1024);
      asm volatile("s_waitcnt vmcnt(0)" : "+v"(t) :: "memory");
      if (!__any(t == SENT)) break;
    }
    TS(12);
    u32x4 of[8];
    _Pragma("unroll 1") for (unsigned spins = 0; spins < (1u << 20); ++spins) {
      gld16_sc1<0>(of[0], osrc, l16); gld16_sc1<1024>(of[1], osrc, l16); gld16_sc1<2048>(of[2], osrc, l16); gld16_sc1<3072>(of[3], osrc, l16);
      gld16_sc1<0>(of[4], osrc + 4096, l16); gld16_sc1<1024>(of[5], osrc + 4096, l16); gld16_sc1<2048>(of[6], osrc + 4096, l16); gld16_sc1<3072>(of[7], osrc + 4096, l16);
      asm volatile("s_waitcnt vmcnt(0)" : "+v"(of[0]), "+v"(of[1]), "+v"(of[2]), "+v"(of[3]), "+v"(of[4]), "+v"(of[5]), "+v"(of[6]), "+v"(of[7]) :: "memory");
      unsigned mx = 0;
#pragma unroll
      for (int i = 0; i < 8; ++i) mx = max(mx, max4(of[i]));
      if (!__any(mx == SENT)) break;
    }
    TS(13);
    const int q = r16 >> 2, p = lane & 3;
    unsigned wa = (kq >> 1) * R1 + (kq & 1) * 16384 + 128 * (4 * h + q) + 16 * ((2 * (g & 1) + (p >> 1)) ^ ((q >> 1) << 2)) + 8 * (p & 1); PIN(wa);
    const unsigned wb = wa ^ 64;
    f32x16 c0 = {0}, c1 = {0};
#pragma unroll
    for (int ks = 0; ks < 8; ++ks) {
      c0 = MFMA32(trd2(wa + 2048 * ks, wa + 2048 * ks + 1024), AS8(of[ks]), c0);
      c1 = MFMA32(trd2(wb + 2048 * ks, wb + 2048 * ks + 1024), AS8(of[ks]), c1);
    }
    const unsigned rbase = 32768 + rh * R1;
    const f32x4 G[8] = {{c0[0], c0[1], c0[2], c0[3]}, {c0[4], c0[5], c0[6], c0[7]}, {c0[8], c0[9], c0[10], c0[11]}, {c0[12], c0[13], c0[14], c0[15]},
                        {c1[0], c1[1], c1[2], c1[3]}, {c1[4], c1[5], c1[6], c1[7]}, {c1[8], c1[9], c1[10], c1[11]}, {c1[12], c1[13], c1[14], c1[15]}};
    unsigned wr = rbase + 2048 * kq + l16; PIN(wr);
#pragma unroll
    for (int j = 0; j < 4; ++j) { *LP(f32x4, wr + 8192 * j) = G[2 * j]; *LP(f32x4, wr + 8192 * j + 1024) = G[2 * j + 1]; }
    BAR();
    unsigned rd = rbase + 8192 * kq + l16; PIN(rd);
    f32x4 a = *LP(f32x4, rd), b = *LP(f32x4, rd + 1024);
#pragma unroll
    for (int s = 1; s < 4; ++s) { a += *LP(f32x4, rd + 2048 * s); b += *LP(f32x4, rd + 2048 * s + 1024); }
    auto s0 = __builtin_amdgcn_permlane32_swap(cvtpk(a[0], a[1]), cvtpk(b[0], b[1]), false, false);
    auto s1 = __builtin_amdgcn_permlane32_swap(cvtpk(a[2], a[3]), cvtpk(b[2], b[3]), false, false);
    gst16(out, (row0 + 32 * rh + r32) * 2048 + 1024 + 128 * hg + 32 * kq + 16 * h, (u32x4){s0[0], s1[0], s0[1], s1[1]});
  }
  TS_FLUSH();
}

extern "C" void kernel_launch(void* const* d_in, const int* in_sizes, int n_in, void* d_out, int out_size, void* d_ws, size_t ws_size, hipStream_t stream) {
  (void)in_sizes; (void)n_in; (void)out_size; (void)ws_size;
  (void)hipFuncSetAttribute((const void*)fused_kernel, hipFuncAttributeMaxDynamicSharedMemorySize, LDS_BYTES);
  fused_kernel<<<256, 768, LDS_BYTES, stream>>>((const u16*)d_in[0], (const u16*)d_in[1], (const u16*)d_in[2], (const u16*)d_in[3], (const u16*)d_in[4],
      (const u16*)d_in[5], (const u16*)d_in[6], (const u16*)d_in[7], (const u16*)d_in[8], (const u16*)d_in[9], (const u16*)d_in[10], (const u16*)d_in[11],
      (const u16*)d_in[12], (const u16*)d_in[13], (u16*)d_out, (char*)d_ws);
}
```

```cpp
#define DIAG 1
#include <hip/hip_runtime.h>
#include <hip/hip_bf16.h>
#pragma clang diagnostic ignored "-Wint-to-pointer-cast"

typedef unsigned short u16;
typedef unsigned long long u64;
typedef __attribute__((ext_vector_type(8))) short bf16x8;
typedef __attribute__((ext_vector_type(4))) short s16x4;
typedef __attribute__((ext_vector_type(16))) float f32x16;
typedef __attribute__((ext_vector_type(4))) float f32x4;
typedef __attribute__((ext_vector_type(2))) float f32x2;
typedef __attribute__((ext_vector_type(4))) unsigned u32x4;
typedef __attribute__((ext_vector_type(2))) unsigned u32x2;
typedef __attribute__((ext_vector_type(2))) __bf16 bf16x2;
#define LAS __attribute__((address_space(3)))
#define LP(T, off) ((LAS T*)(unsigned)(off))
#define SBAR() __builtin_amdgcn_sched_barrier(0)
#define PIN(x) asm volatile("" : "+v"(x))
#define MFMA32(a, b, c) __builtin_amdgcn_mfma_f32_32x32x16_bf16(a, b, c, 0, 0, 0)
#define MFMA16(a, b, c) __builtin_amdgcn_mfma_f32_16x16x32_bf16(a, b, c, 0, 0, 0)
#define AS8(x) __builtin_bit_cast(bf16x8, x)
#define EX2(v) __builtin_amdgcn_exp2f(v)
#define BAR() asm volatile("s_waitcnt lgkmcnt(0)\n\ts_barrier" ::: "memory")
#define WAITV(n) asm volatile("s_waitcnt vmcnt(" #n ")" ::: "memory")

#define STAMP 0
#define TS(i) do { if ((i) == DIAG) asm volatile("s_memrealtime %0\n\ts_waitcnt lgkmcnt(0)" : "=s"(dg1_) :: "memory"); } while (0)
#define TSR(i) do { asm volatile("s_memrealtime %0\n\ts_waitcnt lgkmcnt(0)" : "=s"(dg0_) :: "memory"); dg1_ = dg0_; } while (0)
#define TS_FLUSH() do { TS(14); if (w == 0) { const u64 tgt_ = dg0_ + 6000 + 4 * (dg1_ - dg0_); u64 n_; do { __builtin_amdgcn_s_sleep(8); asm volatile("s_memrealtime %0\n\ts_waitcnt lgkmcnt(0)" : "=s"(n_) :: "memory"); } while (n_ < tgt_); } } while (0)

constexpr unsigned WS_EPOCH = 0, WS_FLAG = 1024, WS_KMAX = 4096, WS_K = 8192, WS_V = WS_K + 65536, WS_O = 1u << 18, WS_UT = WS_O + (2u << 20), WS_YT = WS_UT + (2u << 20), WS_DBG = 8u << 20;
constexpr unsigned R0 = 0, R1 = 65536, R2 = 131072, LDS_BYTES = 163840;
constexpr unsigned KA = R2, KA_STRIDE = 4608;
constexpr unsigned UC = R2 + 9216, UC_STRIDE = 4160;
constexpr unsigned ONES = R2 + 28672;
constexpr unsigned PART = 40960;
constexpr float QSCALE = 0.25f * 1.4426950408889634f;
constexpr float SAFE_BOUND = 40.f;
constexpr unsigned SENT = 0xffffffffu;
constexpr int CODE_KB = 15;

__device__ __forceinline__ unsigned cvtpk(float lo, float hi) { f32x2 v = {lo, hi}; return __builtin_bit_cast(unsigned, __builtin_convertvector(v, bf16x2)); }
__device__ __forceinline__ float bflo(unsigned x) { return __uint_as_float(x << 16); }
__device__ __forceinline__ float bfhi(unsigned x) { return __uint_as_float(x & 0xffff0000u); }
template <int C> __device__ __forceinline__ float dpp(float v) { return __int_as_float(__builtin_amdgcn_update_dpp(0, __float_as_int(v), C, 0xf, 0xf, true)); }
__device__ __forceinline__ float x16(float v, float& o) { auto r = __builtin_amdgcn_permlane16_swap(__float_as_uint(v), __float_as_uint(v), false, false); o = __uint_as_float(r[1]); return __uint_as_float(r[0]); }
__device__ __forceinline__ float x32(float v, float& o) { auto r = __builtin_amdgcn_permlane32_swap(__float_as_uint(v), __float_as_uint(v), false, false); o = __uint_as_float(r[1]); return __uint_as_float(r[0]); }
__device__ __forceinline__ float row_sum(float v) { v += dpp<0x128>(v); v += dpp<0x124>(v); v += dpp<0x122>(v); v += dpp<0x121>(v); return v; }
__device__ __forceinline__ float row_max(float v) { v = fmaxf(v, dpp<0x128>(v)); v = fmaxf(v, dpp<0x124>(v)); v = fmaxf(v, dpp<0x122>(v)); v = fmaxf(v, dpp<0x121>(v)); return v; }
__device__ __forceinline__ float col_sum(float v) { float o; v = x16(v, o); v += o; v = x32(v, o); return v + o; }
__device__ __forceinline__ float col_max(float v) { float o; v = x16(v, o); v = fmaxf(v, o); v = x32(v, o); return fmaxf(v, o); }

__device__ __forceinline__ void dma16(const void* base, unsigned voff, unsigned ldst) {
  asm volatile("s_mov_b32 m0, %2\n\ts_nop 0\n\tglobal_load_lds_dwordx4 %0, %1" :: "v"(voff), "s"(base), "s"(ldst) : "memory");
}
__device__ __forceinline__ void dma16x2(const void* base, unsigned voff, unsigned ldst) {
  asm volatile("s_mov_b32 m0, %2\n\ts_nop 0\n\tglobal_load_lds_dwordx4 %0, %1\n\tglobal_load_lds_dwordx4 %0, %1 offset:1024" :: "v"(voff), "s"(base), "s"(ldst) : "memory");
}
__device__ __forceinline__ void dma16x4v(const void* base, unsigned v0, unsigned v1, unsigned v2, unsigned v3, unsigned ldst) {
  asm volatile("s_mov_b32 m0, %5\n\ts_nop 0\n\tglobal_load_lds_dwordx4 %0, %4\n\tglobal_load_lds_dwordx4 %1, %4 offset:1024\n\t"
               "global_load_lds_dwordx4 %2, %4 offset:2048\n\tglobal_load_lds_dwordx4 %3, %4 offset:3072" :: "v"(v0), "v"(v1), "v"(v2), "v"(v3), "s"(base), "s"(ldst) : "memory");
}
__device__ __forceinline__ void dma16x4_sc1(const void* base, unsigned voff, unsigned ldst) {
  asm volatile("s_mov_b32 m0, %2\n\ts_nop 0\n\tglobal_load_lds_dwordx4 %0, %1 sc1\n\tglobal_load_lds_dwordx4 %0, %1 offset:1024 sc1\n\t"
               "global_load_lds_dwordx4 %0, %1 offset:2048 sc1\n\tglobal_load_lds_dwordx4 %0, %1 offset:3072 sc1" :: "v"(voff), "s"(base), "s"(ldst) : "memory");
}
template <int OFF> __device__ __forceinline__ void gld16(u32x4& d, const void* base, unsigned voff) { asm volatile("global_load_dwordx4 %0, %1, %2 offset:%3" : "=v"(d) : "v"(voff), "s"(base), "i"(OFF) : "memory"); }
template <int OFF> __device__ __forceinline__ void gld16_sc1(u32x4& d, const void* base, unsigned voff) { asm volatile("global_load_dwordx4 %0, %1, %2 offset:%3 sc1" : "=v"(d) : "v"(voff), "s"(base), "i"(OFF) : "memory"); }
template <int OFF> __device__ __forceinline__ void gld4_sc1(unsigned& d, const void* base, unsigned voff) { asm volatile("global_load_dword %0, %1, %2 offset:%3 sc1" : "=v"(d) : "v"(voff), "s"(base), "i"(OFF) : "memory"); }
__device__ __forceinline__ void gld2(unsigned& d, const void* base, unsigned voff) { asm volatile("global_load_ushort %0, %1, %2" : "=v"(d) : "v"(voff), "s"(base) : "memory"); }
__device__ __forceinline__ void gld8_sc1(u32x2& d, const void* base, unsigned voff) { asm volatile("global_load_dwordx2 %0, %1, %2 sc1" : "=v"(d) : "v"(voff), "s"(base) : "memory"); }
template <int OFF> __device__ __forceinline__ void gst16_sc1(void* base, unsigned voff, u32x4 d) { asm volatile("global_store_dwordx4 %0, %1, %2 offset:%3 sc1\n\ts_nop 1" :: "v"(voff), "v"(d), "s"(base), "i"(OFF) : "memory"); }
__device__ __forceinline__ void gst16(void* base, unsigned voff, u32x4 d) { asm volatile("global_store_dwordx4 %0, %1, %2\n\ts_nop 1" :: "v"(voff), "v"(d), "s"(base) : "memory"); }
__device__ __forceinline__ void gst8_sc1(void* base, unsigned voff, u32x2 d) { asm volatile("global_store_dwordx2 %0, %1, %2 sc1" :: "v"(voff), "v"(d), "s"(base) : "memory"); }
__device__ __forceinline__ void gst4_sc1(void* base, unsigned voff, unsigned d) { asm volatile("global_store_dword %0, %1, %2 sc1" :: "v"(voff), "v"(d), "s"(base) : "memory"); }
__device__ __forceinline__ s16x4 trd(unsigned a) { return __builtin_bit_cast(s16x4, __builtin_amdgcn_ds_read_tr16_b64_v4i16(LP(s16x4, a))); }
__device__ __forceinline__ bf16x8 trd2(unsigned a0, unsigned a1) { s16x4 x = trd(a0), y = trd(a1); return (bf16x8){x[0], x[1], x[2], x[3], y[0], y[1], y[2], y[3]}; }
__device__ __forceinline__ unsigned max4(u32x4 v) { return max(max(v[0], v[1]), max(v[2], v[3])); }

template <bool FR> __device__ __forceinline__ f32x2 zpow(f32x2 ab, float e) {
  f32x2 t = ab * e;
  float m = EX2(t.x);
  float a = FR ? __builtin_amdgcn_fractf(t.y) : t.y;
  f32x2 cs = {__builtin_amdgcn_cosf(a), __builtin_amdgcn_sinf(a)};
  return cs * m;
}

__global__ void __launch_bounds__(768, 3)
fused_kernel(const u16* __restrict__ u, const u16* __restrict__ Are, const u16* __restrict__ Aim, const u16* __restrict__ Bre, const u16* __restrict__ Bim,
             const u16* __restrict__ Cre, const u16* __restrict__ Cim, const u16* __restrict__ logdt, const u16* __restrict__ Dsk,
             const u16* __restrict__ x, const u16* __restrict__ wq, const u16* __restrict__ wk, const u16* __restrict__ wv, const u16* __restrict__ wo,
             u16* __restrict__ out, char* __restrict__ ws) {
  u64 pc0; asm volatile("s_getpc_b64 %0" : "=s"(pc0));
  const int tid = threadIdx.x, lane = tid & 63;
  const int w = __builtin_amdgcn_readfirstlane(tid >> 6);
  const int bid = blockIdx.x, xcd = bid & 7, loc = bid >> 3;
  const int rg = xcd * 4 + (loc >> 3), hg = loc & 7, row0 = rg * 64, ch0 = xcd * 64 + loc * 2;
  const int r16 = lane & 15, g = lane >> 4, r32 = lane & 31, h = lane >> 5;
  const unsigned l16 = 16 * lane;
  const unsigned wvo = (lane >> 3) * 1024 + 16 * ((lane & 7) ^ (((lane >> 4) & 1) << 2));
  u64 dg0_, dg1_;
  TSR(0); TS(1);

  if (w >= 8) {
    const int a = w - 8;
    u64 tag = 0;
    if (a == 0) {
      u32x2 ep; gld8_sc1(ep, ws + WS_EPOCH, 0);
      const unsigned ust = R0 + 1024 * ((8 * hg + 56) & 63);
      const u16* ub = u + row0 * 512 + 64 * hg;
      const unsigned uvo = (lane >> 3) * 1024 + 16 * (lane & 7);
      dma16x4v(ub, uvo, uvo + 7168, uvo + 2 * 7168, uvo + 3 * 7168, ust); dma16x4v(ub + 16384, uvo, uvo + 7168, uvo + 2 * 7168, uvo + 3 * 7168, ust + 4096);
      const u32x4 sent = {SENT, SENT, SENT, SENT};
      char* os = ws + WS_O + (rg * 64 + 4 * hg) * 1024; char* ys = ws + WS_YT + ch0 * 4096;
      gst16_sc1<0>(os, l16, sent); gst16_sc1<1024>(os, l16, sent); gst16_sc1<2048>(os, l16, sent); gst16_sc1<3072>(os, l16, sent);
      gst16_sc1<0>(os + 32768, l16, sent); gst16_sc1<1024>(os + 32768, l16, sent); gst16_sc1<2048>(os + 32768, l16, sent); gst16_sc1<3072>(os + 32768, l16, sent);
      gst16_sc1<0>(ys, l16, sent); gst16_sc1<1024>(ys, l16, sent); gst16_sc1<2048>(ys, l16, sent); gst16_sc1<3072>(ys, l16, sent);
      gst16_sc1<0>(ys + 4096, l16, sent); gst16_sc1<1024>(ys + 4096, l16, sent); gst16_sc1<2048>(ys + 4096, l16, sent); gst16_sc1<3072>(ys + 4096, l16, sent);
      asm volatile("s_waitcnt vmcnt(16)" : "+v"(ep) :: "memory");
      tag = (((u64)(unsigned)__builtin_amdgcn_readfirstlane(ep[1]) << 32) | (unsigned)__builtin_amdgcn_readfirstlane(ep[0])) + 1;
      TS(2);
      unsigned ta = ust + 128 * (8 * g + (r16 >> 2)) + 8 * (lane & 3); PIN(ta);
      char* ut = ws + WS_UT + (64 * hg) * 4096 + row0 * 2;
      const unsigned so = r16 * 4096 + 16 * g;
#pragma unroll
      for (int j = 0; j < 8; ++j) {
        const bf16x8 v = trd2(ta + 4096 * (j >> 2) + 32 * (j & 3), ta + 4096 * (j >> 2) + 32 * (j & 3) + 512);
        if (j >> 2) gst16_sc1<64>(ut + 65536 * (j & 3), so, __builtin_bit_cast(u32x4, v)); else gst16_sc1<0>(ut + 65536 * (j & 3), so, __builtin_bit_cast(u32x4, v));
      }
    } else if (a == 3) {
      if (loc < CODE_KB) {
        unsigned dummy; asm volatile("global_load_dword %0, %1, %2\n\ts_waitcnt vmcnt(0)" : "=v"(dummy) : "v"(loc * 1024 + l16), "s"(pc0) : "memory");
      }
    }
    auto dma_x = [&](int i0, int i1) { _Pragma("unroll 1") for (int i = i0; i < i1; ++i) { const int r = (8 * hg + i) & 63; dma16(x + (row0 + r) * 512, 16 * (lane ^ (r & 15)), R0 + 1024 * r); } };
    auto dma_wq = [&](int i0, int i1) { _Pragma("unroll 1") for (int i = i0; i < i1; ++i) { const int p = (16 * (loc >> 3) + i) & 63; dma16(wq + p * 4096 + 64 * hg, wvo, R1 + 1024 * p); } };
    dma_x(0, a == 1 ? 8 : 0); dma_wq(0, a == 2 ? 16 : 0);
    BAR();
    TS(3);
    if (a == 0) {
      unsigned rb = R2 + l16; PIN(rb);
      f32x4 dk = *LP(f32x4, rb), dv = *LP(f32x4, rb + 1024);
#pragma unroll
      for (int s = 1; s < 8; ++s) { dk += *LP(f32x4, rb + 4096 * s); dv += *LP(f32x4, rb + 4096 * s + 1024); }
      const int kk0 = row0 + 8 * hg, t = kk0 >> 5, c = (kk0 >> 3) & 3;
      const u32x2 kp = {cvtpk(dk[0], dk[1]), cvtpk(dk[2], dk[3])}, vp = {cvtpk(dv[0], dv[1]), cvtpk(dv[2], dv[3])};
      if (r16 < 8) gst8_sc1(ws, WS_K + t * 1024 + ((g & 1) * 32 + 8 * c + r16) * 16 + 8 * (g >> 1), kp);
      if (g < 2) gst8_sc1(ws, WS_V + t * 1024 + 512 * (c >> 1) + 256 * g + 16 * r16 + 8 * (c & 1), vp);
      const float k2 = row_max(col_sum(dk[0] * dk[0] + dk[1] * dk[1] + dk[2] * dk[2] + dk[3] * dk[3]));
      if (lane == 0) gst4_sc1(ws, WS_KMAX + 4 * bid, __float_as_uint(k2));
      TS(4);
      WAITV(0);
      if (lane == 0) gst8_sc1(ws, WS_FLAG + 8 * bid, (u32x2){(unsigned)tag, (unsigned)(tag >> 32)});
    } else {
      dma_x(a == 1 ? 8 : 43, a == 1 ? 43 : a == 2 ? 0 : 64); dma_wq(a == 2 ? 16 : 43, a == 2 ? 43 : a == 1 ? 0 : 64);
      TS(4);
      WAITV(0);
    }
    TS(5);
    BAR();
    TS(6);
    const float k0 = *LP(float, R2 + 4096 * (2 * (a & 1)) + 3072) + *LP(float, R2 + 4096 * (2 * (a & 1) + 1) + 3072);
    if (a == 0) {
      _Pragma("unroll 1") for (unsigned spins = 0; spins < (1u << 20); ++spins) {
        u32x4 f0, f1; gld16_sc1<0>(f0, ws + WS_FLAG, 32 * lane); gld16_sc1<16>(f1, ws + WS_FLAG, 32 * lane);
        asm volatile("s_waitcnt vmcnt(0)" : "+v"(f0), "+v"(f1) :: "memory");
        const unsigned lo = (unsigned)tag, hi = (unsigned)(tag >> 32);
        const bool ok = f0[0] == lo && f0[1] == hi && f0[2] == lo && f0[3] == hi && f1[0] == lo && f1[1] == hi && f1[2] == lo && f1[3] == hi;
        if (__all(ok)) break;
        __builtin_amdgcn_s_sleep(1);
      }
      u32x4 km; gld16_sc1<0>(km, ws + WS_KMAX, l16);
      asm volatile("s_waitcnt vmcnt(0)" : "+v"(km) :: "memory");
      float m = fmaxf(fmaxf(__uint_as_float(km[0]), __uint_as_float(km[1])), fmaxf(__uint_as_float(km[2]), __uint_as_float(km[3])));
      m = __builtin_amdgcn_sqrtf(col_max(row_max(m))) * 1.01f;
      if (lane == 0) *LP(float, R2 + 1028) = m;
      if (bid == 0 && lane == 0) gst8_sc1(ws, WS_EPOCH, (u32x2){(unsigned)tag, (unsigned)(tag >> 32)});
      WAITV(0);
    }
    TS(7);
    BAR();
    TS(8);
    const unsigned kvo = (a & 1) * 65536 + (a >> 1) * 4096;
    dma16x4_sc1(ws + WS_K + kvo, l16, kvo);
    const int ch = a >> 1;
    u32x4 uA, uB; unsigned uAp, uBp;
    { const char* us = ws + WS_UT + (ch0 + ch) * 4096 + (a & 1) * 2048;
      gld16_sc1<0>(uA, us, l16); gld16_sc1<1024>(uB, us, l16); gld4_sc1<-4>(uAp, us, l16); gld4_sc1<1020>(uBp, us, l16); }
#pragma unroll
    for (int r = 1; r < 5; ++r) dma16x4_sc1(ws + WS_K + kvo + 8192 * r, l16, kvo + 8192 * r);
    const float qk_ = fmaxf(fmaxf(*LP(float, R2 + 1024), *LP(float, R2 + 4096 + 1024)), fmaxf(*LP(float, R2 + 8192 + 1024), *LP(float, R2 + 12288 + 1024))) * *LP(float, R2 + 1028);
    const bool unsafe = (unsigned)__builtin_amdgcn_readfirstlane(__float_as_int(qk_)) > __float_as_uint(SAFE_BOUND);
    __builtin_amdgcn_s_setprio(2);
    WAITV(20);
    BAR();
    TS(9);
    for (int task = a; task < 6; task += 4) {
      unsigned pb = PART + 1024 * task + l16; PIN(pb);
      f32x4 v = *LP(f32x4, pb);
#pragma unroll
      for (int s = 1; s < 4; ++s) v += *LP(f32x4, pb + 6144 * s);
      pb += R1; PIN(pb);
#pragma unroll
      for (int s = 0; s < 4; ++s) v += *LP(f32x4, pb + 6144 * s);
      const int c = task >= 3, tau = task - 3 * c;
      const unsigned kb = KA + KA_STRIDE * c + 2 * 2055;
      const unsigned c01 = cvtpk(v[0], v[1]), c23 = cvtpk(v[2], v[3]);
      const u16 b0 = (u16)c01, b1 = (u16)(c01 >> 16), b2 = (u16)c23, b3 = (u16)(c23 >> 16);
      if (tau == 0) {
        const unsigned p = kb - 2 * (4 * g + 4 + 16 * r16);
        *LP(u16, p + 6) = b0; *LP(u16, p + 4) = b1; *LP(u16, p + 2) = b2; *LP(u16, p) = b3;
        if (lane == 63) *LP(u16, p - 2) = b3;
      } else if (tau == 1) {
        const bool up = g >= 2;
        const unsigned a0 = kb - 2 * ((up ? 516 + 256 * (g - 2) : 258 + 8 * g) + 16 * r16) - 8, st = up ? 8 : 4;
        const unsigned a1 = a0 - st, a2 = a1 - st, a3 = a2 - st;
        *LP(u16, a0 + 8) = b0; *LP(u16, a2 + 8) = b2;
        *LP(u16, a1 + 6) = b1; *LP(u16, a1 + 8) = b1; *LP(u16, a1 + 10) = b1;
        *LP(u16, a3 + 6) = b3; *LP(u16, a3 + 8) = b3; *LP(u16, a3 + 10) = b3;
        if (up) {
          *LP(u16, a0 + 6) = b0; *LP(u16, a0 + 10) = b0; *LP(u16, a2 + 6) = b2; *LP(u16, a2 + 10) = b2;
          *LP(u16, a1 + 4) = b1; *LP(u16, a1 + 12) = b1; *LP(u16, a3 + 4) = b3; *LP(u16, a3 + 12) = b3;
        }
        if (lane == 31) *LP(u16, a3 + 4) = b3;
        if (lane == 63) { *LP(u16, a3 + 2) = b3; *LP(u16, a3) = b3; }
      } else if ((g >> 1) == c) {
        const unsigned p = kb - 2 * (1032 + 512 * (g & 1) + 16 * r16) - 536;
        const unsigned d0 = cvtpk(v[0], v[0]), d1 = cvtpk(v[1], v[1]), d2 = cvtpk(v[2], v[2]), d3 = cvtpk(v[3], v[3]);
        *LP(u32x2, p + 536 - 6) = (u32x2){d0, d0}; *LP(unsigned, p + 536 + 2) = d0; *LP(u16, p + 536 + 6) = b0;
        *LP(u32x2, p + 520 - 6) = (u32x2){d1, d1}; *LP(u32x2, p + 520 + 2) = (u32x2){d1, d1}; *LP(u16, p + 520 - 8) = b1;
        *LP(u32x2, p + 24 - 6) = (u32x2){d2, d2}; *LP(unsigned, p + 24 + 2) = d2; *LP(u16, p + 24 + 6) = b2;
        *LP(u32x2, p + 8 - 6) = (u32x2){d3, d3}; *LP(u32x2, p + 8 + 2) = (u32x2){d3, d3}; *LP(u16, p) = b3;
      }
    }
    if (a >= 2) {
      const unsigned kb = KA + KA_STRIDE * (a & 1) + 2 * 2055;
      if (lane < 60) *LP(u32x2, kb + 2 + 8 * lane) = (u32x2){0, 0};
      if (lane == 60) *LP(u16, kb) = (u16)cvtpk(k0, 0.f);
      if (lane < 4) *LP(u32x4, UC + UC_STRIDE * (2 * (a & 1) + (lane >> 1)) + 16 * (lane & 1)) = (u32x4){0, 0, 0, 0};
    }
    asm volatile("s_waitcnt vmcnt(16)" : "+v"(uA), "+v"(uB), "+v"(uAp), "+v"(uBp) :: "memory");
    {
      if (lane == 0 && (a & 1) == 0) uAp = 0;
      const unsigned ub = UC + UC_STRIDE * 2 * ch + 32 + (a & 1) * 2048 + l16;
      *LP(u32x4, ub) = uA; *LP(u32x4, ub + 1024) = uB;
      *LP(u32x4, ub + UC_STRIDE) = (u32x4){__builtin_amdgcn_alignbit(uA[0], uAp, 16), __builtin_amdgcn_alignbit(uA[1], uA[0], 16), __builtin_amdgcn_alignbit(uA[2], uA[1], 16), __builtin_amdgcn_alignbit(uA[3], uA[2], 16)};
      *LP(u32x4, ub + UC_STRIDE + 1024) = (u32x4){__builtin_amdgcn_alignbit(uB[0], uBp, 16), __builtin_amdgcn_alignbit(uB[1], uB[0], 16), __builtin_amdgcn_alignbit(uB[2], uB[1], 16), __builtin_amdgcn_alignbit(uB[3], uB[2], 16)};
    }
    WAITV(0);
    TS(10);
    BAR();
#pragma unroll
    for (int r = 5; r < 8; ++r) dma16x4_sc1(ws + WS_K + kvo + 8192 * r, l16, kvo + 8192 * r);
    if (unsafe) { WAITV(0); BAR(); }
    {
      const int cs = a & 1;
      unsigned ba = KA + KA_STRIDE * ch + 2 * (2040 - 16 * r16 + 8 * g) - 3584;
      unsigned aa = UC + UC_STRIDE * (2 * ch + 1 - (r16 & 1)) + 2 * (2 + (r16 & 14) + 8 * g);
      f32x4 y0 = {0, 0, 0, 0}, y1 = y0, y2 = y0, y3 = y0;
      for (int rr = 0; rr < 8; ++rr) {
        bf16x8 A[8], B[8];
#pragma unroll
        for (int i = 0; i < 8; ++i) {
          const LAS unsigned* p = LP(unsigned, aa + 1024 * (i >> 1)) + 128 * (i & 1);
          A[i] = AS8(((u32x4){p[0], p[1], p[2], p[3]}));
          B[i] = LP(bf16x8, ba)[32 * (7 - i)];
        }
        if (cs == 0) {
          y0 = MFMA16(A[0], B[0], y0);
#pragma unroll
          for (int t = 0; t <= 3; ++t) y1 = MFMA16(A[t], B[3 - t], y1);
#pragma unroll
          for (int t = 0; t <= 4; ++t) y2 = MFMA16(A[t], B[4 - t], y2);
#pragma unroll
          for (int t = 0; t <= 7; ++t) y3 = MFMA16(A[t], B[7 - t], y3);
        } else {
#pragma unroll
          for (int t = 0; t <= 1; ++t) y0 = MFMA16(A[t], B[1 - t], y0);
#pragma unroll
          for (int t = 0; t <= 2; ++t) y1 = MFMA16(A[t], B[2 - t], y1);
#pragma unroll
          for (int t = 0; t <= 5; ++t) y2 = MFMA16(A[t], B[5 - t], y2);
#pragma unroll
          for (int t = 0; t <= 6; ++t) y3 = MFMA16(A[t], B[6 - t], y3);
        }
        ba += 64; aa += 64;
      }
      char* yb = ws + WS_YT + (ch0 + ch) * 4096; const unsigned yo = 32 * r16 + 8 * g;
      gst8_sc1(yb, yo + 512 * (cs ? 1 : 0), (u32x2){min(cvtpk(y0[0], y0[1]), SENT - 1), min(cvtpk(y0[2], y0[3]), SENT - 1)});
      gst8_sc1(yb, yo + 512 * (cs ? 2 : 3), (u32x2){min(cvtpk(y1[0], y1[1]), SENT - 1), min(cvtpk(y1[2], y1[3]), SENT - 1)});
      gst8_sc1(yb, yo + 512 * (cs ? 5 : 4), (u32x2){min(cvtpk(y2[0], y2[1]), SENT - 1), min(cvtpk(y2[2], y2[3]), SENT - 1)});
      gst8_sc1(yb, yo + 512 * (cs ? 6 : 7), (u32x2){min(cvtpk(y3[0], y3[1]), SENT - 1), min(cvtpk(y3[2], y3[3]), SENT - 1)});
    }
    TS(11);
    WAITV(0);
    BAR();
    TS(12);
#pragma unroll
    for (int j = 0; j < 4; ++j)
      dma16x4v(wo + (128 * a + 32 * j) * 512 + 64 * hg, wvo, wvo + 7168, wvo + 2 * 7168, wvo + 3 * 7168, (a >> 1) * 65536 + (a & 1) * 16384 + 4096 * j);
    {
      u32x4 u0, u1, d0, d1, ya, yb;
      const unsigned uo = (row0 + 16 * a + r16) * 1024 + 128 * hg + 16 * g;
      gld16<0>(u0, u, uo); gld16<64>(u1, u, uo); gld16<0>(d0, Dsk, 128 * hg + 16 * g); gld16<64>(d1, Dsk, 128 * hg + 16 * g);
      const char* ys = ws + WS_YT + (64 * hg) * 4096 + (row0 + 16 * a) * 2; const unsigned yo = (lane >> 1) * 4096 + 16 * (lane & 1);
      _Pragma("unroll 1") for (unsigned spins = 0; spins < (1u << 20); ++spins) {
        gld16_sc1<0>(ya, ys, yo); gld16_sc1<0>(yb, ys + 32 * 4096, yo);
        asm volatile("s_waitcnt vmcnt(0)" : "+v"(ya), "+v"(yb) :: "memory");
        if (!__any(max(max4(ya), max4(yb)) == SENT)) break;
        __builtin_amdgcn_s_sleep(4);
      }
      asm volatile("" : "+v"(u0), "+v"(u1), "+v"(d0), "+v"(d1) :: "memory");
      const unsigned sc = R2 + 2048 * a;
      *LP(u32x4, sc + l16) = ya; *LP(u32x4, sc + 1024 + l16) = yb;
      const unsigned ta = sc + 256 * g + 8 * r16;
      const u32x4 t0 = __builtin_bit_cast(u32x4, trd2(ta, ta + 128)), t1 = __builtin_bit_cast(u32x4, trd2(ta + 1024, ta + 1152));
      u32x4 o0, o1;
#pragma unroll
      for (int i = 0; i < 4; ++i) {
        const unsigned s0 = cvtpk(bflo(u0[i]) * bflo(d0[i]), bfhi(u0[i]) * bfhi(d0[i])), s1 = cvtpk(bflo(u1[i]) * bflo(d1[i]), bfhi(u1[i]) * bfhi(d1[i]));
        o0[i] = cvtpk(bflo(t0[i]) + bflo(s0), bfhi(t0[i]) + bfhi(s0)); o1[i] = cvtpk(bflo(t1[i]) + bflo(s1), bfhi(t1[i]) + bfhi(s1));
      }
      const unsigned oo = (row0 + 16 * a + r16) * 2048 + 128 * hg + 16 * g;
      gst16(out, oo, o0); gst16(out, oo + 64, o1);
    }
    TS(13);
    WAITV(0);
    TS_FLUSH();
    return;
  }

  const unsigned slice = R2 + 4096 * w;
  u32x4 xk0, xk1;
  { const unsigned vo = (row0 + 8 * hg + (r16 & 7)) * 1024 + 128 * w + 16 * g;
    gld16<0>(xk0, x, vo); gld16<64>(xk1, x, vo); }
  dma16x2(wk + 1024 * w, l16, slice); dma16x2(wv + 1024 * w, l16, slice + 2048);
  unsigned p_ar, p_ai, p_br, p_bi, p_cr, p_ci, p_dt;
  { const unsigned t = tid & 255, d = ch0 + (t >> 7), vo = (d * 128 + (t & 127)) * 2;
    gld2(p_ar, Are, vo); gld2(p_ai, Aim, vo); gld2(p_br, Bre, vo); gld2(p_bi, Bim, vo); gld2(p_cr, Cre, vo); gld2(p_ci, Cim, vo); gld2(p_dt, logdt, d * 2); }
  asm volatile("s_waitcnt vmcnt(7)" : "+v"(xk0), "+v"(xk1) :: "memory");
  TS(2);
  {
    const unsigned ta = slice + 256 * g + 8 * r16;
    bf16x8 wk0 = trd2(ta, ta + 128), wk1 = trd2(ta + 1024, ta + 1152), wv0 = trd2(ta + 2048, ta + 2176), wv1 = trd2(ta + 3072, ta + 3200);
    f32x4 dk = {0, 0, 0, 0}, dv = {0, 0, 0, 0};
    dk = MFMA16(wk0, AS8(xk0), dk); dv = MFMA16(AS8(xk0), wv0, dv);
    dk = MFMA16(wk1, AS8(xk1), dk); dv = MFMA16(AS8(xk1), wv1, dv);
    PIN(dk); PIN(dv);
    *LP(f32x4, slice + l16) = dk; *LP(f32x4, slice + 1024 + l16) = dv;
  }
  asm volatile("s_waitcnt vmcnt(0)" : "+v"(p_ar), "+v"(p_ai), "+v"(p_br), "+v"(p_bi), "+v"(p_cr), "+v"(p_ci), "+v"(p_dt) :: "memory");
  if (w < 4) {
    const float dt = bflo(cvtpk(expf(bflo(p_dt)), 0.f));
    const float al = (dt * bflo(p_ar)) * 1.4426950408889634f;
    float be = (dt * bflo(p_ai)) * 0.15915494309189535f; be -= __builtin_rintf(be);
    const float br = dt * bflo(p_br), bi = dt * bflo(p_bi), cr = bflo(p_cr), ci = bflo(p_ci);
    const float xr = cr * br - ci * bi, xi = cr * bi + ci * br;
    *LP(f32x4, slice + 2048 + l16) = (f32x4){al, be, xr, xi};
    const float s = col_sum(row_sum(xr));
    if (lane == 0) *LP(float, slice + 3072) = s;
  }
  BAR();
  TS(3);
  f32x4 kacc[6];
  {
    const float eB = (float)(16 * r16), e1 = (float)(r16 + 1);
    const float e2 = (float)(r16 < 8 ? 258 + 2 * r16 : 516 + 256 * ((r16 - 8) >> 2) + 4 * (r16 & 3));
    const float e3 = (float)(1032 + 256 * ((r16 & 7) >> 1) + 8 * (r16 & 1));
    unsigned pa = R2 + 4096 * (w >> 2) + 2048 + 16 * ((16 * w + 4 * g) & 63); PIN(pa);
    u32x4 fb[2], f1[2], f2[2], f3;
#pragma unroll
    for (int c = 0; c < 2; ++c)
#pragma unroll
      for (int i = 0; i < 4; ++i) {
        const f32x4 P = *LP(f32x4, pa + 8192 * c + 16 * i);
        const f32x2 ab = {P[0], P[1]};
        const f32x2 wv_ = zpow<false>(ab, eB);
        fb[c][i] = cvtpk(P[2] * wv_.x - P[3] * wv_.y, P[2] * wv_.y + P[3] * wv_.x);
        const f32x2 a1 = zpow<false>(ab, e1); f1[c][i] = cvtpk(a1.x, -a1.y);
        const f32x2 a2 = zpow<true>(ab, e2); f2[c][i] = cvtpk(a2.x, -a2.y);
      }
    pa += 8192 * (r16 >> 3);
#pragma unroll
    for (int i = 0; i < 4; ++i) {
      const f32x2 ab = *LP(f32x2, pa + 16 * i);
      const f32x2 a3 = zpow<true>(ab, e3); f3[i] = cvtpk(a3.x, -a3.y);
    }
    const f32x4 z4 = {0, 0, 0, 0};
#pragma unroll
    for (int c = 0; c < 2; ++c) {
      kacc[3 * c + 0] = MFMA16(AS8(f1[c]), AS8(fb[c]), z4);
      kacc[3 * c + 1] = MFMA16(AS8(f2[c]), AS8(fb[c]), z4);
      kacc[3 * c + 2] = MFMA16(AS8(f3), AS8(fb[c]), z4);
    }
#pragma unroll
    for (int i = 0; i < 6; ++i) PIN(kacc[i]);
  }
  TS(5);
  BAR();
  TS(6);
  if (w < 4) {
    const int rh = w >> 1, hp = w & 1;
    const unsigned xa = R0 + 1024 * (32 * rh + r32) + 16 * (h ^ r16);
    const int q = r16 >> 2, p = lane & 3;
    unsigned wa = R1 + 128 * (8 * h + q) + 16 * ((4 * hp + 2 * (g & 1) + (p >> 1)) ^ ((q >> 1) << 2)) + 8 * (p & 1); PIN(wa);
    f32x16 acc = {0};
#pragma unroll
    for (int ks = 0; ks < 32; ++ks) {
      const bf16x8 b = *LP(bf16x8, (xa ^ ((ks & 7) << 5)) + 256 * (ks >> 3));
      const bf16x8 a = trd2(wa + 2048 * ks, wa + 2048 * ks + 512);
      acc = MFMA32(a, b, acc);
    }
    acc *= QSCALE;
    float n0 = 0, n1 = 0;
#pragma unroll
    for (int i = 0; i < 8; ++i) { n0 += acc[i] * acc[i]; n1 += acc[8 + i] * acc[8 + i]; }
    float o; n0 = x32(n0, o); n0 += o; n1 = x32(n1, o); n1 += o;
    n0 = __builtin_amdgcn_sqrtf(n0); n1 = __builtin_amdgcn_sqrtf(n1);
    const unsigned qs = R2 + 4096 * (4 + w);
    *LP(u32x4, qs + 2048 + l16) = (u32x4){cvtpk(acc[0], acc[1]), cvtpk(acc[2], acc[3]), cvtpk(acc[4], acc[5]), cvtpk(acc[6], acc[7])};
    *LP(u32x4, qs + 3072 + l16) = (u32x4){cvtpk(acc[8], acc[9]), cvtpk(acc[10], acc[11]), cvtpk(acc[12], acc[13]), cvtpk(acc[14], acc[15])};
    *LP(float, slice + 4 * lane) = n0; *LP(float, slice + 256 + 4 * lane) = n1;
    const float nm = col_max(row_max(fmaxf(n0, n1)));
    if (lane == 0) *LP(float, slice + 1024) = nm;
  } else if (w == 4 && lane < 12) {
    const unsigned j = lane >> 2, o1 = j ? 0u : 0x3f803f80u;
    *LP(u32x4, ONES + 512 * (lane & 3) + (j == 2 ? 64 : 16 * j)) = (u32x4){o1, o1, o1, o1};
  }
  TS(7);
  BAR();
  TS(8);
  const int qt = w & 3, hs = w >> 2;
  const bf16x8 qf = *LP(bf16x8, R2 + 4096 * (4 + qt) + 2048 + 1024 * hs + l16);
  const float kmax = *LP(float, R2 + 1028);
  float sh = *LP(float, R2 + 4096 * qt + 256 * hs + 4 * lane) * kmax;
  const float qmax = fmaxf(fmaxf(*LP(float, R2 + 1024), *LP(float, R2 + 4096 + 1024)), fmaxf(*LP(float, R2 + 8192 + 1024), *LP(float, R2 + 12288 + 1024)));
  const bool unsafe = (unsigned)__builtin_amdgcn_readfirstlane(__float_as_int(qmax * kmax)) > __float_as_uint(SAFE_BOUND);
  {
    const unsigned pb = (w >> 2) * R1 + PART + (w & 3) * 6144 + l16;
#pragma unroll
    for (int i = 0; i < 6; ++i) *LP(f32x4, pb + 1024 * i) = kacc[i];
  }
  BAR();
  TS(9);
  if (unsafe) {
    BAR(); BAR();
    const f32x16 z = {0};
    float m = -INFINITY;
    for (int t = 0; t < 64; ++t) {
      const f32x16 s = MFMA32(*LP(bf16x8, R0 + 1024 * t + l16), qf, z);
#pragma unroll
      for (int i = 0; i < 16; ++i) m = fmaxf(m, s[i]);
    }
    float o; m = x32(m, o); sh = fmaxf(m, o);
  }
  f32x16 oacc = {0};
  {
    f32x16 mneg;
#pragma unroll
    for (int i = 0; i < 16; ++i) mneg[i] = -sh;
    PIN(mneg);
    const bool lo = (lane & 16) == 0;
    unsigned kp = R0 + l16;
    unsigned vp = lo ? R1 + 256 * h + 16 * r16 : ONES + (r32 == 16 ? 0 : (r32 >= 20 && r32 < 28) ? 64 : 16);
    const unsigned vinc = lo ? 2048 : 0;
    const int tb = unsafe ? -1 : 4;
    f32x16 sA, sB; u32x4 pA0, pA1, pB0 = {0, 0, 0, 0}, pB1;
    bf16x8 kf = *LP(bf16x8, kp), v0, v1;
    sA = MFMA32(kf, qf, mneg);
    kf = *LP(bf16x8, kp + 1024);
    v0 = *LP(bf16x8, vp); v1 = v0;
#pragma unroll
    for (int i = 0; i < 16; ++i) sB[i] = 0.f;
#define CV(P, j, S, i) P[j] = cvtpk(S[i], S[i + 1])
#define E4(S, i) do { S[i] = EX2(S[i]); S[i + 1] = EX2(S[i + 1]); S[i + 2] = EX2(S[i + 2]); S[i + 3] = EX2(S[i + 3]); \
                      asm volatile("" : "+v"(S[i]), "+v"(S[i + 1]), "+v"(S[i + 2]), "+v"(S[i + 3])); SBAR(); } while (0)
#define UNIT(S, P0, T, Q0, Q1, KOFF, VOFF) do { \
      E4(S, 0); CV(Q1, 0, T, 8); CV(Q1, 1, T, 10); CV(Q1, 2, T, 12); CV(Q1, 3, T, 14); PIN(Q1); SBAR(); \
      T = MFMA32(kf, qf, mneg); kf = *LP(bf16x8, kp + KOFF); SBAR(); E4(S, 4); \
      oacc = MFMA32(v0, AS8(Q0), oacc); v0 = *LP(bf16x8, vp + VOFF); SBAR(); E4(S, 8); \
      oacc = MFMA32(v1, AS8(Q1), oacc); v1 = *LP(bf16x8, vp + VOFF + 512); SBAR(); \
      CV(P0, 0, S, 0); CV(P0, 1, S, 2); CV(P0, 2, S, 4); CV(P0, 3, S, 6); PIN(P0); SBAR(); E4(S, 12); } while (0)
    for (int t = 0; t < 64; t += 4) {
      if (t == tb || t == 32) BAR();
      UNIT(sA, pA0, sB, pB0, pB1, 2048, 0);
      UNIT(sB, pB0, sA, pA0, pA1, 3072, 1024);
      vp += vinc;
      UNIT(sA, pA0, sB, pB0, pB1, 4096, 0);
      UNIT(sB, pB0, sA, pA0, pA1, 5120, 1024);
      kp += 4096; vp += vinc;
    }
    CV(pB1, 0, sB, 8); CV(pB1, 1, sB, 10); CV(pB1, 2, sB, 12); CV(pB1, 3, sB, 14);
    oacc = MFMA32(v0, AS8(pB0), oacc); oacc = MFMA32(v1, AS8(pB1), oacc);
  }
  TS(10);
  {
    const float rl = __builtin_amdgcn_rcpf(oacc[8]);
    float o; const float r0 = x32(rl, o);
    u32x4 of_ = {cvtpk(oacc[0] * r0, oacc[1] * r0), cvtpk(oacc[2] * r0, oacc[3] * r0), cvtpk(oacc[4] * r0, oacc[5] * r0), cvtpk(oacc[6] * r0, oacc[7] * r0)};
#pragma unroll
    for (int i = 0; i < 4; ++i) of_[i] = min(of_[i], SENT - 1);
    gst16_sc1<0>(ws + WS_O + ((rg * 2 + (qt >> 1)) * 32 + 4 * hg + 2 * (qt & 1) + hs) * 1024, l16, of_);
  }
  BAR();
  TS(11);
  {
    const int rh = w >> 2, kq = w & 3;
    const char* osrc = ws + WS_O + ((rg * 2 + rh) * 32 + 8 * kq) * 1024;
    _Pragma("unroll 1") for (unsigned spins = 0; spins < (1u << 20); ++spins) {
      unsigned t; gld4_sc1<1020>(t, osrc, (lane & 7) * 1024);
      asm volatile("s_waitcnt vmcnt(0)" : "+v"(t) :: "memory");
      if (!__any(t == SENT)) break;
    }
    TS(12);
    u32x4 of[8];
    _Pragma("unroll 1") for (unsigned spins = 0; spins < (1u << 20); ++spins) {
      gld16_sc1<0>(of[0], osrc, l16); gld16_sc1<1024>(of[1], osrc, l16); gld16_sc1<2048>(of[2], osrc, l16); gld16_sc1<3072>(of[3], osrc, l16);
      gld16_sc1<0>(of[4], osrc + 4096, l16); gld16_sc1<1024>(of[5], osrc + 4096, l16); gld16_sc1<2048>(of[6], osrc + 4096, l16); gld16_sc1<3072>(of[7], osrc + 4096, l16);
      asm volatile("s_waitcnt vmcnt(0)" : "+v"(of[0]), "+v"(of[1]), "+v"(of[2]), "+v"(of[3]), "+v"(of[4]), "+v"(of[5]), "+v"(of[6]), "+v"(of[7]) :: "memory");
      unsigned mx = 0;
#pragma unroll
      for (int i = 0; i < 8; ++i) mx = max(mx, max4(of[i]));
      if (!__any(mx == SENT)) break;
    }
    TS(13);
    const int q = r16 >> 2, p = lane & 3;
    unsigned wa = (kq >> 1) * R1 + (kq & 1) * 16384 + 128 * (4 * h + q) + 16 * ((2 * (g & 1) + (p >> 1)) ^ ((q >> 1) << 2)) + 8 * (p & 1); PIN(wa);
    const unsigned wb = wa ^ 64;
    f32x16 c0 = {0}, c1 = {0};
#pragma unroll
    for (int ks = 0; ks < 8; ++ks) {
      c0 = MFMA32(trd2(wa + 2048 * ks, wa + 2048 * ks + 1024), AS8(of[ks]), c0);
      c1 = MFMA32(trd2(wb + 2048 * ks, wb + 2048 * ks + 1024), AS8(of[ks]), c1);
    }
    const unsigned rbase = 32768 + rh * R1;
    const f32x4 G[8] = {{c0[0], c0[1], c0[2], c0[3]}, {c0[4], c0[5], c0[6], c0[7]}, {c0[8], c0[9], c0[10], c0[11]}, {c0[12], c0[13], c0[14], c0[15]},
                        {c1[0], c1[1], c1[2], c1[3]}, {c1[4], c1[5], c1[6], c1[7]}, {c1[8], c1[9], c1[10], c1[11]}, {c1[12], c1[13], c1[14], c1[15]}};
    unsigned wr = rbase + 2048 * kq + l16; PIN(wr);
#pragma unroll
    for (int j = 0; j < 4; ++j) { *LP(f32x4, wr + 8192 * j) = G[2 * j]; *LP(f32x4, wr + 8192 * j + 1024) = G[2 * j + 1]; }
    BAR();
    unsigned rd = rbase + 8192 * kq + l16; PIN(rd);
    f32x4 a = *LP(f32x4, rd), b = *LP(f32x4, rd + 1024);
#pragma unroll
    for (int s = 1; s < 4; ++s) { a += *LP(f32x4, rd + 2048 * s); b += *LP(f32x4, rd + 2048 * s + 1024); }
    auto s0 = __builtin_amdgcn_permlane32_swap(cvtpk(a[0], a[1]), cvtpk(b[0], b[1]), false, false);
    auto s1 = __builtin_amdgcn_permlane32_swap(cvtpk(a[2], a[3]), cvtpk(b[2], b[3]), false, false);
    gst16(out, (row0 + 32 * rh + r32) * 2048 + 1024 + 128 * hg + 32 * kq + 16 * h, (u32x4){s0[0], s1[0], s0[1], s1[1]});
  }
  TS_FLUSH();
}

extern "C" void kernel_launch(void* const* d_in, const int* in_sizes, int n_in, void* d_out, int out_size, void* d_ws, size_t ws_size, hipStream_t stream) {
  (void)in_sizes; (void)n_in; (void)out_size; (void)ws_size;
  (void)hipFuncSetAttribute((const void*)fused_kernel, hipFuncAttributeMaxDynamicSharedMemorySize, LDS_BYTES);
  fused_kernel<<<256, 768, LDS_BYTES, stream>>>((const u16*)d_in[0], (const u16*)d_in[1], (const u16*)d_in[2], (const u16*)d_in[3], (const u16*)d_in[4],
      (const u16*)d_in[5], (const u16*)d_in[6], (const u16*)d_in[7], (const u16*)d_in[8], (const u16*)d_in[9], (const u16*)d_in[10], (const u16*)d_in[11],
      (const u16*)d_in[12], (const u16*)d_in[13], (u16*)d_out, (char*)d_ws);
}
```

```cpp
#define DIAG 1
#define DMODE 0
#include <hip/hip_runtime.h>
#include <hip/hip_bf16.h>
#pragma clang diagnostic ignored "-Wint-to-pointer-cast"

typedef unsigned short u16;
typedef unsigned long long u64;
typedef __attribute__((ext_vector_type(8))) short bf16x8;
typedef __attribute__((ext_vector_type(4))) short s16x4;
typedef __attribute__((ext_vector_type(16))) float f32x16;
typedef __attribute__((ext_vector_type(4))) float f32x4;
typedef __attribute__((ext_vector_type(2))) float f32x2;
typedef __attribute__((ext_vector_type(4))) unsigned u32x4;
typedef __attribute__((ext_vector_type(2))) unsigned u32x2;
typedef __attribute__((ext_vector_type(2))) __bf16 bf16x2;
#define LAS __attribute__((address_space(3)))
#define LP(T, off) ((LAS T*)(unsigned)(off))
#define SBAR() __builtin_amdgcn_sched_barrier(0)
#define PIN(x) asm volatile("" : "+v"(x))
#define MFMA32(a, b, c) __builtin_amdgcn_mfma_f32_32x32x16_bf16(a, b, c, 0, 0, 0)
#define MFMA16(a, b, c) __builtin_amdgcn_mfma_f32_16x16x32_bf16(a, b, c, 0, 0, 0)
#define AS8(x) __builtin_bit_cast(bf16x8, x)
#define EX2(v) __builtin_amdgcn_exp2f(v)
#define BAR() asm volatile("s_waitcnt lgkmcnt(0)\n\ts_barrier" ::: "memory")
#define WAITV(n) asm volatile("s_waitcnt vmcnt(" #n ")" ::: "memory")

#define STAMP 0
#define TS(i) do { if ((i) == DIAG) asm volatile("s_memrealtime %0\n\ts_waitcnt lgkmcnt(0)" : "=s"(dg1_) :: "memory"); } while (0)
#define TSR(i) do { asm volatile("s_memrealtime %0\n\ts_waitcnt lgkmcnt(0)" : "=s"(dg0_) :: "memory"); dg1_ = dg0_; } while (0)
#define TS_FLUSH() do { TS(14); if (w == 0) { const unsigned mn_ = __builtin_amdgcn_readfirstlane(*LP(int, ONES + 128)), mx_ = __builtin_amdgcn_readfirstlane(*LP(int, ONES + 132)); \
  const unsigned tgt_ = mn_ + 4000 + (DMODE == 0 ? 0 : DMODE == 1 ? 4 * (mx_ - mn_) : 4 * ((unsigned)dg1_ - mn_)); u64 n_; do { __builtin_amdgcn_s_sleep(8); asm volatile("s_memrealtime %0\n\ts_waitcnt lgkmcnt(0)" : "=s"(n_) :: "memory"); } while ((int)((unsigned)n_ - tgt_) < 0); } } while (0)

constexpr unsigned WS_EPOCH = 0, WS_FLAG = 1024, WS_KMAX = 4096, WS_K = 8192, WS_V = WS_K + 65536, WS_O = 1u << 18, WS_UT = WS_O + (2u << 20), WS_YT = WS_UT + (2u << 20), WS_DBG = 8u << 20;
constexpr unsigned R0 = 0, R1 = 65536, R2 = 131072, LDS_BYTES = 163840;
constexpr unsigned KA = R2, KA_STRIDE = 4608;
constexpr unsigned UC = R2 + 9216, UC_STRIDE = 4160;
constexpr unsigned ONES = R2 + 28672;
constexpr unsigned PART = 40960;
constexpr float QSCALE = 0.25f * 1.4426950408889634f;
constexpr float SAFE_BOUND = 40.f;
constexpr unsigned SENT = 0xffffffffu;
constexpr int CODE_KB = 15;

__device__ __forceinline__ unsigned cvtpk(float lo, float hi) { f32x2 v = {lo, hi}; return __builtin_bit_cast(unsigned, __builtin_convertvector(v, bf16x2)); }
__device__ __forceinline__ float bflo(unsigned x) { return __uint_as_float(x << 16); }
__device__ __forceinline__ float bfhi(unsigned x) { return __uint_as_float(x & 0xffff0000u); }
template <int C> __device__ __forceinline__ float dpp(float v) { return __int_as_float(__builtin_amdgcn_update_dpp(0, __float_as_int(v), C, 0xf, 0xf, true)); }
__device__ __forceinline__ float x16(float v, float& o) { auto r = __builtin_amdgcn_permlane16_swap(__float_as_uint(v), __float_as_uint(v), false, false); o = __uint_as_float(r[1]); return __uint_as_float(r[0]); }
__device__ __forceinline__ float x32(float v, float& o) { auto r = __builtin_amdgcn_permlane32_swap(__float_as_uint(v), __float_as_uint(v), false, false); o = __uint_as_float(r[1]); return __uint_as_float(r[0]); }
__device__ __forceinline__ float row_sum(float v) { v += dpp<0x128>(v); v += dpp<0x124>(v); v += dpp<0x122>(v); v += dpp<0x121>(v); return v; }
__device__ __forceinline__ float row_max(float v) { v = fmaxf(v, dpp<0x128>(v)); v = fmaxf(v, dpp<0x124>(v)); v = fmaxf(v, dpp<0x122>(v)); v = fmaxf(v, dpp<0x121>(v)); return v; }
__device__ __forceinline__ float col_sum(float v) { float o; v = x16(v, o); v += o; v = x32(v, o); return v + o; }
__device__ __forceinline__ float col_max(float v) { float o; v = x16(v, o); v = fmaxf(v, o); v = x32(v, o); return fmaxf(v, o); }

__device__ __forceinline__ void dma16(const void* base, unsigned voff, unsigned ldst) {
  asm volatile("s_mov_b32 m0, %2\n\ts_nop 0\n\tglobal_load_lds_dwordx4 %0, %1" :: "v"(voff), "s"(base), "s"(ldst) : "memory");
}
__device__ __forceinline__ void dma16x2(const void* base, unsigned voff, unsigned ldst) {
  asm volatile("s_mov_b32 m0, %2\n\ts_nop 0\n\tglobal_load_lds_dwordx4 %0, %1\n\tglobal_load_lds_dwordx4 %0, %1 offset:1024" :: "v"(voff), "s"(base), "s"(ldst) : "memory");
}
__device__ __forceinline__ void dma16x4v(const void* base, unsigned v0, unsigned v1, unsigned v2, unsigned v3, unsigned ldst) {
  asm volatile("s_mov_b32 m0, %5\n\ts_nop 0\n\tglobal_load_lds_dwordx4 %0, %4\n\tglobal_load_lds_dwordx4 %1, %4 offset:1024\n\t"
               "global_load_lds_dwordx4 %2, %4 offset:2048\n\tglobal_load_lds_dwordx4 %3, %4 offset:3072" :: "v"(v0), "v"(v1), "v"(v2), "v"(v3), "s"(base), "s"(ldst) : "memory");
}
__device__ __forceinline__ void dma16x4_sc1(const void* base, unsigned voff, unsigned ldst) {
  asm volatile("s_mov_b32 m0, %2\n\ts_nop 0\n\tglobal_load_lds_dwordx4 %0, %1 sc1\n\tglobal_load_lds_dwordx4 %0, %1 offset:1024 sc1\n\t"
               "global_load_lds_dwordx4 %0, %1 offset:2048 sc1\n\tglobal_load_lds_dwordx4 %0, %1 offset:3072 sc1" :: "v"(voff), "s"(base), "s"(ldst) : "memory");
}
template <int OFF> __device__ __forceinline__ void gld16(u32x4& d, const void* base, unsigned voff) { asm volatile("global_load_dwordx4 %0, %1, %2 offset:%3" : "=v"(d) : "v"(voff), "s"(base), "i"(OFF) : "memory"); }
template <int OFF> __device__ __forceinline__ void gld16_sc1(u32x4& d, const void* base, unsigned voff) { asm volatile("global_load_dwordx4 %0, %1, %2 offset:%3 sc1" : "=v"(d) : "v"(voff), "s"(base), "i"(OFF) : "memory"); }
template <int OFF> __device__ __forceinline__ void gld4_sc1(unsigned& d, const void* base, unsigned voff) { asm volatile("global_load_dword %0, %1, %2 offset:%3 sc1" : "=v"(d) : "v"(voff), "s"(base), "i"(OFF) : "memory"); }
__device__ __forceinline__ void gld2(unsigned& d, const void* base, unsigned voff) { asm volatile("global_load_ushort %0, %1, %2" : "=v"(d) : "v"(voff), "s"(base) : "memory"); }
__device__ __forceinline__ void gld8_sc1(u32x2& d, const void* base, unsigned voff) { asm volatile("global_load_dwordx2 %0, %1, %2 sc1" : "=v"(d) : "v"(voff), "s"(base) : "memory"); }
template <int OFF> __device__ __forceinline__ void gst16_sc1(void* base, unsigned voff, u32x4 d) { asm volatile("global_store_dwordx4 %0, %1, %2 offset:%3 sc1\n\ts_nop 1" :: "v"(voff), "v"(d), "s"(base), "i"(OFF) : "memory"); }
__device__ __forceinline__ void gst16(void* base, unsigned voff, u32x4 d) { asm volatile("global_store_dwordx4 %0, %1, %2\n\ts_nop 1" :: "v"(voff), "v"(d), "s"(base) : "memory"); }
__device__ __forceinline__ void gst8_sc1(void* base, unsigned voff, u32x2 d) { asm volatile("global_store_dwordx2 %0, %1, %2 sc1" :: "v"(voff), "v"(d), "s"(base) : "memory"); }
__device__ __forceinline__ void gst4_sc1(void* base, unsigned voff, unsigned d) { asm volatile("global_store_dword %0, %1, %2 sc1" :: "v"(voff), "v"(d), "s"(base) : "memory"); }
__device__ __forceinline__ s16x4 trd(unsigned a) { return __builtin_bit_cast(s16x4, __builtin_amdgcn_ds_read_tr16_b64_v4i16(LP(s16x4, a))); }
__device__ __forceinline__ bf16x8 trd2(unsigned a0, unsigned a1) { s16x4 x = trd(a0), y = trd(a1); return (bf16x8){x[0], x[1], x[2], x[3], y[0], y[1], y[2], y[3]}; }
__device__ __forceinline__ unsigned max4(u32x4 v) { return max(max(v[0], v[1]), max(v[2], v[3])); }

template <bool FR> __device__ __forceinline__ f32x2 zpow(f32x2 ab, float e) {
  f32x2 t = ab * e;
  float m = EX2(t.x);
  float a = FR ? __builtin_amdgcn_fractf(t.y) : t.y;
  f32x2 cs = {__builtin_amdgcn_cosf(a), __builtin_amdgcn_sinf(a)};
  return cs * m;
}

__global__ void __launch_bounds__(768, 3)
fused_kernel(const u16* __restrict__ u, const u16* __restrict__ Are, const u16* __restrict__ Aim, const u16* __restrict__ Bre, const u16* __restrict__ Bim,
             const u16* __restrict__ Cre, const u16* __restrict__ Cim, const u16* __restrict__ logdt, const u16* __restrict__ Dsk,
             const u16* __restrict__ x, const u16* __restrict__ wq, const u16* __restrict__ wk, const u16* __restrict__ wv, const u16* __restrict__ wo,
             u16* __restrict__ out, char* __restrict__ ws) {
  u64 dg0_, dg1_; asm volatile("s_memrealtime %0\n\ts_waitcnt lgkmcnt(0)" : "=s"(dg0_) :: "memory"); __builtin_amdgcn_sched_barrier(0); dg1_ = dg0_;
  u64 pc0; asm volatile("s_getpc_b64 %0" : "=s"(pc0));
  const int tid = threadIdx.x, lane = tid & 63;
  const int w = __builtin_amdgcn_readfirstlane(tid >> 6);
  const int bid = blockIdx.x, xcd = bid & 7, loc = bid >> 3;
  const int rg = xcd * 4 + (loc >> 3), hg = loc & 7, row0 = rg * 64, ch0 = xcd * 64 + loc * 2;
  const int r16 = lane & 15, g = lane >> 4, r32 = lane & 31, h = lane >> 5;
  const unsigned l16 = 16 * lane;
  const unsigned wvo = (lane >> 3) * 1024 + 16 * ((lane & 7) ^ (((lane >> 4) & 1) << 2));

  if (w >= 8) {
    const int a = w - 8;
    u64 tag = 0;
    if (a == 0) {
      u32x2 ep; gld8_sc1(ep, ws + WS_EPOCH, 0);
      const unsigned ust = R0 + 1024 * ((8 * hg + 56) & 63);
      const u16* ub = u + row0 * 512 + 64 * hg;
      const unsigned uvo = (lane >> 3) * 1024 + 16 * (lane & 7);
      dma16x4v(ub, uvo, uvo + 7168, uvo + 2 * 7168, uvo + 3 * 7168, ust); dma16x4v(ub + 16384, uvo, uvo + 7168, uvo + 2 * 7168, uvo + 3 * 7168, ust + 4096);
      const u32x4 sent = {SENT, SENT, SENT, SENT};
      char* os = ws + WS_O + (rg * 64 + 4 * hg) * 1024; char* ys = ws + WS_YT + ch0 * 4096;
      gst16_sc1<0>(os, l16, sent); gst16_sc1<1024>(os, l16, sent); gst16_sc1<2048>(os, l16, sent); gst16_sc1<3072>(os, l16, sent);
      gst16_sc1<0>(os + 32768, l16, sent); gst16_sc1<1024>(os + 32768, l16, sent); gst16_sc1<2048>(os + 32768, l16, sent); gst16_sc1<3072>(os + 32768, l16, sent);
      gst16_sc1<0>(ys, l16, sent); gst16_sc1<1024>(ys, l16, sent); gst16_sc1<2048>(ys, l16, sent); gst16_sc1<3072>(ys, l16, sent);
      gst16_sc1<0>(ys + 4096, l16, sent); gst16_sc1<1024>(ys + 4096, l16, sent); gst16_sc1<2048>(ys + 4096, l16, sent); gst16_sc1<3072>(ys + 4096, l16, sent);
      asm volatile("s_waitcnt vmcnt(16)" : "+v"(ep) :: "memory");
      tag = (((u64)(unsigned)__builtin_amdgcn_readfirstlane(ep[1]) << 32) | (unsigned)__builtin_amdgcn_readfirstlane(ep[0])) + 1;
      TS(2);
      unsigned ta = ust + 128 * (8 * g + (r16 >> 2)) + 8 * (lane & 3); PIN(ta);
      char* ut = ws + WS_UT + (64 * hg) * 4096 + row0 * 2;
      const unsigned so = r16 * 4096 + 16 * g;
#pragma unroll
      for (int j = 0; j < 8; ++j) {
        const bf16x8 v = trd2(ta + 4096 * (j >> 2) + 32 * (j & 3), ta + 4096 * (j >> 2) + 32 * (j & 3) + 512);
        if (j >> 2) gst16_sc1<64>(ut + 65536 * (j & 3), so, __builtin_bit_cast(u32x4, v)); else gst16_sc1<0>(ut + 65536 * (j & 3), so, __builtin_bit_cast(u32x4, v));
      }
    } else if (a == 3) {
      if (loc < CODE_KB) {
        unsigned dummy; asm volatile("global_load_dword %0, %1, %2\n\ts_waitcnt vmcnt(0)" : "=v"(dummy) : "v"(loc * 1024 + l16), "s"(pc0) : "memory");
      }
    }
    auto dma_x = [&](int i0, int i1) { _Pragma("unroll 1") for (int i = i0; i < i1; ++i) { const int r = (8 * hg + i) & 63; dma16(x + (row0 + r) * 512, 16 * (lane ^ (r & 15)), R0 + 1024 * r); } };
    auto dma_wq = [&](int i0, int i1) { _Pragma("unroll 1") for (int i = i0; i < i1; ++i) { const int p = (16 * (loc >> 3) + i) & 63; dma16(wq + p * 4096 + 64 * hg, wvo, R1 + 1024 * p); } };
    dma_x(0, a == 1 ? 8 : 0); dma_wq(0, a == 2 ? 16 : 0);
    BAR();
    TS(3);
    if (a == 0) {
      unsigned rb = R2 + l16; PIN(rb);
      f32x4 dk = *LP(f32x4, rb), dv = *LP(f32x4, rb + 1024);
#pragma unroll
      for (int s = 1; s < 8; ++s) { dk += *LP(f32x4, rb + 4096 * s); dv += *LP(f32x4, rb + 4096 * s + 1024); }
      const int kk0 = row0 + 8 * hg, t = kk0 >> 5, c = (kk0 >> 3) & 3;
      const u32x2 kp = {cvtpk(dk[0], dk[1]), cvtpk(dk[2], dk[3])}, vp = {cvtpk(dv[0], dv[1]), cvtpk(dv[2], dv[3])};
      if (r16 < 8) gst8_sc1(ws, WS_K + t * 1024 + ((g & 1) * 32 + 8 * c + r16) * 16 + 8 * (g >> 1), kp);
      if (g < 2) gst8_sc1(ws, WS_V + t * 1024 + 512 * (c >> 1) + 256 * g + 16 * r16 + 8 * (c & 1), vp);
      const float k2 = row_max(col_sum(dk[0] * dk[0] + dk[1] * dk[1] + dk[2] * dk[2] + dk[3] * dk[3]));
      if (lane == 0) gst4_sc1(ws, WS_KMAX + 4 * bid, __float_as_uint(k2));
      if (lane == 0) gst4_sc1(ws, WS_KMAX + 1024 + 4 * bid, (unsigned)dg0_);
      TS(4);
      WAITV(0);
      if (lane == 0) gst8_sc1(ws, WS_FLAG + 8 * bid, (u32x2){(unsigned)tag, (unsigned)(tag >> 32)});
    } else {
      dma_x(a == 1 ? 8 : 43, a == 1 ? 43 : a == 2 ? 0 : 64); dma_wq(a == 2 ? 16 : 43, a == 2 ? 43 : a == 1 ? 0 : 64);
      TS(4);
      WAITV(0);
    }
    TS(5);
    BAR();
    TS(6);
    const float k0 = *LP(float, R2 + 4096 * (2 * (a & 1)) + 3072) + *LP(float, R2 + 4096 * (2 * (a & 1) + 1) + 3072);
    if (a == 0) {
      _Pragma("unroll 1") for (unsigned spins = 0; spins < (1u << 20); ++spins) {
        u32x4 f0, f1; gld16_sc1<0>(f0, ws + WS_FLAG, 32 * lane); gld16_sc1<16>(f1, ws + WS_FLAG, 32 * lane);
        asm volatile("s_waitcnt vmcnt(0)" : "+v"(f0), "+v"(f1) :: "memory");
        const unsigned lo = (unsigned)tag, hi = (unsigned)(tag >> 32);
        const bool ok = f0[0] == lo && f0[1] == hi && f0[2] == lo && f0[3] == hi && f1[0] == lo && f1[1] == hi && f1[2] == lo && f1[3] == hi;
        if (__all(ok)) break;
        __builtin_amdgcn_s_sleep(1);
      }
      u32x4 km; gld16_sc1<0>(km, ws + WS_KMAX, l16);
      asm volatile("s_waitcnt vmcnt(0)" : "+v"(km) :: "memory");
      float m = fmaxf(fmaxf(__uint_as_float(km[0]), __uint_as_float(km[1])), fmaxf(__uint_as_float(km[2]), __uint_as_float(km[3])));
      m = __builtin_amdgcn_sqrtf(col_max(row_max(m))) * 1.01f;
      if (lane == 0) *LP(float, R2 + 1028) = m;
      { u32x4 st; gld16_sc1<1024>(st, ws + WS_KMAX, l16); asm volatile("s_waitcnt vmcnt(0)" : "+v"(st) :: "memory");
        int base = (int)(unsigned)dg0_; int mn = 1 << 30, mx = -(1 << 30);
        for (int i = 0; i < 4; ++i) { int d = (int)st[i] - base; mn = min(mn, d); mx = max(mx, d); }
        for (int o = 1; o < 64; o <<= 1) { mn = min(mn, __shfl_xor(mn, o)); mx = max(mx, __shfl_xor(mx, o)); }
        if (lane == 0) { *LP(int, ONES + 128) = mn + base; *LP(int, ONES + 132) = mx + base; } }
      if (bid == 0 && lane == 0) gst8_sc1(ws, WS_EPOCH, (u32x2){(unsigned)tag, (unsigned)(tag >> 32)});
      WAITV(0);
    }
    TS(7);
    BAR();
    TS(8);
    const unsigned kvo = (a & 1) * 65536 + (a >> 1) * 4096;
    dma16x4_sc1(ws + WS_K + kvo, l16, kvo);
    const int ch = a >> 1;
    u32x4 uA, uB; unsigned uAp, uBp;
    { const char* us = ws + WS_UT + (ch0 + ch) * 4096 + (a & 1) * 2048;
      gld16_sc1<0>(uA, us, l16); gld16_sc1<1024>(uB, us, l16); gld4_sc1<-4>(uAp, us, l16); gld4_sc1<1020>(uBp, us, l16); }
#pragma unroll
    for (int r = 1; r < 5; ++r) dma16x4_sc1(ws + WS_K + kvo + 8192 * r, l16, kvo + 8192 * r);
    const float qk_ = fmaxf(fmaxf(*LP(float, R2 + 1024), *LP(float, R2 + 4096 + 1024)), fmaxf(*LP(float, R2 + 8192 + 1024), *LP(float, R2 + 12288 + 1024))) * *LP(float, R2 + 1028);
    const bool unsafe = (unsigned)__builtin_amdgcn_readfirstlane(__float_as_int(qk_)) > __float_as_uint(SAFE_BOUND);
    __builtin_amdgcn_s_setprio(2);
    WAITV(20);
    BAR();
    TS(9);
    for (int task = a; task < 6; task += 4) {
      unsigned pb = PART + 1024 * task + l16; PIN(pb);
      f32x4 v = *LP(f32x4, pb);
#pragma unroll
      for (int s = 1; s < 4; ++s) v += *LP(f32x4, pb + 6144 * s);
      pb += R1; PIN(pb);
#pragma unroll
      for (int s = 0; s < 4; ++s) v += *LP(f32x4, pb + 6144 * s);
      const int c = task >= 3, tau = task - 3 * c;
      const unsigned kb = KA + KA_STRIDE * c + 2 * 2055;
      const unsigned c01 = cvtpk(v[0], v[1]), c23 = cvtpk(v[2], v[3]);
      const u16 b0 = (u16)c01, b1 = (u16)(c01 >> 16), b2 = (u16)c23, b3 = (u16)(c23 >> 16);
      if (tau == 0) {
        const unsigned p = kb - 2 * (4 * g + 4 + 16 * r16);
        *LP(u16, p + 6) = b0; *LP(u16, p + 4) = b1; *LP(u16, p + 2) = b2; *LP(u16, p) = b3;
        if (lane == 63) *LP(u16, p - 2) = b3;
      } else if (tau == 1) {
        const bool up = g >= 2;
        const unsigned a0 = kb - 2 * ((up ? 516 + 256 * (g - 2) : 258 + 8 * g) + 16 * r16) - 8, st = up ? 8 : 4;
        const unsigned a1 = a0 - st, a2 = a1 - st, a3 = a2 - st;
        *LP(u16, a0 + 8) = b0; *LP(u16, a2 + 8) = b2;
        *LP(u16, a1 + 6) = b1; *LP(u16, a1 + 8) = b1; *LP(u16, a1 + 10) = b1;
        *LP(u16, a3 + 6) = b3; *LP(u16, a3 + 8) = b3; *LP(u16, a3 + 10) = b3;
        if (up) {
          *LP(u16, a0 + 6) = b0; *LP(u16, a0 + 10) = b0; *LP(u16, a2 + 6) = b2; *LP(u16, a2 + 10) = b2;
          *LP(u16, a1 + 4) = b1; *LP(u16, a1 + 12) = b1; *LP(u16, a3 + 4) = b3; *LP(u16, a3 + 12) = b3;
        }
        if (lane == 31) *LP(u16, a3 + 4) = b3;
        if (lane == 63) { *LP(u16, a3 + 2) = b3; *LP(u16, a3) = b3; }
      } else if ((g >> 1) == c) {
        const unsigned p = kb - 2 * (1032 + 512 * (g & 1) + 16 * r16) - 536;
        const unsigned d0 = cvtpk(v[0], v[0]), d1 = cvtpk(v[1], v[1]), d2 = cvtpk(v[2], v[2]), d3 = cvtpk(v[3], v[3]);
        *LP(u32x2, p + 536 - 6) = (u32x2){d0, d0}; *LP(unsigned, p + 536 + 2) = d0; *LP(u16, p + 536 + 6) = b0;
        *LP(u32x2, p + 520 - 6) = (u32x2){d1, d1}; *LP(u32x2, p + 520 + 2) = (u32x2){d1, d1}; *LP(u16, p + 520 - 8) = b1;
        *LP(u32x2, p + 24 - 6) = (u32x2){d2, d2}; *LP(unsigned, p + 24 + 2) = d2; *LP(u16, p + 24 + 6) = b2;
        *LP(u32x2, p + 8 - 6) = (u32x2){d3, d3}; *LP(u32x2, p + 8 + 2) = (u32x2){d3, d3}; *LP(u16, p) = b3;
      }
    }
    if (a >= 2) {
      const unsigned kb = KA + KA_STRIDE * (a & 1) + 2 * 2055;
      if (lane < 60) *LP(u32x2, kb + 2 + 8 * lane) = (u32x2){0, 0};
      if (lane == 60) *LP(u16, kb) = (u16)cvtpk(k0, 0.f);
      if (lane < 4) *LP(u32x4, UC + UC_STRIDE * (2 * (a & 1) + (lane >> 1)) + 16 * (lane & 1)) = (u32x4){0, 0, 0, 0};
    }
    asm volatile("s_waitcnt vmcnt(16)" : "+v"(uA), "+v"(uB), "+v"(uAp), "+v"(uBp) :: "memory");
    {
      if (lane == 0 && (a & 1) == 0) uAp = 0;
      const unsigned ub = UC + UC_STRIDE * 2 * ch + 32 + (a & 1) * 2048 + l16;
      *LP(u32x4, ub) = uA; *LP(u32x4, ub + 1024) = uB;
      *LP(u32x4, ub + UC_STRIDE) = (u32x4){__builtin_amdgcn_alignbit(uA[0], uAp, 16), __builtin_amdgcn_alignbit(uA[1], uA[0], 16), __builtin_amdgcn_alignbit(uA[2], uA[1], 16), __builtin_amdgcn_alignbit(uA[3], uA[2], 16)};
      *LP(u32x4, ub + UC_STRIDE + 1024) = (u32x4){__builtin_amdgcn_alignbit(uB[0], uBp, 16), __builtin_amdgcn_alignbit(uB[1], uB[0], 16), __builtin_amdgcn_alignbit(uB[2], uB[1], 16), __builtin_amdgcn_alignbit(uB[3], uB[2], 16)};
    }
    WAITV(0);
    TS(10);
    BAR();
#pragma unroll
    for (int r = 5; r < 8; ++r) dma16x4_sc1(ws + WS_K + kvo + 8192 * r, l16, kvo + 8192 * r);
    if (unsafe) { WAITV(0); BAR(); }
    {
      const int cs = a & 1;
      unsigned ba = KA + KA_STRIDE * ch + 2 * (2040 - 16 * r16 + 8 * g) - 3584;
      unsigned aa = UC + UC_STRIDE * (2 * ch + 1 - (r16 & 1)) + 2 * (2 + (r16 & 14) + 8 * g);
      f32x4 y0 = {0, 0, 0, 0}, y1 = y0, y2 = y0, y3 = y0;
      for (int rr = 0; rr < 8; ++rr) {
        bf16x8 A[8], B[8];
#pragma unroll
        for (int i = 0; i < 8; ++i) {
          const LAS unsigned* p = LP(unsigned, aa + 1024 * (i >> 1)) + 128 * (i & 1);
          A[i] = AS8(((u32x4){p[0], p[1], p[2], p[3]}));
          B[i] = LP(bf16x8, ba)[32 * (7 - i)];
        }
        if (cs == 0) {
          y0 = MFMA16(A[0], B[0], y0);
#pragma unroll
          for (int t = 0; t <= 3; ++t) y1 = MFMA16(A[t], B[3 - t], y1);
#pragma unroll
          for (int t = 0; t <= 4; ++t) y2 = MFMA16(A[t], B[4 - t], y2);
#pragma unroll
          for (int t = 0; t <= 7; ++t) y3 = MFMA16(A[t], B[7 - t], y3);
        } else {
#pragma unroll
          for (int t = 0; t <= 1; ++t) y0 = MFMA16(A[t], B[1 - t], y0);
#pragma unroll
          for (int t = 0; t <= 2; ++t) y1 = MFMA16(A[t], B[2 - t], y1);
#pragma unroll
          for (int t = 0; t <= 5; ++t) y2 = MFMA16(A[t], B[5 - t], y2);
#pragma unroll
          for (int t = 0; t <= 6; ++t) y3 = MFMA16(A[t], B[6 - t], y3);
        }
        ba += 64; aa += 64;
      }
      char* yb = ws + WS_YT + (ch0 + ch) * 4096; const unsigned yo = 32 * r16 + 8 * g;
      gst8_sc1(yb, yo + 512 * (cs ? 1 : 0), (u32x2){min(cvtpk(y0[0], y0[1]), SENT - 1), min(cvtpk(y0[2], y0[3]), SENT - 1)});
      gst8_sc1(yb, yo + 512 * (cs ? 2 : 3), (u32x2){min(cvtpk(y1[0], y1[1]), SENT - 1), min(cvtpk(y1[2], y1[3]), SENT - 1)});
      gst8_sc1(yb, yo + 512 * (cs ? 5 : 4), (u32x2){min(cvtpk(y2[0], y2[1]), SENT - 1), min(cvtpk(y2[2], y2[3]), SENT - 1)});
      gst8_sc1(yb, yo + 512 * (cs ? 6 : 7), (u32x2){min(cvtpk(y3[0], y3[1]), SENT - 1), min(cvtpk(y3[2], y3[3]), SENT - 1)});
    }
    TS(11);
    WAITV(0);
    BAR();
    TS(12);
#pragma unroll
    for (int j = 0; j < 4; ++j)
      dma16x4v(wo + (128 * a + 32 * j) * 512 + 64 * hg, wvo, wvo + 7168, wvo + 2 * 7168, wvo + 3 * 7168, (a >> 1) * 65536 + (a & 1) * 16384 + 4096 * j);
    {
      u32x4 u0, u1, d0, d1, ya, yb;
      const unsigned uo = (row0 + 16 * a + r16) * 1024 + 128 * hg + 16 * g;
      gld16<0>(u0, u, uo); gld16<64>(u1, u, uo); gld16<0>(d0, Dsk, 128 * hg + 16 * g); gld16<64>(d1, Dsk, 128 * hg + 16 * g);
      const char* ys = ws + WS_YT + (64 * hg) * 4096 + (row0 + 16 * a) * 2; const unsigned yo = (lane >> 1) * 4096 + 16 * (lane & 1);
      _Pragma("unroll 1") for (unsigned spins = 0; spins < (1u << 20); ++spins) {
        gld16_sc1<0>(ya, ys, yo); gld16_sc1<0>(yb, ys + 32 * 4096, yo);
        asm volatile("s_waitcnt vmcnt(0)" : "+v"(ya), "+v"(yb) :: "memory");
        if (!__any(max(max4(ya), max4(yb)) == SENT)) break;
        __builtin_amdgcn_s_sleep(4);
      }
      asm volatile("" : "+v"(u0), "+v"(u1), "+v"(d0), "+v"(d1) :: "memory");
      const unsigned sc = R2 + 2048 * a;
      *LP(u32x4, sc + l16) = ya; *LP(u32x4, sc + 1024 + l16) = yb;
      const unsigned ta = sc + 256 * g + 8 * r16;
      const u32x4 t0 = __builtin_bit_cast(u32x4, trd2(ta, ta + 128)), t1 = __builtin_bit_cast(u32x4, trd2(ta + 1024, ta + 1152));
      u32x4 o0, o1;
#pragma unroll
      for (int i = 0; i < 4; ++i) {
        const unsigned s0 = cvtpk(bflo(u0[i]) * bflo(d0[i]), bfhi(u0[i]) * bfhi(d0[i])), s1 = cvtpk(bflo(u1[i]) * bflo(d1[i]), bfhi(u1[i]) * bfhi(d1[i]));
        o0[i] = cvtpk(bflo(t0[i]) + bflo(s0), bfhi(t0[i]) + bfhi(s0)); o1[i] = cvtpk(bflo(t1[i]) + bflo(s1), bfhi(t1[i]) + bfhi(s1));
      }
      const unsigned oo = (row0 + 16 * a + r16) * 2048 + 128 * hg + 16 * g;
      gst16(out, oo, o0); gst16(out, oo + 64, o1);
    }
    TS(13);
    WAITV(0);
    TS_FLUSH();
    return;
  }

  const unsigned slice = R2 + 4096 * w;
  u32x4 xk0, xk1;
  { const unsigned vo = (row0 + 8 * hg + (r16 & 7)) * 1024 + 128 * w + 16 * g;
    gld16<0>(xk0, x, vo); gld16<64>(xk1, x, vo); }
  dma16x2(wk + 1024 * w, l16, slice); dma16x2(wv + 1024 * w, l16, slice + 2048);
  unsigned p_ar, p_ai, p_br, p_bi, p_cr, p_ci, p_dt;
  { const unsigned t = tid & 255, d = ch0 + (t >> 7), vo = (d * 128 + (t & 127)) * 2;
    gld2(p_ar, Are, vo); gld2(p_ai, Aim, vo); gld2(p_br, Bre, vo); gld2(p_bi, Bim, vo); gld2(p_cr, Cre, vo); gld2(p_ci, Cim, vo); gld2(p_dt, logdt, d * 2); }
  asm volatile("s_waitcnt vmcnt(7)" : "+v"(xk0), "+v"(xk1) :: "memory");
  TS(2);
  {
    const unsigned ta = slice + 256 * g + 8 * r16;
    bf16x8 wk0 = trd2(ta, ta + 128), wk1 = trd2(ta + 1024, ta + 1152), wv0 = trd2(ta + 2048, ta + 2176), wv1 = trd2(ta + 3072, ta + 3200);
    f32x4 dk = {0, 0, 0, 0}, dv = {0, 0, 0, 0};
    dk = MFMA16(wk0, AS8(xk0), dk); dv = MFMA16(AS8(xk0), wv0, dv);
    dk = MFMA16(wk1, AS8(xk1), dk); dv = MFMA16(AS8(xk1), wv1, dv);
    PIN(dk); PIN(dv);
    *LP(f32x4, slice + l16) = dk; *LP(f32x4, slice + 1024 + l16) = dv;
  }
  asm volatile("s_waitcnt vmcnt(0)" : "+v"(p_ar), "+v"(p_ai), "+v"(p_br), "+v"(p_bi), "+v"(p_cr), "+v"(p_ci), "+v"(p_dt) :: "memory");
  if (w < 4) {
    const float dt = bflo(cvtpk(expf(bflo(p_dt)), 0.f));
    const float al = (dt * bflo(p_ar)) * 1.4426950408889634f;
    float be = (dt * bflo(p_ai)) * 0.15915494309189535f; be -= __builtin_rintf(be);
    const float br = dt * bflo(p_br), bi = dt * bflo(p_bi), cr = bflo(p_cr), ci = bflo(p_ci);
    const float xr = cr * br - ci * bi, xi = cr * bi + ci * br;
    *LP(f32x4, slice + 2048 + l16) = (f32x4){al, be, xr, xi};
    const float s = col_sum(row_sum(xr));
    if (lane == 0) *LP(float, slice + 3072) = s;
  }
  BAR();
  TS(3);
  f32x4 kacc[6];
  {
    const float eB = (float)(16 * r16), e1 = (float)(r16 + 1);
    const float e2 = (float)(r16 < 8 ? 258 + 2 * r16 : 516 + 256 * ((r16 - 8) >> 2) + 4 * (r16 & 3));
    const float e3 = (float)(1032 + 256 * ((r16 & 7) >> 1) + 8 * (r16 & 1));
    unsigned pa = R2 + 4096 * (w >> 2) + 2048 + 16 * ((16 * w + 4 * g) & 63); PIN(pa);
    u32x4 fb[2], f1[2], f2[2], f3;
#pragma unroll
    for (int c = 0; c < 2; ++c)
#pragma unroll
      for (int i = 0; i < 4; ++i) {
        const f32x4 P = *LP(f32x4, pa + 8192 * c + 16 * i);
        const f32x2 ab = {P[0], P[1]};
        const f32x2 wv_ = zpow<false>(ab, eB);
        fb[c][i] = cvtpk(P[2] * wv_.x - P[3] * wv_.y, P[2] * wv_.y + P[3] * wv_.x);
        const f32x2 a1 = zpow<false>(ab, e1); f1[c][i] = cvtpk(a1.x, -a1.y);
        const f32x2 a2 = zpow<true>(ab, e2); f2[c][i] = cvtpk(a2.x, -a2.y);
      }
    pa += 8192 * (r16 >> 3);
#pragma unroll
    for (int i = 0; i < 4; ++i) {
      const f32x2 ab = *LP(f32x2, pa + 16 * i);
      const f32x2 a3 = zpow<true>(ab, e3); f3[i] = cvtpk(a3.x, -a3.y);
    }
    const f32x4 z4 = {0, 0, 0, 0};
#pragma unroll
    for (int c = 0; c < 2; ++c) {
      kacc[3 * c + 0] = MFMA16(AS8(f1[c]), AS8(fb[c]), z4);
      kacc[3 * c + 1] = MFMA16(AS8(f2[c]), AS8(fb[c]), z4);
      kacc[3 * c + 2] = MFMA16(AS8(f3), AS8(fb[c]), z4);
    }
#pragma unroll
    for (int i = 0; i < 6; ++i) PIN(kacc[i]);
  }
  TS(5);
  BAR();
  TS(6);
  if (w < 4) {
    const int rh = w >> 1, hp = w & 1;
    const unsigned xa = R0 + 1024 * (32 * rh + r32) + 16 * (h ^ r16);
    const int q = r16 >> 2, p = lane & 3;
    unsigned wa = R1 + 128 * (8 * h + q) + 16 * ((4 * hp + 2 * (g & 1) + (p >> 1)) ^ ((q >> 1) << 2)) + 8 * (p & 1); PIN(wa);
    f32x16 acc = {0};
#pragma unroll
    for (int ks = 0; ks < 32; ++ks) {
      const bf16x8 b = *LP(bf16x8, (xa ^ ((ks & 7) << 5)) + 256 * (ks >> 3));
      const bf16x8 a = trd2(wa + 2048 * ks, wa + 2048 * ks + 512);
      acc = MFMA32(a, b, acc);
    }
    acc *= QSCALE;
    float n0 = 0, n1 = 0;
#pragma unroll
    for (int i = 0; i < 8; ++i) { n0 += acc[i] * acc[i]; n1 += acc[8 + i] * acc[8 + i]; }
    float o; n0 = x32(n0, o); n0 += o; n1 = x32(n1, o); n1 += o;
    n0 = __builtin_amdgcn_sqrtf(n0); n1 = __builtin_amdgcn_sqrtf(n1);
    const unsigned qs = R2 + 4096 * (4 + w);
    *LP(u32x4, qs + 2048 + l16) = (u32x4){cvtpk(acc[0], acc[1]), cvtpk(acc[2], acc[3]), cvtpk(acc[4], acc[5]), cvtpk(acc[6], acc[7])};
    *LP(u32x4, qs + 3072 + l16) = (u32x4){cvtpk(acc[8], acc[9]), cvtpk(acc[10], acc[11]), cvtpk(acc[12], acc[13]), cvtpk(acc[14], acc[15])};
    *LP(float, slice + 4 * lane) = n0; *LP(float, slice + 256 + 4 * lane) = n1;
    const float nm = col_max(row_max(fmaxf(n0, n1)));
    if (lane == 0) *LP(float, slice + 1024) = nm;
  } else if (w == 4 && lane < 12) {
    const unsigned j = lane >> 2, o1 = j ? 0u : 0x3f803f80u;
    *LP(u32x4, ONES + 512 * (lane & 3) + (j == 2 ? 64 : 16 * j)) = (u32x4){o1, o1, o1, o1};
  }
  TS(7);
  BAR();
  TS(8);
  const int qt = w & 3, hs = w >> 2;
  const bf16x8 qf = *LP(bf16x8, R2 + 4096 * (4 + qt) + 2048 + 1024 * hs + l16);
  const float kmax = *LP(float, R2 + 1028);
  float sh = *LP(float, R2 + 4096 * qt + 256 * hs + 4 * lane) * kmax;
  const float qmax = fmaxf(fmaxf(*LP(float, R2 + 1024), *LP(float, R2 + 4096 + 1024)), fmaxf(*LP(float, R2 + 8192 + 1024), *LP(float, R2 + 12288 + 1024)));
  const bool unsafe = (unsigned)__builtin_amdgcn_readfirstlane(__float_as_int(qmax * kmax)) > __float_as_uint(SAFE_BOUND);
  {
    const unsigned pb = (w >> 2) * R1 + PART + (w & 3) * 6144 + l16;
#pragma unroll
    for (int i = 0; i < 6; ++i) *LP(f32x4, pb + 1024 * i) = kacc[i];
  }
  BAR();
  TS(9);
  if (unsafe) {
    BAR(); BAR();
    const f32x16 z = {0};
    float m = -INFINITY;
    for (int t = 0; t < 64; ++t) {
      const f32x16 s = MFMA32(*LP(bf16x8, R0 + 1024 * t + l16), qf, z);
#pragma unroll
      for (int i = 0; i < 16; ++i) m = fmaxf(m, s[i]);
    }
    float o; m = x32(m, o); sh = fmaxf(m, o);
  }
  f32x16 oacc = {0};
  {
    f32x16 mneg;
#pragma unroll
    for (int i = 0; i < 16; ++i) mneg[i] = -sh;
    PIN(mneg);
    const bool lo = (lane & 16) == 0;
    unsigned kp = R0 + l16;
    unsigned vp = lo ? R1 + 256 * h + 16 * r16 : ONES + (r32 == 16 ? 0 : (r32 >= 20 && r32 < 28) ? 64 : 16);
    const unsigned vinc = lo ? 2048 : 0;
    const int tb = unsafe ? -1 : 4;
    f32x16 sA, sB; u32x4 pA0, pA1, pB0 = {0, 0, 0, 0}, pB1;
    bf16x8 kf = *LP(bf16x8, kp), v0, v1;
    sA = MFMA32(kf, qf, mneg);
    kf = *LP(bf16x8, kp + 1024);
    v0 = *LP(bf16x8, vp); v1 = v0;
#pragma unroll
    for (int i = 0; i < 16; ++i) sB[i] = 0.f;
#define CV(P, j, S, i) P[j] = cvtpk(S[i], S[i + 1])
#define E4(S, i) do { S[i] = EX2(S[i]); S[i + 1] = EX2(S[i + 1]); S[i + 2] = EX2(S[i + 2]); S[i + 3] = EX2(S[i + 3]); \
                      asm volatile("" : "+v"(S[i]), "+v"(S[i + 1]), "+v"(S[i + 2]), "+v"(S[i + 3])); SBAR(); } while (0)
#define UNIT(S, P0, T, Q0, Q1, KOFF, VOFF) do { \
      E4(S, 0); CV(Q1, 0, T, 8); CV(Q1, 1, T, 10); CV(Q1, 2, T, 12); CV(Q1, 3, T, 14); PIN(Q1); SBAR(); \
      T = MFMA32(kf, qf, mneg); kf = *LP(bf16x8, kp + KOFF); SBAR(); E4(S, 4); \
      oacc = MFMA32(v0, AS8(Q0), oacc); v0 = *LP(bf16x8, vp + VOFF); SBAR(); E4(S, 8); \
      oacc = MFMA32(v1, AS8(Q1), oacc); v1 = *LP(bf16x8, vp + VOFF + 512); SBAR(); \
      CV(P0, 0, S, 0); CV(P0, 1, S, 2); CV(P0, 2, S, 4); CV(P0, 3, S, 6); PIN(P0); SBAR(); E4(S, 12); } while (0)
    for (int t = 0; t < 64; t += 4) {
      if (t == tb || t == 32) BAR();
      UNIT(sA, pA0, sB, pB0, pB1, 2048, 0);
      UNIT(sB, pB0, sA, pA0, pA1, 3072, 1024);
      vp += vinc;
      UNIT(sA, pA0, sB, pB0, pB1, 4096, 0);
      UNIT(sB, pB0, sA, pA0, pA1, 5120, 1024);
      kp += 4096; vp += vinc;
    }
    CV(pB1, 0, sB, 8); CV(pB1, 1, sB, 10); CV(pB1, 2, sB, 12); CV(pB1, 3, sB, 14);
    oacc = MFMA32(v0, AS8(pB0), oacc); oacc = MFMA32(v1, AS8(pB1), oacc);
  }
  TS(10);
  {
    const float rl = __builtin_amdgcn_rcpf(oacc[8]);
    float o; const float r0 = x32(rl, o);
    u32x4 of_ = {cvtpk(oacc[0] * r0, oacc[1] * r0), cvtpk(oacc[2] * r0, oacc[3] * r0), cvtpk(oacc[4] * r0, oacc[5] * r0), cvtpk(oacc[6] * r0, oacc[7] * r0)};
#pragma unroll
    for (int i = 0; i < 4; ++i) of_[i] = min(of_[i], SENT - 1);
    gst16_sc1<0>(ws + WS_O + ((rg * 2 + (qt >> 1)) * 32 + 4 * hg + 2 * (qt & 1) + hs) * 1024, l16, of_);
  }
  BAR();
  TS(11);
  {
    const int rh = w >> 2, kq = w & 3;
    const char* osrc = ws + WS_O + ((rg * 2 + rh) * 32 + 8 * kq) * 1024;
    _Pragma("unroll 1") for (unsigned spins = 0; spins < (1u << 20); ++spins) {
      unsigned t; gld4_sc1<1020>(t, osrc, (lane & 7) * 1024);
      asm volatile("s_waitcnt vmcnt(0)" : "+v"(t) :: "memory");
      if (!__any(t == SENT)) break;
    }
    TS(12);
    u32x4 of[8];
    _Pragma("unroll 1") for (unsigned spins = 0; spins < (1u << 20); ++spins) {
      gld16_sc1<0>(of[0], osrc, l16); gld16_sc1<1024>(of[1], osrc, l16); gld16_sc1<2048>(of[2], osrc, l16); gld16_sc1<3072>(of[3], osrc, l16);
      gld16_sc1<0>(of[4], osrc + 4096, l16); gld16_sc1<1024>(of[5], osrc + 4096, l16); gld16_sc1<2048>(of[6], osrc + 4096, l16); gld16_sc1<3072>(of[7], osrc + 4096, l16);
      asm volatile("s_waitcnt vmcnt(0)" : "+v"(of[0]), "+v"(of[1]), "+v"(of[2]), "+v"(of[3]), "+v"(of[4]), "+v"(of[5]), "+v"(of[6]), "+v"(of[7]) :: "memory");
      unsigned mx = 0;
#pragma unroll
      for (int i = 0; i < 8; ++i) mx = max(mx, max4(of[i]));
      if (!__any(mx == SENT)) break;
    }
    TS(13);
    const int q = r16 >> 2, p = lane & 3;
    unsigned wa = (kq >> 1) * R1 + (kq & 1) * 16384 + 128 * (4 * h + q) + 16 * ((2 * (g & 1) + (p >> 1)) ^ ((q >> 1) << 2)) + 8 * (p & 1); PIN(wa);
    const unsigned wb = wa ^ 64;
    f32x16 c0 = {0}, c1 = {0};
#pragma unroll
    for (int ks = 0; ks < 8; ++ks) {
      c0 = MFMA32(trd2(wa + 2048 * ks, wa + 2048 * ks + 1024), AS8(of[ks]), c0);
      c1 = MFMA32(trd2(wb + 2048 * ks, wb + 2048 * ks + 1024), AS8(of[ks]), c1);
    }
    const unsigned rbase = 32768 + rh * R1;
    const f32x4 G[8] = {{c0[0], c0[1], c0[2], c0[3]}, {c0[4], c0[5], c0[6], c0[7]}, {c0[8], c0[9], c0[10], c0[11]}, {c0[12], c0[13], c0[14], c0[15]},
                        {c1[0], c1[1], c1[2], c1[3]}, {c1[4], c1[5], c1[6], c1[7]}, {c1[8], c1[9], c1[10], c1[11]}, {c1[12], c1[13], c1[14], c1[15]}};
    unsigned wr = rbase + 2048 * kq + l16; PIN(wr);
#pragma unroll
    for (int j = 0; j < 4; ++j) { *LP(f32x4, wr + 8192 * j) = G[2 * j]; *LP(f32x4, wr + 8192 * j + 1024) = G[2 * j + 1]; }
    BAR();
    unsigned rd = rbase + 8192 * kq + l16; PIN(rd);
    f32x4 a = *LP(f32x4, rd), b = *LP(f32x4, rd + 1024);
#pragma unroll
    for (int s = 1; s < 4; ++s) { a += *LP(f32x4, rd + 2048 * s); b += *LP(f32x4, rd + 2048 * s + 1024); }
    auto s0 = __builtin_amdgcn_permlane32_swap(cvtpk(a[0], a[1]), cvtpk(b[0], b[1]), false, false);
    auto s1 = __builtin_amdgcn_permlane32_swap(cvtpk(a[2], a[3]), cvtpk(b[2], b[3]), false, false);
    gst16(out, (row0 + 32 * rh + r32) * 2048 + 1024 + 128 * hg + 32 * kq + 16 * h, (u32x4){s0[0], s1[0], s0[1], s1[1]});
  }
  TS_FLUSH();
}

extern "C" void kernel_launch(void* const* d_in, const int* in_sizes, int n_in, void* d_out, int out_size, void* d_ws, size_t ws_size, hipStream_t stream) {
  (void)in_sizes; (void)n_in; (void)out_size; (void)ws_size;
  (void)hipFuncSetAttribute((const void*)fused_kernel, hipFuncAttributeMaxDynamicSharedMemorySize, LDS_BYTES);
  fused_kernel<<<256, 768, LDS_BYTES, stream>>>((const u16*)d_in[0], (const u16*)d_in[1], (const u16*)d_in[2], (const u16*)d_in[3], (const u16*)d_in[4],
      (const u16*)d_in[5], (const u16*)d_in[6], (const u16*)d_in[7], (const u16*)d_in[8], (const u16*)d_in[9], (const u16*)d_in[10], (const u16*)d_in[11],
      (const u16*)d_in[12], (const u16*)d_in[13], (u16*)d_out, (char*)d_ws);
}
```

```cpp
#include <hip/hip_runtime.h>
#include <hip/hip_bf16.h>
#pragma clang diagnostic ignored "-Wint-to-pointer-cast"

typedef unsigned short u16;
typedef unsigned long long u64;
typedef __attribute__((ext_vector_type(8))) short bf16x8;
typedef __attribute__((ext_vector_type(4))) short s16x4;
typedef __attribute__((ext_vector_type(16))) float f32x16;
typedef __attribute__((ext_vector_type(4))) float f32x4;
typedef __attribute__((ext_vector_type(2))) float f32x2;
typedef __attribute__((ext_vector_type(4))) unsigned u32x4;
typedef __attribute__((ext_vector_type(2))) unsigned u32x2;
typedef __attribute__((ext_vector_type(2))) __bf16 bf16x2;
#define LAS __attribute__((address_space(3)))
#define LP(T, off) ((LAS T*)(unsigned)(off))
#define SBAR() __builtin_amdgcn_sched_barrier(0)
#define PIN(x) asm volatile("" : "+v"(x))
#define MFMA32(a, b, c) __builtin_amdgcn_mfma_f32_32x32x16_bf16(a, b, c, 0, 0, 0)
#define MFMA16(a, b, c) __builtin_amdgcn_mfma_f32_16x16x32_bf16(a, b, c, 0, 0, 0)
#define AS8(x) __builtin_bit_cast(bf16x8, x)
#define EX2(v) __builtin_amdgcn_exp2f(v)
#define BAR() asm volatile("s_waitcnt lgkmcnt(0)\n\ts_barrier" ::: "memory")
#define WAITV(n) asm volatile("s_waitcnt vmcnt(" #n ")" ::: "memory")

#define STAMP 0
#define TS(i) do {} while (0)
#define TSR(i) do {} while (0)
#define TS_FLUSH() do {} while (0)

constexpr unsigned WS_EPOCH = 0, WS_FLAG = 1024, WS_KMAX = 4096, WS_K = 8192, WS_V = WS_K + 65536, WS_O = 1u << 18, WS_UT = WS_O + (2u << 20), WS_YT = WS_UT + (2u << 20), WS_DBG = 8u << 20;
constexpr unsigned R0 = 0, R1 = 65536, R2 = 131072, LDS_BYTES = 163840;
constexpr unsigned KA = R2, KA_STRIDE = 4608;
constexpr unsigned UC = R2 + 9216, UC_STRIDE = 4160;
constexpr unsigned ONES = R2 + 28672;
constexpr unsigned PART = 40960;
constexpr float QSCALE = 0.25f * 1.4426950408889634f;
constexpr float SAFE_BOUND = 40.f;
constexpr unsigned SENT = 0xffffffffu;
constexpr int CODE_KB = 15;

__device__ __forceinline__ unsigned cvtpk(float lo, float hi) { f32x2 v = {lo, hi}; return __builtin_bit_cast(unsigned, __builtin_convertvector(v, bf16x2)); }
__device__ __forceinline__ float bflo(unsigned x) { return __uint_as_float(x << 16); }
__device__ __forceinline__ float bfhi(unsigned x) { return __uint_as_float(x & 0xffff0000u); }
template <int C> __device__ __forceinline__ float dpp(float v) { return __int_as_float(__builtin_amdgcn_update_dpp(0, __float_as_int(v), C, 0xf, 0xf, true)); }
__device__ __forceinline__ float x16(float v, float& o) { auto r = __builtin_amdgcn_permlane16_swap(__float_as_uint(v), __float_as_uint(v), false, false); o = __uint_as_float(r[1]); return __uint_as_float(r[0]); }
__device__ __forceinline__ float x32(float v, float& o) { auto r = __builtin_amdgcn_permlane32_swap(__float_as_uint(v), __float_as_uint(v), false, false); o = __uint_as_float(r[1]); return __uint_as_float(r[0]); }
__device__ __forceinline__ float row_sum(float v) { v += dpp<0x128>(v); v += dpp<0x124>(v); v += dpp<0x122>(v); v += dpp<0x121>(v); return v; }
__device__ __forceinline__ float row_max(float v) { v = fmaxf(v, dpp<0x128>(v)); v = fmaxf(v, dpp<0x124>(v)); v = fmaxf(v, dpp<0x122>(v)); v = fmaxf(v, dpp<0x121>(v)); return v; }
__device__ __forceinline__ float col_sum(float v) { float o; v = x16(v, o); v += o; v = x32(v, o); return v + o; }
__device__ __forceinline__ float col_max(float v) { float o; v = x16(v, o); v = fmaxf(v, o); v = x32(v, o); return fmaxf(v, o); }

__device__ __forceinline__ void dma16(const void* base, unsigned voff, unsigned ldst) {
  asm volatile("s_mov_b32 m0, %2\n\ts_nop 0\n\tglobal_load_lds_dwordx4 %0, %1" :: "v"(voff), "s"(base), "s"(ldst) : "memory");
}
__device__ __forceinline__ void dma16x2(const void* base, unsigned voff, unsigned ldst) {
  asm volatile("s_mov_b32 m0, %2\n\ts_nop 0\n\tglobal_load_lds_dwordx4 %0, %1\n\tglobal_load_lds_dwordx4 %0, %1 offset:1024" :: "v"(voff), "s"(base), "s"(ldst) : "memory");
}
__device__ __forceinline__ void dma16x4v(const void* base, unsigned v0, unsigned v1, unsigned v2, unsigned v3, unsigned ldst) {
  asm volatile("s_mov_b32 m0, %5\n\ts_nop 0\n\tglobal_load_lds_dwordx4 %0, %4\n\tglobal_load_lds_dwordx4 %1, %4 offset:1024\n\t"
               "global_load_lds_dwordx4 %2, %4 offset:2048\n\tglobal_load_lds_dwordx4 %3, %4 offset:3072" :: "v"(v0), "v"(v1), "v"(v2), "v"(v3), "s"(base), "s"(ldst) : "memory");
}
__device__ __forceinline__ void dma16x4_sc1(const void* base, unsigned voff, unsigned ldst) {
  asm volatile("s_mov_b32 m0, %2\n\ts_nop 0\n\tglobal_load_lds_dwordx4 %0, %1 sc1\n\tglobal_load_lds_dwordx4 %0, %1 offset:1024 sc1\n\t"
               "global_load_lds_dwordx4 %0, %1 offset:2048 sc1\n\tglobal_load_lds_dwordx4 %0, %1 offset:3072 sc1" :: "v"(voff), "s"(base), "s"(ldst) : "memory");
}
template <int OFF> __device__ __forceinline__ void gld16(u32x4& d, const void* base, unsigned voff) { asm volatile("global_load_dwordx4 %0, %1, %2 offset:%3" : "=v"(d) : "v"(voff), "s"(base), "i"(OFF) : "memory"); }
template <int OFF> __device__ __forceinline__ void gld16_sc1(u32x4& d, const void* base, unsigned voff) { asm volatile("global_load_dwordx4 %0, %1, %2 offset:%3 sc1" : "=v"(d) : "v"(voff), "s"(base), "i"(OFF) : "memory"); }
template <int OFF> __device__ __forceinline__ void gld4_sc1(unsigned& d, const void* base, unsigned voff) { asm volatile("global_load_dword %0, %1, %2 offset:%3 sc1" : "=v"(d) : "v"(voff), "s"(base), "i"(OFF) : "memory"); }
__device__ __forceinline__ void gld2(unsigned& d, const void* base, unsigned voff) { asm volatile("global_load_ushort %0, %1, %2" : "=v"(d) : "v"(voff), "s"(base) : "memory"); }
__device__ __forceinline__ void gld8_sc1(u32x2& d, const void* base, unsigned voff) { asm volatile("global_load_dwordx2 %0, %1, %2 sc1" : "=v"(d) : "v"(voff), "s"(base) : "memory"); }
template <int OFF> __device__ __forceinline__ void gst16_sc1(void* base, unsigned voff, u32x4 d) { asm volatile("global_store_dwordx4 %0, %1, %2 offset:%3 sc1\n\ts_nop 1" :: "v"(voff), "v"(d), "s"(base), "i"(OFF) : "memory"); }
__device__ __forceinline__ void gst16(void* base, unsigned voff, u32x4 d) { asm volatile("global_store_dwordx4 %0, %1, %2\n\ts_nop 1" :: "v"(voff), "v"(d), "s"(base) : "memory"); }
__device__ __forceinline__ void gst8_sc1(void* base, unsigned voff, u32x2 d) { asm volatile("global_store_dwordx2 %0, %1, %2 sc1" :: "v"(voff), "v"(d), "s"(base) : "memory"); }
__device__ __forceinline__ void gst4_sc1(void* base, unsigned voff, unsigned d) { asm volatile("global_store_dword %0, %1, %2 sc1" :: "v"(voff), "v"(d), "s"(base) : "memory"); }
__device__ __forceinline__ s16x4 trd(unsigned a) { return __builtin_bit_cast(s16x4, __builtin_amdgcn_ds_read_tr16_b64_v4i16(LP(s16x4, a))); }
__device__ __forceinline__ bf16x8 trd2(unsigned a0, unsigned a1) { s16x4 x = trd(a0), y = trd(a1); return (bf16x8){x[0], x[1], x[2], x[3], y[0], y[1], y[2], y[3]}; }
__device__ __forceinline__ unsigned max4(u32x4 v) { return max(max(v[0], v[1]), max(v[2], v[3])); }

template <bool FR> __device__ __forceinline__ f32x2 zpow(f32x2 ab, float e) {
  f32x2 t = ab * e;
  float m = EX2(t.x);
  float a = FR ? __builtin_amdgcn_fractf(t.y) : t.y;
  f32x2 cs = {__builtin_amdgcn_cosf(a), __builtin_amdgcn_sinf(a)};
  return cs * m;
}

__global__ void __launch_bounds__(768, 3)
fused_kernel(const u16* __restrict__ u, const u16* __restrict__ Are, const u16* __restrict__ Aim, const u16* __restrict__ Bre, const u16* __restrict__ Bim,
             const u16* __restrict__ Cre, const u16* __restrict__ Cim, const u16* __restrict__ logdt, const u16* __restrict__ Dsk,
             const u16* __restrict__ x, const u16* __restrict__ wq, const u16* __restrict__ wk, const u16* __restrict__ wv, const u16* __restrict__ wo,
             u16* __restrict__ out, char* __restrict__ ws) {
  u64 pc0; asm volatile("s_getpc_b64 %0" : "=s"(pc0));
  const int tid = threadIdx.x, lane = tid & 63;
  const int w = __builtin_amdgcn_readfirstlane(tid >> 6);
  const int bid = blockIdx.x, xcd = bid & 7, loc = bid >> 3;
  const int rg = xcd * 4 + (loc >> 3), hg = loc & 7, row0 = rg * 64, ch0 = xcd * 64 + loc * 2;
  const int r16 = lane & 15, g = lane >> 4, r32 = lane & 31, h = lane >> 5;
  const unsigned l16 = 16 * lane;
  const unsigned wvo = (lane >> 3) * 1024 + 16 * ((lane & 7) ^ (((lane >> 4) & 1) << 2));
  TSR(0); TS(1);

  if (w >= 8) {
    const int a = w - 8;
    u64 tag = 0;
    if (a == 0) {
      u32x2 ep; gld8_sc1(ep, ws + WS_EPOCH, 0);
      const unsigned ust = R0 + 1024 * ((8 * hg + 56) & 63);
      const u16* ub = u + row0 * 512 + 64 * hg;
      const unsigned uvo = (lane >> 3) * 1024 + 16 * (lane & 7);
      dma16x4v(ub, uvo, uvo + 7168, uvo + 2 * 7168, uvo + 3 * 7168, ust); dma16x4v(ub + 16384, uvo, uvo + 7168, uvo + 2 * 7168, uvo + 3 * 7168, ust + 4096);
      const u32x4 sent = {SENT, SENT, SENT, SENT};
      char* os = ws + WS_O + (rg * 64 + 4 * hg) * 1024; char* ys = ws + WS_YT + ch0 * 4096;
      gst16_sc1<0>(os, l16, sent); gst16_sc1<1024>(os, l16, sent); gst16_sc1<2048>(os, l16, sent); gst16_sc1<3072>(os, l16, sent);
      gst16_sc1<0>(os + 32768, l16, sent); gst16_sc1<1024>(os + 32768, l16, sent); gst16_sc1<2048>(os + 32768, l16, sent); gst16_sc1<3072>(os + 32768, l16, sent);
      gst16_sc1<0>(ys, l16, sent); gst16_sc1<1024>(ys, l16, sent); gst16_sc1<2048>(ys, l16, sent); gst16_sc1<3072>(ys, l16, sent);
      gst16_sc1<0>(ys + 4096, l16, sent); gst16_sc1<1024>(ys + 4096, l16, sent); gst16_sc1<2048>(ys + 4096, l16, sent); gst16_sc1<3072>(ys + 4096, l16, sent);
      asm volatile("s_waitcnt vmcnt(16)" : "+v"(ep) :: "memory");
      tag = (((u64)(unsigned)__builtin_amdgcn_readfirstlane(ep[1]) << 32) | (unsigned)__builtin_amdgcn_readfirstlane(ep[0])) + 1;
      TS(2);
      unsigned ta = ust + 128 * (8 * g + (r16 >> 2)) + 8 * (lane & 3); PIN(ta);
      char* ut = ws + WS_UT + (64 * hg) * 4096 + row0 * 2;
      const unsigned so = r16 * 4096 + 16 * g;
#pragma unroll
      for (int j = 0; j < 8; ++j) {
        const bf16x8 v = trd2(ta + 4096 * (j >> 2) + 32 * (j & 3), ta + 4096 * (j >> 2) + 32 * (j & 3) + 512);
        if (j >> 2) gst16_sc1<64>(ut + 65536 * (j & 3), so, __builtin_bit_cast(u32x4, v)); else gst16_sc1<0>(ut + 65536 * (j & 3), so, __builtin_bit_cast(u32x4, v));
      }
    } else if (a == 3) {
      if (loc < CODE_KB) {
        unsigned dummy; asm volatile("global_load_dword %0, %1, %2\n\ts_waitcnt vmcnt(0)" : "=v"(dummy) : "v"(loc * 1024 + l16), "s"(pc0) : "memory");
      }
    }
    auto dma_x = [&](int i0, int i1) { _Pragma("unroll 1") for (int i = i0; i < i1; ++i) { const int r = (8 * hg + i) & 63; dma16(x + (row0 + r) * 512, 16 * (lane ^ (r & 15)), R0 + 1024 * r); } };
    auto dma_wq = [&](int i0, int i1) { _Pragma("unroll 1") for (int i = i0; i < i1; ++i) { const int p = (16 * (loc >> 3) + i) & 63; dma16(wq + p * 4096 + 64 * hg, wvo, R1 + 1024 * p); } };
    dma_x(0, a == 1 ? 8 : 0); dma_wq(0, a == 2 ? 16 : 0);
    BAR();
    TS(3);
    if (a == 0) {
      unsigned rb = R2 + l16; PIN(rb);
      f32x4 dk = *LP(f32x4, rb), dv = *LP(f32x4, rb + 1024);
#pragma unroll
      for (int s = 1; s < 8; ++s) { dk += *LP(f32x4, rb + 4096 * s); dv += *LP(f32x4, rb + 4096 * s + 1024); }
      const int kk0 = row0 + 8 * hg, t = kk0 >> 5, c = (kk0 >> 3) & 3;
      const u32x2 kp = {cvtpk(dk[0], dk[1]), cvtpk(dk[2], dk[3])}, vp = {cvtpk(dv[0], dv[1]), cvtpk(dv[2], dv[3])};
      if (r16 < 8) gst8_sc1(ws, WS_K + t * 1024 + ((g & 1) * 32 + 8 * c + r16) * 16 + 8 * (g >> 1), kp);
      if (g < 2) gst8_sc1(ws, WS_V + t * 1024 + 512 * (c >> 1) + 256 * g + 16 * r16 + 8 * (c & 1), vp);
      const float k2 = row_max(col_sum(dk[0] * dk[0] + dk[1] * dk[1] + dk[2] * dk[2] + dk[3] * dk[3]));
      if (lane == 0) gst4_sc1(ws, WS_KMAX + 4 * bid, __float_as_uint(k2));
      TS(4);
      WAITV(0);
      if (lane == 0) gst8_sc1(ws, WS_FLAG + 8 * bid, (u32x2){(unsigned)tag, (unsigned)(tag >> 32)});
    } else {
      dma_x(a == 1 ? 8 : 43, a == 1 ? 43 : a == 2 ? 0 : 64); dma_wq(a == 2 ? 16 : 43, a == 2 ? 43 : a == 1 ? 0 : 64);
      TS(4);
      WAITV(0);
    }
    TS(5);
    BAR();
    TS(6);
    const float k0 = *LP(float, R2 + 4096 * (2 * (a & 1)) + 3072) + *LP(float, R2 + 4096 * (2 * (a & 1) + 1) + 3072);
    if (a == 0) {
      _Pragma("unroll 1") for (unsigned spins = 0; spins < (1u << 20); ++spins) {
        u32x4 f0, f1; gld16_sc1<0>(f0, ws + WS_FLAG, 32 * lane); gld16_sc1<16>(f1, ws + WS_FLAG, 32 * lane);
        asm volatile("s_waitcnt vmcnt(0)" : "+v"(f0), "+v"(f1) :: "memory");
        const unsigned lo = (unsigned)tag, hi = (unsigned)(tag >> 32);
        const bool ok = f0[0] == lo && f0[1] == hi && f0[2] == lo && f0[3] == hi && f1[0] == lo && f1[1] == hi && f1[2] == lo && f1[3] == hi;
        if (__all(ok)) break;
        __builtin_amdgcn_s_sleep(1);
      }
      u32x4 km; gld16_sc1<0>(km, ws + WS_KMAX, l16);
      asm volatile("s_waitcnt vmcnt(0)" : "+v"(km) :: "memory");
      float m = fmaxf(fmaxf(__uint_as_float(km[0]), __uint_as_float(km[1])), fmaxf(__uint_as_float(km[2]), __uint_as_float(km[3])));
      m = __builtin_amdgcn_sqrtf(col_max(row_max(m))) * 1.01f;
      if (lane == 0) *LP(float, R2 + 1028) = m;
      if (bid == 0 && lane == 0) gst8_sc1(ws, WS_EPOCH, (u32x2){(unsigned)tag, (unsigned)(tag >> 32)});
      WAITV(0);
    }
    TS(7);
    BAR();
    TS(8);
    const unsigned kvo = (a & 1) * 65536 + (a >> 1) * 4096;
    dma16x4_sc1(ws + WS_K + kvo, l16, kvo);
    const int ch = a >> 1;
    u32x4 uA, uB; unsigned uAp, uBp;
    { const char* us = ws + WS_UT + (ch0 + ch) * 4096 + (a & 1) * 2048;
      gld16_sc1<0>(uA, us, l16); gld16_sc1<1024>(uB, us, l16); gld4_sc1<-4>(uAp, us, l16); gld4_sc1<1020>(uBp, us, l16); }
#pragma unroll
    for (int r = 1; r < 5; ++r) dma16x4_sc1(ws + WS_K + kvo + 8192 * r, l16, kvo + 8192 * r);
    const float qk_ = fmaxf(fmaxf(*LP(float, R2 + 1024), *LP(float, R2 + 4096 + 1024)), fmaxf(*LP(float, R2 + 8192 + 1024), *LP(float, R2 + 12288 + 1024))) * *LP(float, R2 + 1028);
    const bool unsafe = (unsigned)__builtin_amdgcn_readfirstlane(__float_as_int(qk_)) > __float_as_uint(SAFE_BOUND);
    __builtin_amdgcn_s_setprio(2);
    WAITV(20);
    BAR();
    TS(9);
    for (int task = a; task < 6; task += 4) {
      unsigned pb = PART + 1024 * task + l16; PIN(pb);
      f32x4 v = *LP(f32x4, pb);
#pragma unroll
      for (int s = 1; s < 4; ++s) v += *LP(f32x4, pb + 6144 * s);
      pb += R1; PIN(pb);
#pragma unroll
      for (int s = 0; s < 4; ++s) v += *LP(f32x4, pb + 6144 * s);
      const int c = task >= 3, tau = task - 3 * c;
      const unsigned kb = KA + KA_STRIDE * c + 2 * 2055;
      const unsigned c01 = cvtpk(v[0], v[1]), c23 = cvtpk(v[2], v[3]);
      const u16 b0 = (u16)c01, b1 = (u16)(c01 >> 16), b2 = (u16)c23, b3 = (u16)(c23 >> 16);
      if (tau == 0) {
        const unsigned p = kb - 2 * (4 * g + 4 + 16 * r16);
        *LP(u16, p + 6) = b0; *LP(u16, p + 4) = b1; *LP(u16, p + 2) = b2; *LP(u16, p) = b3;
        if (lane == 63) *LP(u16, p - 2) = b3;
      } else if (tau == 1) {
        const bool up = g >= 2;
        const unsigned a0 = kb - 2 * ((up ? 516 + 256 * (g - 2) : 258 + 8 * g) + 16 * r16) - 8, st = up ? 8 : 4;
        const unsigned a1 = a0 - st, a2 = a1 - st, a3 = a2 - st;
        *LP(u16, a0 + 8) = b0; *LP(u16, a2 + 8) = b2;
        *LP(u16, a1 + 6) = b1; *LP(u16, a1 + 8) = b1; *LP(u16, a1 + 10) = b1;
        *LP(u16, a3 + 6) = b3; *LP(u16, a3 + 8) = b3; *LP(u16, a3 + 10) = b3;
        if (up) {
          *LP(u16, a0 + 6) = b0; *LP(u16, a0 + 10) = b0; *LP(u16, a2 + 6) = b2; *LP(u16, a2 + 10) = b2;
          *LP(u16, a1 + 4) = b1; *LP(u16, a1 + 12) = b1; *LP(u16, a3 + 4) = b3; *LP(u16, a3 + 12) = b3;
        }
        if (lane == 31) *LP(u16, a3 + 4) = b3;
        if (lane == 63) { *LP(u16, a3 + 2) = b3; *LP(u16, a3) = b3; }
      } else if ((g >> 1) == c) {
        const unsigned p = kb - 2 * (1032 + 512 * (g & 1) + 16 * r16) - 536;
        const unsigned d0 = cvtpk(v[0], v[0]), d1 = cvtpk(v[1], v[1]), d2 = cvtpk(v[2], v[2]), d3 = cvtpk(v[3], v[3]);
        *LP(u32x2, p + 536 - 6) = (u32x2){d0, d0}; *LP(unsigned, p + 536 + 2) = d0; *LP(u16, p + 536 + 6) = b0;
        *LP(u32x2, p + 520 - 6) = (u32x2){d1, d1}; *LP(u32x2, p + 520 + 2) = (u32x2){d1, d1}; *LP(u16, p + 520 - 8) = b1;
        *LP(u32x2, p + 24 - 6) = (u32x2){d2, d2}; *LP(unsigned, p + 24 + 2) = d2; *LP(u16, p + 24 + 6) = b2;
        *LP(u32x2, p + 8 - 6) = (u32x2){d3, d3}; *LP(u32x2, p + 8 + 2) = (u32x2){d3, d3}; *LP(u16, p) = b3;
      }
    }
    if (a >= 2) {
      const unsigned kb = KA + KA_STRIDE * (a & 1) + 2 * 2055;
      if (lane < 60) *LP(u32x2, kb + 2 + 8 * lane) = (u32x2){0, 0};
      if (lane == 60) *LP(u16, kb) = (u16)cvtpk(k0, 0.f);
      if (lane < 4) *LP(u32x4, UC + UC_STRIDE * (2 * (a & 1) + (lane >> 1)) + 16 * (lane & 1)) = (u32x4){0, 0, 0, 0};
    }
    asm volatile("s_waitcnt vmcnt(16)" : "+v"(uA), "+v"(uB), "+v"(uAp), "+v"(uBp) :: "memory");
    {
      if (lane == 0 && (a & 1) == 0) uAp = 0;
      const unsigned ub = UC + UC_STRIDE * 2 * ch + 32 + (a & 1) * 2048 + l16;
      *LP(u32x4, ub) = uA; *LP(u32x4, ub + 1024) = uB;
      *LP(u32x4, ub + UC_STRIDE) = (u32x4){__builtin_amdgcn_alignbit(uA[0], uAp, 16), __builtin_amdgcn_alignbit(uA[1], uA[0], 16), __builtin_amdgcn_alignbit(uA[2], uA[1], 16), __builtin_amdgcn_alignbit(uA[3], uA[2], 16)};
      *LP(u32x4, ub + UC_STRIDE + 1024) = (u32x4){__builtin_amdgcn_alignbit(uB[0], uBp, 16), __builtin_amdgcn_alignbit(uB[1], uB[0], 16), __builtin_amdgcn_alignbit(uB[2], uB[1], 16), __builtin_amdgcn_alignbit(uB[3], uB[2], 16)};
    }
    WAITV(0);
    TS(10);
    BAR();
#pragma unroll
    for (int r = 5; r < 8; ++r) dma16x4_sc1(ws + WS_K + kvo + 8192 * r, l16, kvo + 8192 * r);
    if (unsafe) { WAITV(0); BAR(); }
    {
      const int cs = a & 1;
      unsigned ba = KA + KA_STRIDE * ch + 2 * (2040 - 16 * r16 + 8 * g) - 3584;
      unsigned aa = UC + UC_STRIDE * (2 * ch + 1 - (r16 & 1)) + 2 * (2 + (r16 & 14) + 8 * g);
      f32x4 y0 = {0, 0, 0, 0}, y1 = y0, y2 = y0, y3 = y0;
      for (int rr = 0; rr < 8; ++rr) {
        bf16x8 A[8], B[8];
#pragma unroll
        for (int i = 0; i < 8; ++i) {
          const LAS unsigned* p = LP(unsigned, aa + 1024 * (i >> 1)) + 128 * (i & 1);
          A[i] = AS8(((u32x4){p[0], p[1], p[2], p[3]}));
          B[i] = LP(bf16x8, ba)[32 * (7 - i)];
        }
        if (cs == 0) {
          y0 = MFMA16(A[0], B[0], y0);
#pragma unroll
          for (int t = 0; t <= 3; ++t) y1 = MFMA16(A[t], B[3 - t], y1);
#pragma unroll
          for (int t = 0; t <= 4; ++t) y2 = MFMA16(A[t], B[4 - t], y2);
#pragma unroll
          for (int t = 0; t <= 7; ++t) y3 = MFMA16(A[t], B[7 - t], y3);
        } else {
#pragma unroll
          for (int t = 0; t <= 1; ++t) y0 = MFMA16(A[t], B[1 - t], y0);
#pragma unroll
          for (int t = 0; t <= 2; ++t) y1 = MFMA16(A[t], B[2 - t], y1);
#pragma unroll
          for (int t = 0; t <= 5; ++t) y2 = MFMA16(A[t], B[5 - t], y2);
#pragma unroll
          for (int t = 0; t <= 6; ++t) y3 = MFMA16(A[t], B[6 - t], y3);
        }
        ba += 64; aa += 64;
      }
      char* yb = ws + WS_YT + (ch0 + ch) * 4096; const unsigned yo = 32 * r16 + 8 * g;
      gst8_sc1(yb, yo + 512 * (cs ? 1 : 0), (u32x2){min(cvtpk(y0[0], y0[1]), SENT - 1), min(cvtpk(y0[2], y0[3]), SENT - 1)});
      gst8_sc1(yb, yo + 512 * (cs ? 2 : 3), (u32x2){min(cvtpk(y1[0], y1[1]), SENT - 1), min(cvtpk(y1[2], y1[3]), SENT - 1)});
      gst8_sc1(yb, yo + 512 * (cs ? 5 : 4), (u32x2){min(cvtpk(y2[0], y2[1]), SENT - 1), min(cvtpk(y2[2], y2[3]), SENT - 1)});
      gst8_sc1(yb, yo + 512 * (cs ? 6 : 7), (u32x2){min(cvtpk(y3[0], y3[1]), SENT - 1), min(cvtpk(y3[2], y3[3]), SENT - 1)});
    }
    TS(11);
    WAITV(0);
    BAR();
    TS(12);
#pragma unroll
    for (int j = 0; j < 4; ++j)
      dma16x4v(wo + (128 * a + 32 * j) * 512 + 64 * hg, wvo, wvo + 7168, wvo + 2 * 7168, wvo + 3 * 7168, (a >> 1) * 65536 + (a & 1) * 16384 + 4096 * j);
    {
      u32x4 u0, u1, d0, d1, ya, yb;
      const unsigned uo = (row0 + 16 * a + r16) * 1024 + 128 * hg + 16 * g;
      gld16<0>(u0, u, uo); gld16<64>(u1, u, uo); gld16<0>(d0, Dsk, 128 * hg + 16 * g); gld16<64>(d1, Dsk, 128 * hg + 16 * g);
      const char* ys = ws + WS_YT + (64 * hg) * 4096 + (row0 + 16 * a) * 2; const unsigned yo = (lane >> 1) * 4096 + 16 * (lane & 1);
      _Pragma("unroll 1") for (unsigned spins = 0; spins < (1u << 20); ++spins) {
        gld16_sc1<0>(ya, ys, yo); gld16_sc1<0>(yb, ys + 32 * 4096, yo);
        asm volatile("s_waitcnt vmcnt(0)" : "+v"(ya), "+v"(yb) :: "memory");
        if (!__any(max(max4(ya), max4(yb)) == SENT)) break;
        __builtin_amdgcn_s_sleep(4);
      }
      asm volatile("" : "+v"(u0), "+v"(u1), "+v"(d0), "+v"(d1) :: "memory");
      const unsigned sc = R2 + 2048 * a;
      *LP(u32x4, sc + l16) = ya; *LP(u32x4, sc + 1024 + l16) = yb;
      const unsigned ta = sc + 256 * g + 8 * r16;
      const u32x4 t0 = __builtin_bit_cast(u32x4, trd2(ta, ta + 128)), t1 = __builtin_bit_cast(u32x4, trd2(ta + 1024, ta + 1152));
      u32x4 o0, o1;
#pragma unroll
      for (int i = 0; i < 4; ++i) {
        const unsigned s0 = cvtpk(bflo(u0[i]) * bflo(d0[i]), bfhi(u0[i]) * bfhi(d0[i])), s1 = cvtpk(bflo(u1[i]) * bflo(d1[i]), bfhi(u1[i]) * bfhi(d1[i]));
        o0[i] = cvtpk(bflo(t0[i]) + bflo(s0), bfhi(t0[i]) + bfhi(s0)); o1[i] = cvtpk(bflo(t1[i]) + bflo(s1), bfhi(t1[i]) + bfhi(s1));
      }
      const unsigned oo = (row0 + 16 * a + r16) * 2048 + 128 * hg + 16 * g;
      gst16(out, oo, o0); gst16(out, oo + 64, o1);
    }
    TS(13);
    WAITV(0);
    TS_FLUSH();
    return;
  }

  const unsigned slice = R2 + 4096 * w;
  u32x4 xk0, xk1;
  { const unsigned vo = (row0 + 8 * hg + (r16 & 7)) * 1024 + 128 * w + 16 * g;
    gld16<0>(xk0, x, vo); gld16<64>(xk1, x, vo); }
  dma16x2(wk + 1024 * w, l16, slice); dma16x2(wv + 1024 * w, l16, slice + 2048);
  unsigned p_ar, p_ai, p_br, p_bi, p_cr, p_ci, p_dt;
  { const unsigned t = tid & 255, d = ch0 + (t >> 7), vo = (d * 128 + (t & 127)) * 2;
    gld2(p_ar, Are, vo); gld2(p_ai, Aim, vo); gld2(p_br, Bre, vo); gld2(p_bi, Bim, vo); gld2(p_cr, Cre, vo); gld2(p_ci, Cim, vo); gld2(p_dt, logdt, d * 2); }
  asm volatile("s_waitcnt vmcnt(7)" : "+v"(xk0), "+v"(xk1) :: "memory");
  TS(2);
  {
    const unsigned ta = slice + 256 * g + 8 * r16;
    bf16x8 wk0 = trd2(ta, ta + 128), wk1 = trd2(ta + 1024, ta + 1152), wv0 = trd2(ta + 2048, ta + 2176), wv1 = trd2(ta + 3072, ta + 3200);
    f32x4 dk = {0, 0, 0, 0}, dv = {0, 0, 0, 0};
    dk = MFMA16(wk0, AS8(xk0), dk); dv = MFMA16(AS8(xk0), wv0, dv);
    dk = MFMA16(wk1, AS8(xk1), dk); dv = MFMA16(AS8(xk1), wv1, dv);
    PIN(dk); PIN(dv);
    *LP(f32x4, slice + l16) = dk; *LP(f32x4, slice + 1024 + l16) = dv;
  }
  asm volatile("s_waitcnt vmcnt(0)" : "+v"(p_ar), "+v"(p_ai), "+v"(p_br), "+v"(p_bi), "+v"(p_cr), "+v"(p_ci), "+v"(p_dt) :: "memory");
  if (w < 4) {
    const float dt = bflo(cvtpk(expf(bflo(p_dt)), 0.f));
    const float al = (dt * bflo(p_ar)) * 1.4426950408889634f;
    float be = (dt * bflo(p_ai)) * 0.15915494309189535f; be -= __builtin_rintf(be);
    const float br = dt * bflo(p_br), bi = dt * bflo(p_bi), cr = bflo(p_cr), ci = bflo(p_ci);
    const float xr = cr * br - ci * bi, xi = cr * bi + ci * br;
    *LP(f32x4, slice + 2048 + l16) = (f32x4){al, be, xr, xi};
    const float s = col_sum(row_sum(xr));
    if (lane == 0) *LP(float, slice + 3072) = s;
  }
  BAR();
  TS(3);
  f32x4 kacc[6];
  {
    const float eB = (float)(16 * r16), e1 = (float)(r16 + 1);
    const float e2 = (float)(r16 < 8 ? 258 + 2 * r16 : 516 + 256 * ((r16 - 8) >> 2) + 4 * (r16 & 3));
    const float e3 = (float)(1032 + 256 * ((r16 & 7) >> 1) + 8 * (r16 & 1));
    unsigned pa = R2 + 4096 * (w >> 2) + 2048 + 16 * ((16 * w + 4 * g) & 63); PIN(pa);
    u32x4 fb[2], f1[2], f2[2], f3;
#pragma unroll
    for (int c = 0; c < 2; ++c)
#pragma unroll
      for (int i = 0; i < 4; ++i) {
        const f32x4 P = *LP(f32x4, pa + 8192 * c + 16 * i);
        const f32x2 ab = {P[0], P[1]};
        const f32x2 wv_ = zpow<false>(ab, eB);
        fb[c][i] = cvtpk(P[2] * wv_.x - P[3] * wv_.y, P[2] * wv_.y + P[3] * wv_.x);
        const f32x2 a1 = zpow<false>(ab, e1); f1[c][i] = cvtpk(a1.x, -a1.y);
        const f32x2 a2 = zpow<true>(ab, e2); f2[c][i] = cvtpk(a2.x, -a2.y);
      }
    pa += 8192 * (r16 >> 3);
#pragma unroll
    for (int i = 0; i < 4; ++i) {
      const f32x2 ab = *LP(f32x2, pa + 16 * i);
      const f32x2 a3 = zpow<true>(ab, e3); f3[i] = cvtpk(a3.x, -a3.y);
    }
    const f32x4 z4 = {0, 0, 0, 0};
#pragma unroll
    for (int c = 0; c < 2; ++c) {
      kacc[3 * c + 0] = MFMA16(AS8(f1[c]), AS8(fb[c]), z4);
      kacc[3 * c + 1] = MFMA16(AS8(f2[c]), AS8(fb[c]), z4);
      kacc[3 * c + 2] = MFMA16(AS8(f3), AS8(fb[c]), z4);
    }
#pragma unroll
    for (int i = 0; i < 6; ++i) PIN(kacc[i]);
  }
  TS(5);
  BAR();
  TS(6);
  if (w < 4) {
    const int rh = w >> 1, hp = w & 1;
    const unsigned xa = R0 + 1024 * (32 * rh + r32) + 16 * (h ^ r16);
    const int q = r16 >> 2, p = lane & 3;
    unsigned wa = R1 + 128 * (8 * h + q) + 16 * ((4 * hp + 2 * (g & 1) + (p >> 1)) ^ ((q >> 1) << 2)) + 8 * (p & 1); PIN(wa);
    f32x16 acc = {0};
#pragma unroll
    for (int ks = 0; ks < 32; ++ks) {
      const bf16x8 b = *LP(bf16x8, (xa ^ ((ks & 7) << 5)) + 256 * (ks >> 3));
      const bf16x8 a = trd2(wa + 2048 * ks, wa + 2048 * ks + 512);
      acc = MFMA32(a, b, acc);
    }
    acc *= QSCALE;
    float n0 = 0, n1 = 0;
#pragma unroll
    for (int i = 0; i < 8; ++i) { n0 += acc[i] * acc[i]; n1 += acc[8 + i] * acc[8 + i]; }
    float o; n0 = x32(n0, o); n0 += o; n1 = x32(n1, o); n1 += o;
    n0 = __builtin_amdgcn_sqrtf(n0); n1 = __builtin_amdgcn_sqrtf(n1);
    const unsigned qs = R2 + 4096 * (4 + w);
    *LP(u32x4, qs + 2048 + l16) = (u32x4){cvtpk(acc[0], acc[1]), cvtpk(acc[2], acc[3]), cvtpk(acc[4], acc[5]), cvtpk(acc[6], acc[7])};
    *LP(u32x4, qs + 3072 + l16) = (u32x4){cvtpk(acc[8], acc[9]), cvtpk(acc[10], acc[11]), cvtpk(acc[12], acc[13]), cvtpk(acc[14], acc[15])};
    *LP(float, slice + 4 * lane) = n0; *LP(float, slice + 256 + 4 * lane) = n1;
    const float nm = col_max(row_max(fmaxf(n0, n1)));
    if (lane == 0) *LP(float, slice + 1024) = nm;
  } else if (w == 4 && lane < 12) {
    const unsigned j = lane >> 2, o1 = j ? 0u : 0x3f803f80u;
    *LP(u32x4, ONES + 512 * (lane & 3) + (j == 2 ? 64 : 16 * j)) = (u32x4){o1, o1, o1, o1};
  }
  TS(7);
  BAR();
  TS(8);
  const int qt = w & 3, hs = w >> 2;
  const bf16x8 qf = *LP(bf16x8, R2 + 4096 * (4 + qt) + 2048 + 1024 * hs + l16);
  const float kmax = *LP(float, R2 + 1028);
  float sh = *LP(float, R2 + 4096 * qt + 256 * hs + 4 * lane) * kmax;
  const float qmax = fmaxf(fmaxf(*LP(float, R2 + 1024), *LP(float, R2 + 4096 + 1024)), fmaxf(*LP(float, R2 + 8192 + 1024), *LP(float, R2 + 12288 + 1024)));
  const bool unsafe = (unsigned)__builtin_amdgcn_readfirstlane(__float_as_int(qmax * kmax)) > __float_as_uint(SAFE_BOUND);
  {
    const unsigned pb = (w >> 2) * R1 + PART + (w & 3) * 6144 + l16;
#pragma unroll
    for (int i = 0; i < 6; ++i) *LP(f32x4, pb + 1024 * i) = kacc[i];
  }
  BAR();
  TS(9);
  if (loc == 5 && w == 7) asm volatile("buffer_wbl2 sc0 sc1" ::: "memory");
  if (unsafe) {
    BAR(); BAR();
    const f32x16 z = {0};
    float m = -INFINITY;
    for (int t = 0; t < 64; ++t) {
      const f32x16 s = MFMA32(*LP(bf16x8, R0 + 1024 * t + l16), qf, z);
#pragma unroll
      for (int i = 0; i < 16; ++i) m = fmaxf(m, s[i]);
    }
    float o; m = x32(m, o); sh = fmaxf(m, o);
  }
  f32x16 oacc = {0};
  {
    f32x16 mneg;
#pragma unroll
    for (int i = 0; i < 16; ++i) mneg[i] = -sh;
    PIN(mneg);
    const bool lo = (lane & 16) == 0;
    unsigned kp = R0 + l16;
    unsigned vp = lo ? R1 + 256 * h + 16 * r16 : ONES + (r32 == 16 ? 0 : (r32 >= 20 && r32 < 28) ? 64 : 16);
    const unsigned vinc = lo ? 2048 : 0;
    const int tb = unsafe ? -1 : 4;
    f32x16 sA, sB; u32x4 pA0, pA1, pB0 = {0, 0, 0, 0}, pB1;
    bf16x8 kf = *LP(bf16x8, kp), v0, v1;
    sA = MFMA32(kf, qf, mneg);
    kf = *LP(bf16x8, kp + 1024);
    v0 = *LP(bf16x8, vp); v1 = v0;
#pragma unroll
    for (int i = 0; i < 16; ++i) sB[i] = 0.f;
#define CV(P, j, S, i) P[j] = cvtpk(S[i], S[i + 1])
#define E4(S, i) do { S[i] = EX2(S[i]); S[i + 1] = EX2(S[i + 1]); S[i + 2] = EX2(S[i + 2]); S[i + 3] = EX2(S[i + 3]); \
                      asm volatile("" : "+v"(S[i]), "+v"(S[i + 1]), "+v"(S[i + 2]), "+v"(S[i + 3])); SBAR(); } while (0)
#define UNIT(S, P0, T, Q0, Q1, KOFF, VOFF) do { \
      E4(S, 0); CV(Q1, 0, T, 8); CV(Q1, 1, T, 10); CV(Q1, 2, T, 12); CV(Q1, 3, T, 14); PIN(Q1); SBAR(); \
      T = MFMA32(kf, qf, mneg); kf = *LP(bf16x8, kp + KOFF); SBAR(); E4(S, 4); \
      oacc = MFMA32(v0, AS8(Q0), oacc); v0 = *LP(bf16x8, vp + VOFF); SBAR(); E4(S, 8); \
      oacc = MFMA32(v1, AS8(Q1), oacc); v1 = *LP(bf16x8, vp + VOFF + 512); SBAR(); \
      CV(P0, 0, S, 0); CV(P0, 1, S, 2); CV(P0, 2, S, 4); CV(P0, 3, S, 6); PIN(P0); SBAR(); E4(S, 12); } while (0)
    for (int t = 0; t < 64; t += 4) {
      if (t == tb || t == 32) BAR();
      UNIT(sA, pA0, sB, pB0, pB1, 2048, 0);
      UNIT(sB, pB0, sA, pA0, pA1, 3072, 1024);
      vp += vinc;
      UNIT(sA, pA0, sB, pB0, pB1, 4096, 0);
      UNIT(sB, pB0, sA, pA0, pA1, 5120, 1024);
      kp += 4096; vp += vinc;
    }
    CV(pB1, 0, sB, 8); CV(pB1, 1, sB, 10); CV(pB1, 2, sB, 12); CV(pB1, 3, sB, 14);
    oacc = MFMA32(v0, AS8(pB0), oacc); oacc = MFMA32(v1, AS8(pB1), oacc);
  }
  TS(10);
  {
    const float rl = __builtin_amdgcn_rcpf(oacc[8]);
    float o; const float r0 = x32(rl, o);
    u32x4 of_ = {cvtpk(oacc[0] * r0, oacc[1] * r0), cvtpk(oacc[2] * r0, oacc[3] * r0), cvtpk(oacc[4] * r0, oacc[5] * r0), cvtpk(oacc[6] * r0, oacc[7] * r0)};
#pragma unroll
    for (int i = 0; i < 4; ++i) of_[i] = min(of_[i], SENT - 1);
    gst16_sc1<0>(ws + WS_O + ((rg * 2 + (qt >> 1)) * 32 + 4 * hg + 2 * (qt & 1) + hs) * 1024, l16, of_);
  }
  BAR();
  TS(11);
  {
    const int rh = w >> 2, kq = w & 3;
    const char* osrc = ws + WS_O + ((rg * 2 + rh) * 32 + 8 * kq) * 1024;
    _Pragma("unroll 1") for (unsigned spins = 0; spins < (1u << 20); ++spins) {
      unsigned t; gld4_sc1<1020>(t, osrc, (lane & 7) * 1024);
      asm volatile("s_waitcnt vmcnt(0)" : "+v"(t) :: "memory");
      if (!__any(t == SENT)) break;
    }
    TS(12);
    u32x4 of[8];
    _Pragma("unroll 1") for (unsigned spins = 0; spins < (1u << 20); ++spins) {
      gld16_sc1<0>(of[0], osrc, l16); gld16_sc1<1024>(of[1], osrc, l16); gld16_sc1<2048>(of[2], osrc, l16); gld16_sc1<3072>(of[3], osrc, l16);
      gld16_sc1<0>(of[4], osrc + 4096, l16); gld16_sc1<1024>(of[5], osrc + 4096, l16); gld16_sc1<2048>(of[6], osrc + 4096, l16); gld16_sc1<3072>(of[7], osrc + 4096, l16);
      asm volatile("s_waitcnt vmcnt(0)" : "+v"(of[0]), "+v"(of[1]), "+v"(of[2]), "+v"(of[3]), "+v"(of[4]), "+v"(of[5]), "+v"(of[6]), "+v"(of[7]) :: "memory");
      unsigned mx = 0;
#pragma unroll
      for (int i = 0; i < 8; ++i) mx = max(mx, max4(of[i]));
      if (!__any(mx == SENT)) break;
    }
    TS(13);
    const int q = r16 >> 2, p = lane & 3;
    unsigned wa = (kq >> 1) * R1 + (kq & 1) * 16384 + 128 * (4 * h + q) + 16 * ((2 * (g & 1) + (p >> 1)) ^ ((q >> 1) << 2)) + 8 * (p & 1); PIN(wa);
    const unsigned wb = wa ^ 64;
    f32x16 c0 = {0}, c1 = {0};
#pragma unroll
    for (int ks = 0; ks < 8; ++ks) {
      c0 = MFMA32(trd2(wa + 2048 * ks, wa + 2048 * ks + 1024), AS8(of[ks]), c0);
      c1 = MFMA32(trd2(wb + 2048 * ks, wb + 2048 * ks + 1024), AS8(of[ks]), c1);
    }
    const unsigned rbase = 32768 + rh * R1;
    const f32x4 G[8] = {{c0[0], c0[1], c0[2], c0[3]}, {c0[4], c0[5], c0[6], c0[7]}, {c0[8], c0[9], c0[10], c0[11]}, {c0[12], c0[13], c0[14], c0[15]},
                        {c1[0], c1[1], c1[2], c1[3]}, {c1[4], c1[5], c1[6], c1[7]}, {c1[8], c1[9], c1[10], c1[11]}, {c1[12], c1[13], c1[14], c1[15]}};
    unsigned wr = rbase + 2048 * kq + l16; PIN(wr);
#pragma unroll
    for (int j = 0; j < 4; ++j) { *LP(f32x4, wr + 8192 * j) = G[2 * j]; *LP(f32x4, wr + 8192 * j + 1024) = G[2 * j + 1]; }
    BAR();
    unsigned rd = rbase + 8192 * kq + l16; PIN(rd);
    f32x4 a = *LP(f32x4, rd), b = *LP(f32x4, rd + 1024);
#pragma unroll
    for (int s = 1; s < 4; ++s) { a += *LP(f32x4, rd + 2048 * s); b += *LP(f32x4, rd + 2048 * s + 1024); }
    auto s0 = __builtin_amdgcn_permlane32_swap(cvtpk(a[0], a[1]), cvtpk(b[0], b[1]), false, false);
    auto s1 = __builtin_amdgcn_permlane32_swap(cvtpk(a[2], a[3]), cvtpk(b[2], b[3]), false, false);
    gst16(out, (row0 + 32 * rh + r32) * 2048 + 1024 + 128 * hg + 32 * kq + 16 * h, (u32x4){s0[0], s1[0], s0[1], s1[1]});
  }
  TS_FLUSH();
}

extern "C" void kernel_launch(void* const* d_in, const int* in_sizes, int n_in, void* d_out, int out_size, void* d_ws, size_t ws_size, hipStream_t stream) {
  (void)in_sizes; (void)n_in; (void)out_size; (void)ws_size;
  (void)hipFuncSetAttribute((const void*)fused_kernel, hipFuncAttributeMaxDynamicSharedMemorySize, LDS_BYTES);
  fused_kernel<<<256, 768, LDS_BYTES, stream>>>((const u16*)d_in[0], (const u16*)d_in[1], (const u16*)d_in[2], (const u16*)d_in[3], (const u16*)d_in[4],
      (const u16*)d_in[5], (const u16*)d_in[6], (const u16*)d_in[7], (const u16*)d_in[8], (const u16*)d_in[9], (const u16*)d_in[10], (const u16*)d_in[11],
      (const u16*)d_in[12], (const u16*)d_in[13], (u16*)d_out, (char*)d_ws);
}
```

```cpp
#define DIAG 1
#define DMODE 0
#include <hip/hip_runtime.h>
#include <hip/hip_bf16.h>
#pragma clang diagnostic ignored "-Wint-to-pointer-cast"

typedef unsigned short u16;
typedef unsigned long long u64;
typedef __attribute__((ext_vector_type(8))) short bf16x8;
typedef __attribute__((ext_vector_type(4))) short s16x4;
typedef __attribute__((ext_vector_type(16))) float f32x16;
typedef __attribute__((ext_vector_type(4))) float f32x4;
typedef __attribute__((ext_vector_type(2))) float f32x2;
typedef __attribute__((ext_vector_type(4))) unsigned u32x4;
typedef __attribute__((ext_vector_type(2))) unsigned u32x2;
typedef __attribute__((ext_vector_type(2))) __bf16 bf16x2;
#define LAS __attribute__((address_space(3)))
#define LP(T, off) ((LAS T*)(unsigned)(off))
#define SBAR() __builtin_amdgcn_sched_barrier(0)
#define PIN(x) asm volatile("" : "+v"(x))
#define MFMA32(a, b, c) __builtin_amdgcn_mfma_f32_32x32x16_bf16(a, b, c, 0, 0, 0)
#define MFMA16(a, b, c) __builtin_amdgcn_mfma_f32_16x16x32_bf16(a, b, c, 0, 0, 0)
#define AS8(x) __builtin_bit_cast(bf16x8, x)
#define EX2(v) __builtin_amdgcn_exp2f(v)
#define BAR() asm volatile("s_waitcnt lgkmcnt(0)\n\ts_barrier" ::: "memory")
#define WAITV(n) asm volatile("s_waitcnt vmcnt(" #n ")" ::: "memory")

#define STAMP 0
#define TS(i) do { if ((i) == DIAG) asm volatile("s_memrealtime %0\n\ts_waitcnt lgkmcnt(0)" : "=s"(dg1_) :: "memory"); } while (0)
#define TSR(i) do { asm volatile("s_memrealtime %0\n\ts_waitcnt lgkmcnt(0)" : "=s"(dg0_) :: "memory"); dg1_ = dg0_; } while (0)
#define TS_FLUSH() do { TS(14); if (w == 0) { const unsigned mn_ = __builtin_amdgcn_readfirstlane(*LP(int, ONES + 128)), mx_ = __builtin_amdgcn_readfirstlane(*LP(int, ONES + 132)); \
  const unsigned tgt_ = mn_ + 6000 + (DMODE == 0 ? 0 : DMODE == 1 ? 4 * (mx_ - mn_) : 4 * ((unsigned)dg1_ - mn_)); u64 n_; do { __builtin_amdgcn_s_sleep(8); asm volatile("s_memrealtime %0\n\ts_waitcnt lgkmcnt(0)" : "=s"(n_) :: "memory"); } while ((int)((unsigned)n_ - tgt_) < 0); } } while (0)

constexpr unsigned WS_EPOCH = 0, WS_FLAG = 1024, WS_KMAX = 4096, WS_K = 8192, WS_V = WS_K + 65536, WS_O = 1u << 18, WS_UT = WS_O + (2u << 20), WS_YT = WS_UT + (2u << 20), WS_DBG = 8u << 20;
constexpr unsigned R0 = 0, R1 = 65536, R2 = 131072, LDS_BYTES = 163840;
constexpr unsigned KA = R2, KA_STRIDE = 4608;
constexpr unsigned UC = R2 + 9216, UC_STRIDE = 4160;
constexpr unsigned ONES = R2 + 28672;
constexpr unsigned PART = 40960;
constexpr float QSCALE = 0.25f * 1.4426950408889634f;
constexpr float SAFE_BOUND = 40.f;
constexpr unsigned SENT = 0xffffffffu;
constexpr int CODE_KB = 15;

__device__ __forceinline__ unsigned cvtpk(float lo, float hi) { f32x2 v = {lo, hi}; return __builtin_bit_cast(unsigned, __builtin_convertvector(v, bf16x2)); }
__device__ __forceinline__ float bflo(unsigned x) { return __uint_as_float(x << 16); }
__device__ __forceinline__ float bfhi(unsigned x) { return __uint_as_float(x & 0xffff0000u); }
template <int C> __device__ __forceinline__ float dpp(float v) { return __int_as_float(__builtin_amdgcn_update_dpp(0, __float_as_int(v), C, 0xf, 0xf, true)); }
__device__ __forceinline__ float x16(float v, float& o) { auto r = __builtin_amdgcn_permlane16_swap(__float_as_uint(v), __float_as_uint(v), false, false); o = __uint_as_float(r[1]); return __uint_as_float(r[0]); }
__device__ __forceinline__ float x32(float v, float& o) { auto r = __builtin_amdgcn_permlane32_swap(__float_as_uint(v), __float_as_uint(v), false, false); o = __uint_as_float(r[1]); return __uint_as_float(r[0]); }
__device__ __forceinline__ float row_sum(float v) { v += dpp<0x128>(v); v += dpp<0x124>(v); v += dpp<0x122>(v); v += dpp<0x121>(v); return v; }
__device__ __forceinline__ float row_max(float v) { v = fmaxf(v, dpp<0x128>(v)); v = fmaxf(v, dpp<0x124>(v)); v = fmaxf(v, dpp<0x122>(v)); v = fmaxf(v, dpp<0x121>(v)); return v; }
__device__ __forceinline__ float col_sum(float v) { float o; v = x16(v, o); v += o; v = x32(v, o); return v + o; }
__device__ __forceinline__ float col_max(float v) { float o; v = x16(v, o); v = fmaxf(v, o); v = x32(v, o); return fmaxf(v, o); }

__device__ __forceinline__ void dma16(const void* base, unsigned voff, unsigned ldst) {
  asm volatile("s_mov_b32 m0, %2\n\ts_nop 0\n\tglobal_load_lds_dwordx4 %0, %1" :: "v"(voff), "s"(base), "s"(ldst) : "memory");
}
__device__ __forceinline__ void dma16x2(const void* base, unsigned voff, unsigned ldst) {
  asm volatile("s_mov_b32 m0, %2\n\ts_nop 0\n\tglobal_load_lds_dwordx4 %0, %1\n\tglobal_load_lds_dwordx4 %0, %1 offset:1024" :: "v"(voff), "s"(base), "s"(ldst) : "memory");
}
__device__ __forceinline__ void dma16x4v(const void* base, unsigned v0, unsigned v1, unsigned v2, unsigned v3, unsigned ldst) {
  asm volatile("s_mov_b32 m0, %5\n\ts_nop 0\n\tglobal_load_lds_dwordx4 %0, %4\n\tglobal_load_lds_dwordx4 %1, %4 offset:1024\n\t"
               "global_load_lds_dwordx4 %2, %4 offset:2048\n\tglobal_load_lds_dwordx4 %3, %4 offset:3072" :: "v"(v0), "v"(v1), "v"(v2), "v"(v3), "s"(base), "s"(ldst) : "memory");
}
__device__ __forceinline__ void dma16x4_sc1(const void* base, unsigned voff, unsigned ldst) {
  asm volatile("s_mov_b32 m0, %2\n\ts_nop 0\n\tglobal_load_lds_dwordx4 %0, %1 sc1\n\tglobal_load_lds_dwordx4 %0, %1 offset:1024 sc1\n\t"
               "global_load_lds_dwordx4 %0, %1 offset:2048 sc1\n\tglobal_load_lds_dwordx4 %0, %1 offset:3072 sc1" :: "v"(voff), "s"(base), "s"(ldst) : "memory");
}
template <int OFF> __device__ __forceinline__ void gld16(u32x4& d, const void* base, unsigned voff) { asm volatile("global_load_dwordx4 %0, %1, %2 offset:%3" : "=v"(d) : "v"(voff), "s"(base), "i"(OFF) : "memory"); }
template <int OFF> __device__ __forceinline__ void gld16_sc1(u32x4& d, const void* base, unsigned voff) { asm volatile("global_load_dwordx4 %0, %1, %2 offset:%3 sc1" : "=v"(d) : "v"(voff), "s"(base), "i"(OFF) : "memory"); }
template <int OFF> __device__ __forceinline__ void gld4_sc1(unsigned& d, const void* base, unsigned voff) { asm volatile("global_load_dword %0, %1, %2 offset:%3 sc1" : "=v"(d) : "v"(voff), "s"(base), "i"(OFF) : "memory"); }
__device__ __forceinline__ void gld2(unsigned& d, const void* base, unsigned voff) { asm volatile("global_load_ushort %0, %1, %2" : "=v"(d) : "v"(voff), "s"(base) : "memory"); }
__device__ __forceinline__ void gld8_sc1(u32x2& d, const void* base, unsigned voff) { asm volatile("global_load_dwordx2 %0, %1, %2 sc1" : "=v"(d) : "v"(voff), "s"(base) : "memory"); }
template <int OFF> __device__ __forceinline__ void gst16_sc1(void* base, unsigned voff, u32x4 d) { asm volatile("global_store_dwordx4 %0, %1, %2 offset:%3 sc1\n\ts_nop 1" :: "v"(voff), "v"(d), "s"(base), "i"(OFF) : "memory"); }
__device__ __forceinline__ void gst16(void* base, unsigned voff, u32x4 d) { asm volatile("global_store_dwordx4 %0, %1, %2\n\ts_nop 1" :: "v"(voff), "v"(d), "s"(base) : "memory"); }
__device__ __forceinline__ void gst8_sc1(void* base, unsigned voff, u32x2 d) { asm volatile("global_store_dwordx2 %0, %1, %2 sc1" :: "v"(voff), "v"(d), "s"(base) : "memory"); }
__device__ __forceinline__ void gst4_sc1(void* base, unsigned voff, unsigned d) { asm volatile("global_store_dword %0, %1, %2 sc1" :: "v"(voff), "v"(d), "s"(base) : "memory"); }
__device__ __forceinline__ s16x4 trd(unsigned a) { return __builtin_bit_cast(s16x4, __builtin_amdgcn_ds_read_tr16_b64_v4i16(LP(s16x4, a))); }
__device__ __forceinline__ bf16x8 trd2(unsigned a0, unsigned a1) { s16x4 x = trd(a0), y = trd(a1); return (bf16x8){x[0], x[1], x[2], x[3], y[0], y[1], y[2], y[3]}; }
__device__ __forceinline__ unsigned max4(u32x4 v) { return max(max(v[0], v[1]), max(v[2], v[3])); }

template <bool FR> __device__ __forceinline__ f32x2 zpow(f32x2 ab, float e) {
  f32x2 t = ab * e;
  float m = EX2(t.x);
  float a = FR ? __builtin_amdgcn_fractf(t.y) : t.y;
  f32x2 cs = {__builtin_amdgcn_cosf(a), __builtin_amdgcn_sinf(a)};
  return cs * m;
}

__global__ void __launch_bounds__(768, 3)
fused_kernel(const u16* u_, const u16* Are_, const u16* Aim_, const u16* Bre_, const u16* Bim_, const u16* Cre_, const u16* Cim_, const u16* logdt_, const u16* Dsk_,
             const u16* x_, const u16* wq_, const u16* wk_, const u16* wv_, const u16* wo_, u16* out_, char* ws_) {
  typedef __attribute__((ext_vector_type(8))) u64 u64x8;
  u64 dg0_, dg1_; asm volatile("s_memrealtime %0\n\ts_waitcnt lgkmcnt(0)" : "=s"(dg0_) :: "memory"); dg1_ = dg0_;
  u64x8 ka_, kb_;
  asm volatile("s_load_dwordx16 %0, %2, 0\n\ts_load_dwordx16 %1, %2, 64\n\ts_waitcnt lgkmcnt(0)" : "=&s"(ka_), "=&s"(kb_) : "s"(__builtin_amdgcn_kernarg_segment_ptr()) : "memory");
  const u16 *u = (const u16*)ka_[0], *Are = (const u16*)ka_[1], *Aim = (const u16*)ka_[2], *Bre = (const u16*)ka_[3], *Bim = (const u16*)ka_[4], *Cre = (const u16*)ka_[5], *Cim = (const u16*)ka_[6], *logdt = (const u16*)ka_[7];
  const u16 *Dsk = (const u16*)kb_[0], *x = (const u16*)kb_[1], *wq = (const u16*)kb_[2], *wk = (const u16*)kb_[3], *wv = (const u16*)kb_[4], *wo = (const u16*)kb_[5];
  u16* out = (u16*)kb_[6]; char* ws = (char*)kb_[7];
  u64 pc0; asm volatile("s_getpc_b64 %0" : "=s"(pc0));
  const int tid = threadIdx.x, lane = tid & 63;
  const int w = __builtin_amdgcn_readfirstlane(tid >> 6);
  const int bid = blockIdx.x, xcd = bid & 7, loc = bid >> 3;
  const int rg = xcd * 4 + (loc >> 3), hg = loc & 7, row0 = rg * 64, ch0 = xcd * 64 + loc * 2;
  const int r16 = lane & 15, g = lane >> 4, r32 = lane & 31, h = lane >> 5;
  const unsigned l16 = 16 * lane;
  const unsigned wvo = (lane >> 3) * 1024 + 16 * ((lane & 7) ^ (((lane >> 4) & 1) << 2));
  TS(1);

  if (w >= 8) {
    const int a = w - 8;
    u64 tag = 0;
    if (a == 0) {
      u32x2 ep; gld8_sc1(ep, ws + WS_EPOCH, 0);
      const unsigned ust = R0 + 1024 * ((8 * hg + 56) & 63);
      const u16* ub = u + row0 * 512 + 64 * hg;
      const unsigned uvo = (lane >> 3) * 1024 + 16 * (lane & 7);
      dma16x4v(ub, uvo, uvo + 7168, uvo + 2 * 7168, uvo + 3 * 7168, ust); dma16x4v(ub + 16384, uvo, uvo + 7168, uvo + 2 * 7168, uvo + 3 * 7168, ust + 4096);
      const u32x4 sent = {SENT, SENT, SENT, SENT};
      char* os = ws + WS_O + (rg * 64 + 4 * hg) * 1024; char* ys = ws + WS_YT + ch0 * 4096;
      gst16_sc1<0>(os, l16, sent); gst16_sc1<1024>(os, l16, sent); gst16_sc1<2048>(os, l16, sent); gst16_sc1<3072>(os, l16, sent);
      gst16_sc1<0>(os + 32768, l16, sent); gst16_sc1<1024>(os + 32768, l16, sent); gst16_sc1<2048>(os + 32768, l16, sent); gst16_sc1<3072>(os + 32768, l16, sent);
      gst16_sc1<0>(ys, l16, sent); gst16_sc1<1024>(ys, l16, sent); gst16_sc1<2048>(ys, l16, sent); gst16_sc1<3072>(ys, l16, sent);
      gst16_sc1<0>(ys + 4096, l16, sent); gst16_sc1<1024>(ys + 4096, l16, sent); gst16_sc1<2048>(ys + 4096, l16, sent); gst16_sc1<3072>(ys + 4096, l16, sent);
      asm volatile("s_waitcnt vmcnt(16)" : "+v"(ep) :: "memory");
      tag = (((u64)(unsigned)__builtin_amdgcn_readfirstlane(ep[1]) << 32) | (unsigned)__builtin_amdgcn_readfirstlane(ep[0])) + 1;
      TS(2);
      unsigned ta = ust + 128 * (8 * g + (r16 >> 2)) + 8 * (lane & 3); PIN(ta);
      char* ut = ws + WS_UT + (64 * hg) * 4096 + row0 * 2;
      const unsigned so = r16 * 4096 + 16 * g;
#pragma unroll
      for (int j = 0; j < 8; ++j) {
        const bf16x8 v = trd2(ta + 4096 * (j >> 2) + 32 * (j & 3), ta + 4096 * (j >> 2) + 32 * (j & 3) + 512);
        if (j >> 2) gst16_sc1<64>(ut + 65536 * (j & 3), so, __builtin_bit_cast(u32x4, v)); else gst16_sc1<0>(ut + 65536 * (j & 3), so, __builtin_bit_cast(u32x4, v));
      }
    } else if (a == 3) {
      if (loc < CODE_KB) {
        unsigned dummy; asm volatile("global_load_dword %0, %1, %2\n\ts_waitcnt vmcnt(0)" : "=v"(dummy) : "v"(loc * 1024 + l16), "s"(pc0) : "memory");
      }
    }
    auto dma_x = [&](int i0, int i1) { _Pragma("unroll 1") for (int i = i0; i < i1; ++i) { const int r = (8 * hg + i) & 63; dma16(x + (row0 + r) * 512, 16 * (lane ^ (r & 15)), R0 + 1024 * r); } };
    auto dma_wq = [&](int i0, int i1) { _Pragma("unroll 1") for (int i = i0; i < i1; ++i) { const int p = (16 * (loc >> 3) + i) & 63; dma16(wq + p * 4096 + 64 * hg, wvo, R1 + 1024 * p); } };
    dma_x(0, a == 1 ? 8 : 0); dma_wq(0, a == 2 ? 16 : 0);
    BAR();
    TS(3);
    if (a == 0) {
      unsigned rb = R2 + l16; PIN(rb);
      f32x4 dk = *LP(f32x4, rb), dv = *LP(f32x4, rb + 1024);
#pragma unroll
      for (int s = 1; s < 8; ++s) { dk += *LP(f32x4, rb + 4096 * s); dv += *LP(f32x4, rb + 4096 * s + 1024); }
      const int kk0 = row0 + 8 * hg, t = kk0 >> 5, c = (kk0 >> 3) & 3;
      const u32x2 kp = {cvtpk(dk[0], dk[1]), cvtpk(dk[2], dk[3])}, vp = {cvtpk(dv[0], dv[1]), cvtpk(dv[2], dv[3])};
      if (r16 < 8) gst8_sc1(ws, WS_K + t * 1024 + ((g & 1) * 32 + 8 * c + r16) * 16 + 8 * (g >> 1), kp);
      if (g < 2) gst8_sc1(ws, WS_V + t * 1024 + 512 * (c >> 1) + 256 * g + 16 * r16 + 8 * (c & 1), vp);
      const float k2 = row_max(col_sum(dk[0] * dk[0] + dk[1] * dk[1] + dk[2] * dk[2] + dk[3] * dk[3]));
      if (lane == 0) gst4_sc1(ws, WS_KMAX + 4 * bid, __float_as_uint(k2));
      if (lane == 0) gst4_sc1(ws, WS_KMAX + 1024 + 4 * bid, (unsigned)dg0_);
      TS(4);
      WAITV(0);
      if (lane == 0) gst8_sc1(ws, WS_FLAG + 8 * bid, (u32x2){(unsigned)tag, (unsigned)(tag >> 32)});
    } else {
      dma_x(a == 1 ? 8 : 43, a == 1 ? 43 : a == 2 ? 0 : 64); dma_wq(a == 2 ? 16 : 43, a == 2 ? 43 : a == 1 ? 0 : 64);
      TS(4);
      WAITV(0);
    }
    TS(5);
    BAR();
    TS(6);
    const float k0 = *LP(float, R2 + 4096 * (2 * (a & 1)) + 3072) + *LP(float, R2 + 4096 * (2 * (a & 1) + 1) + 3072);
    if (a == 0) {
      _Pragma("unroll 1") for (unsigned spins = 0; spins < (1u << 20); ++spins) {
        u32x4 f0, f1; gld16_sc1<0>(f0, ws + WS_FLAG, 32 * lane); gld16_sc1<16>(f1, ws + WS_FLAG, 32 * lane);
        asm volatile("s_waitcnt vmcnt(0)" : "+v"(f0), "+v"(f1) :: "memory");
        const unsigned lo = (unsigned)tag, hi = (unsigned)(tag >> 32);
        const bool ok = f0[0] == lo && f0[1] == hi && f0[2] == lo && f0[3] == hi && f1[0] == lo && f1[1] == hi && f1[2] == lo && f1[3] == hi;
        if (__all(ok)) break;
        __builtin_amdgcn_s_sleep(1);
      }
      u32x4 km; gld16_sc1<0>(km, ws + WS_KMAX, l16);
      asm volatile("s_waitcnt vmcnt(0)" : "+v"(km) :: "memory");
      float m = fmaxf(fmaxf(__uint_as_float(km[0]), __uint_as_float(km[1])), fmaxf(__uint_as_float(km[2]), __uint_as_float(km[3])));
      m = __builtin_amdgcn_sqrtf(col_max(row_max(m))) * 1.01f;
      if (lane == 0) *LP(float, R2 + 1028) = m;
      { u32x4 st; gld16_sc1<1024>(st, ws + WS_KMAX, l16); asm volatile("s_waitcnt vmcnt(0)" : "+v"(st) :: "memory");
        int base = (int)(unsigned)dg0_; int mn = 1 << 30, mx = -(1 << 30);
        for (int i = 0; i < 4; ++i) { int d = (int)st[i] - base; mn = min(mn, d); mx = max(mx, d); }
        for (int o = 1; o < 64; o <<= 1) { mn = min(mn, __shfl_xor(mn, o)); mx = max(mx, __shfl_xor(mx, o)); }
        if (lane == 0) { *LP(int, ONES + 128) = mn + base; *LP(int, ONES + 132) = mx + base; } }
      if (bid == 0 && lane == 0) gst8_sc1(ws, WS_EPOCH, (u32x2){(unsigned)tag, (unsigned)(tag >> 32)});
      WAITV(0);
    }
    TS(7);
    BAR();
    TS(8);
    const unsigned kvo = (a & 1) * 65536 + (a >> 1) * 4096;
    dma16x4_sc1(ws + WS_K + kvo, l16, kvo);
    const int ch = a >> 1;
    u32x4 uA, uB; unsigned uAp, uBp;
    { const char* us = ws + WS_UT + (ch0 + ch) * 4096 + (a & 1) * 2048;
      gld16_sc1<0>(uA, us, l16); gld16_sc1<1024>(uB, us, l16); gld4_sc1<-4>(uAp, us, l16); gld4_sc1<1020>(uBp, us, l16); }
#pragma unroll
    for (int r = 1; r < 5; ++r) dma16x4_sc1(ws + WS_K + kvo + 8192 * r, l16, kvo + 8192 * r);
    const float qk_ = fmaxf(fmaxf(*LP(float, R2 + 1024), *LP(float, R2 + 4096 + 1024)), fmaxf(*LP(float, R2 + 8192 + 1024), *LP(float, R2 + 12288 + 1024))) * *LP(float, R2 + 1028);
    const bool unsafe = (unsigned)__builtin_amdgcn_readfirstlane(__float_as_int(qk_)) > __float_as_uint(SAFE_BOUND);
    __builtin_amdgcn_s_setprio(2);
    WAITV(20);
    BAR();
    TS(9);
    for (int task = a; task < 6; task += 4) {
      unsigned pb = PART + 1024 * task + l16; PIN(pb);
      f32x4 v = *LP(f32x4, pb);
#pragma unroll
      for (int s = 1; s < 4; ++s) v += *LP(f32x4, pb + 6144 * s);
      pb += R1; PIN(pb);
#pragma unroll
      for (int s = 0; s < 4; ++s) v += *LP(f32x4, pb + 6144 * s);
      const int c = task >= 3, tau = task - 3 * c;
      const unsigned kb = KA + KA_STRIDE * c + 2 * 2055;
      const unsigned c01 = cvtpk(v[0], v[1]), c23 = cvtpk(v[2], v[3]);
      const u16 b0 = (u16)c01, b1 = (u16)(c01 >> 16), b2 = (u16)c23, b3 = (u16)(c23 >> 16);
      if (tau == 0) {
        const unsigned p = kb - 2 * (4 * g + 4 + 16 * r16);
        *LP(u16, p + 6) = b0; *LP(u16, p + 4) = b1; *LP(u16, p + 2) = b2; *LP(u16, p) = b3;
        if (lane == 63) *LP(u16, p - 2) = b3;
      } else if (tau == 1) {
        const bool up = g >= 2;
        const unsigned a0 = kb - 2 * ((up ? 516 + 256 * (g - 2) : 258 + 8 * g) + 16 * r16) - 8, st = up ? 8 : 4;
        const unsigned a1 = a0 - st, a2 = a1 - st, a3 = a2 - st;
        *LP(u16, a0 + 8) = b0; *LP(u16, a2 + 8) = b2;
        *LP(u16, a1 + 6) = b1; *LP(u16, a1 + 8) = b1; *LP(u16, a1 + 10) = b1;
        *LP(u16, a3 + 6) = b3; *LP(u16, a3 + 8) = b3; *LP(u16, a3 + 10) = b3;
        if (up) {
          *LP(u16, a0 + 6) = b0; *LP(u16, a0 + 10) = b0; *LP(u16, a2 + 6) = b2; *LP(u16, a2 + 10) = b2;
          *LP(u16, a1 + 4) = b1; *LP(u16, a1 + 12) = b1; *LP(u16, a3 + 4) = b3; *LP(u16, a3 + 12) = b3;
        }
        if (lane == 31) *LP(u16, a3 + 4) = b3;
        if (lane == 63) { *LP(u16, a3 + 2) = b3; *LP(u16, a3) = b3; }
      } else if ((g >> 1) == c) {
        const unsigned p = kb - 2 * (1032 + 512 * (g & 1) + 16 * r16) - 536;
        const unsigned d0 = cvtpk(v[0], v[0]), d1 = cvtpk(v[1], v[1]), d2 = cvtpk(v[2], v[2]), d3 = cvtpk(v[3], v[3]);
        *LP(u32x2, p + 536 - 6) = (u32x2){d0, d0}; *LP(unsigned, p + 536 + 2) = d0; *LP(u16, p + 536 + 6) = b0;
        *LP(u32x2, p + 520 - 6) = (u32x2){d1, d1}; *LP(u32x2, p + 520 + 2) = (u32x2){d1, d1}; *LP(u16, p + 520 - 8) = b1;
        *LP(u32x2, p + 24 - 6) = (u32x2){d2, d2}; *LP(unsigned, p + 24 + 2) = d2; *LP(u16, p + 24 + 6) = b2;
        *LP(u32x2, p + 8 - 6) = (u32x2){d3, d3}; *LP(u32x2, p + 8 + 2) = (u32x2){d3, d3}; *LP(u16, p) = b3;
      }
    }
    if (a >= 2) {
      const unsigned kb = KA + KA_STRIDE * (a & 1) + 2 * 2055;
      if (lane < 60) *LP(u32x2, kb + 2 + 8 * lane) = (u32x2){0, 0};
      if (lane == 60) *LP(u16, kb) = (u16)cvtpk(k0, 0.f);
      if (lane < 4) *LP(u32x4, UC + UC_STRIDE * (2 * (a & 1) + (lane >> 1)) + 16 * (lane & 1)) = (u32x4){0, 0, 0, 0};
    }
    asm volatile("s_waitcnt vmcnt(16)" : "+v"(uA), "+v"(uB), "+v"(uAp), "+v"(uBp) :: "memory");
    {
      if (lane == 0 && (a & 1) == 0) uAp = 0;
      const unsigned ub = UC + UC_STRIDE * 2 * ch + 32 + (a & 1) * 2048 + l16;
      *LP(u32x4, ub) = uA; *LP(u32x4, ub + 1024) = uB;
      *LP(u32x4, ub + UC_STRIDE) = (u32x4){__builtin_amdgcn_alignbit(uA[0], uAp, 16), __builtin_amdgcn_alignbit(uA[1], uA[0], 16), __builtin_amdgcn_alignbit(uA[2], uA[1], 16), __builtin_amdgcn_alignbit(uA[3], uA[2], 16)};
      *LP(u32x4, ub + UC_STRIDE + 1024) = (u32x4){__builtin_amdgcn_alignbit(uB[0], uBp, 16), __builtin_amdgcn_alignbit(uB[1], uB[0], 16), __builtin_amdgcn_alignbit(uB[2], uB[1], 16), __builtin_amdgcn_alignbit(uB[3], uB[2], 16)};
    }
    WAITV(0);
    TS(10);
    BAR();
#pragma unroll
    for (int r = 5; r < 8; ++r) dma16x4_sc1(ws + WS_K + kvo + 8192 * r, l16, kvo + 8192 * r);
    if (unsafe) { WAITV(0); BAR(); }
    {
      const int cs = a & 1;
      unsigned ba = KA + KA_STRIDE * ch + 2 * (2040 - 16 * r16 + 8 * g) - 3584;
      unsigned aa = UC + UC_STRIDE * (2 * ch + 1 - (r16 & 1)) + 2 * (2 + (r16 & 14) + 8 * g);
      f32x4 y0 = {0, 0, 0, 0}, y1 = y0, y2 = y0, y3 = y0;
      for (int rr = 0; rr < 8; ++rr) {
        bf16x8 A[8], B[8];
#pragma unroll
        for (int i = 0; i < 8; ++i) {
          const LAS unsigned* p = LP(unsigned, aa + 1024 * (i >> 1)) + 128 * (i & 1);
          A[i] = AS8(((u32x4){p[0], p[1], p[2], p[3]}));
          B[i] = LP(bf16x8, ba)[32 * (7 - i)];
        }
        if (cs == 0) {
          y0 = MFMA16(A[0], B[0], y0);
#pragma unroll
          for (int t = 0; t <= 3; ++t) y1 = MFMA16(A[t], B[3 - t], y1);
#pragma unroll
          for (int t = 0; t <= 4; ++t) y2 = MFMA16(A[t], B[4 - t], y2);
#pragma unroll
          for (int t = 0; t <= 7; ++t) y3 = MFMA16(A[t], B[7 - t], y3);
        } else {
#pragma unroll
          for (int t = 0; t <= 1; ++t) y0 = MFMA16(A[t], B[1 - t], y0);
#pragma unroll
          for (int t = 0; t <= 2; ++t) y1 = MFMA16(A[t], B[2 - t], y1);
#pragma unroll
          for (int t = 0; t <= 5; ++t) y2 = MFMA16(A[t], B[5 - t], y2);
#pragma unroll
          for (int t = 0; t <= 6; ++t) y3 = MFMA16(A[t], B[6 - t], y3);
        }
        ba += 64; aa += 64;
      }
      char* yb = ws + WS_YT + (ch0 + ch) * 4096; const unsigned yo = 32 * r16 + 8 * g;
      gst8_sc1(yb, yo + 512 * (cs ? 1 : 0), (u32x2){min(cvtpk(y0[0], y0[1]), SENT - 1), min(cvtpk(y0[2], y0[3]), SENT - 1)});
      gst8_sc1(yb, yo + 512 * (cs ? 2 : 3), (u32x2){min(cvtpk(y1[0], y1[1]), SENT - 1), min(cvtpk(y1[2], y1[3]), SENT - 1)});
      gst8_sc1(yb, yo + 512 * (cs ? 5 : 4), (u32x2){min(cvtpk(y2[0], y2[1]), SENT - 1), min(cvtpk(y2[2], y2[3]), SENT - 1)});
      gst8_sc1(yb, yo + 512 * (cs ? 6 : 7), (u32x2){min(cvtpk(y3[0], y3[1]), SENT - 1), min(cvtpk(y3[2], y3[3]), SENT - 1)});
    }
    TS(11);
    WAITV(0);
    BAR();
    TS(12);
#pragma unroll
    for (int j = 0; j < 4; ++j)
      dma16x4v(wo + (128 * a + 32 * j) * 512 + 64 * hg, wvo, wvo + 7168, wvo + 2 * 7168, wvo + 3 * 7168, (a >> 1) * 65536 + (a & 1) * 16384 + 4096 * j);
    {
      u32x4 u0, u1, d0, d1, ya, yb;
      const unsigned uo = (row0 + 16 * a + r16) * 1024 + 128 * hg + 16 * g;
      gld16<0>(u0, u, uo); gld16<64>(u1, u, uo); gld16<0>(d0, Dsk, 128 * hg + 16 * g); gld16<64>(d1, Dsk, 128 * hg + 16 * g);
      const char* ys = ws + WS_YT + (64 * hg) * 4096 + (row0 + 16 * a) * 2; const unsigned yo = (lane >> 1) * 4096 + 16 * (lane & 1);
      _Pragma("unroll 1") for (unsigned spins = 0; spins < (1u << 20); ++spins) {
        gld16_sc1<0>(ya, ys, yo); gld16_sc1<0>(yb, ys + 32 * 4096, yo);
        asm volatile("s_waitcnt vmcnt(0)" : "+v"(ya), "+v"(yb) :: "memory");
        if (!__any(max(max4(ya), max4(yb)) == SENT)) break;
        __builtin_amdgcn_s_sleep(4);
      }
      asm volatile("" : "+v"(u0), "+v"(u1), "+v"(d0), "+v"(d1) :: "memory");
      const unsigned sc = R2 + 2048 * a;
      *LP(u32x4, sc + l16) = ya; *LP(u32x4, sc + 1024 + l16) = yb;
      const unsigned ta = sc + 256 * g + 8 * r16;
      const u32x4 t0 = __builtin_bit_cast(u32x4, trd2(ta, ta + 128)), t1 = __builtin_bit_cast(u32x4, trd2(ta + 1024, ta + 1152));
      u32x4 o0, o1;
#pragma unroll
      for (int i = 0; i < 4; ++i) {
        const unsigned s0 = cvtpk(bflo(u0[i]) * bflo(d0[i]), bfhi(u0[i]) * bfhi(d0[i])), s1 = cvtpk(bflo(u1[i]) * bflo(d1[i]), bfhi(u1[i]) * bfhi(d1[i]));
        o0[i] = cvtpk(bflo(t0[i]) + bflo(s0), bfhi(t0[i]) + bfhi(s0)); o1[i] = cvtpk(bflo(t1[i]) + bflo(s1), bfhi(t1[i]) + bfhi(s1));
      }
      const unsigned oo = (row0 + 16 * a + r16) * 2048 + 128 * hg + 16 * g;
      gst16(out, oo, o0); gst16(out, oo + 64, o1);
    }
    TS(13);
    WAITV(0);
    TS_FLUSH();
    return;
  }

  const unsigned slice = R2 + 4096 * w;
  u32x4 xk0, xk1;
  { const unsigned vo = (row0 + 8 * hg + (r16 & 7)) * 1024 + 128 * w + 16 * g;
    gld16<0>(xk0, x, vo); gld16<64>(xk1, x, vo); }
  dma16x2(wk + 1024 * w, l16, slice); dma16x2(wv + 1024 * w, l16, slice + 2048);
  unsigned p_ar, p_ai, p_br, p_bi, p_cr, p_ci, p_dt;
  { const unsigned t = tid & 255, d = ch0 + (t >> 7), vo = (d * 128 + (t & 127)) * 2;
    gld2(p_ar, Are, vo); gld2(p_ai, Aim, vo); gld2(p_br, Bre, vo); gld2(p_bi, Bim, vo); gld2(p_cr, Cre, vo); gld2(p_ci, Cim, vo); gld2(p_dt, logdt, d * 2); }
  asm volatile("s_waitcnt vmcnt(7)" : "+v"(xk0), "+v"(xk1) :: "memory");
  TS(2);
  {
    const unsigned ta = slice + 256 * g + 8 * r16;
    bf16x8 wk0 = trd2(ta, ta + 128), wk1 = trd2(ta + 1024, ta + 1152), wv0 = trd2(ta + 2048, ta + 2176), wv1 = trd2(ta + 3072, ta + 3200);
    f32x4 dk = {0, 0, 0, 0}, dv = {0, 0, 0, 0};
    dk = MFMA16(wk0, AS8(xk0), dk); dv = MFMA16(AS8(xk0), wv0, dv);
    dk = MFMA16(wk1, AS8(xk1), dk); dv = MFMA16(AS8(xk1), wv1, dv);
    PIN(dk); PIN(dv);
    *LP(f32x4, slice + l16) = dk; *LP(f32x4, slice + 1024 + l16) = dv;
  }
  asm volatile("s_waitcnt vmcnt(0)" : "+v"(p_ar), "+v"(p_ai), "+v"(p_br), "+v"(p_bi), "+v"(p_cr), "+v"(p_ci), "+v"(p_dt) :: "memory");
  if (w < 4) {
    const float dt = bflo(cvtpk(expf(bflo(p_dt)), 0.f));
    const float al = (dt * bflo(p_ar)) * 1.4426950408889634f;
    float be = (dt * bflo(p_ai)) * 0.15915494309189535f; be -= __builtin_rintf(be);
    const float br = dt * bflo(p_br), bi = dt * bflo(p_bi), cr = bflo(p_cr), ci = bflo(p_ci);
    const float xr = cr * br - ci * bi, xi = cr * bi + ci * br;
    *LP(f32x4, slice + 2048 + l16) = (f32x4){al, be, xr, xi};
    const float s = col_sum(row_sum(xr));
    if (lane == 0) *LP(float, slice + 3072) = s;
  }
  BAR();
  TS(3);
  f32x4 kacc[6];
  {
    const float eB = (float)(16 * r16), e1 = (float)(r16 + 1);
    const float e2 = (float)(r16 < 8 ? 258 + 2 * r16 : 516 + 256 * ((r16 - 8) >> 2) + 4 * (r16 & 3));
    const float e3 = (float)(1032 + 256 * ((r16 & 7) >> 1) + 8 * (r16 & 1));
    unsigned pa = R2 + 4096 * (w >> 2) + 2048 + 16 * ((16 * w + 4 * g) & 63); PIN(pa);
    u32x4 fb[2], f1[2], f2[2], f3;
#pragma unroll
    for (int c = 0; c < 2; ++c)
#pragma unroll
      for (int i = 0; i < 4; ++i) {
        const f32x4 P = *LP(f32x4, pa + 8192 * c + 16 * i);
        const f32x2 ab = {P[0], P[1]};
        const f32x2 wv_ = zpow<false>(ab, eB);
        fb[c][i] = cvtpk(P[2] * wv_.x - P[3] * wv_.y, P[2] * wv_.y + P[3] * wv_.x);
        const f32x2 a1 = zpow<false>(ab, e1); f1[c][i] = cvtpk(a1.x, -a1.y);
        const f32x2 a2 = zpow<true>(ab, e2); f2[c][i] = cvtpk(a2.x, -a2.y);
      }
    pa += 8192 * (r16 >> 3);
#pragma unroll
    for (int i = 0; i < 4; ++i) {
      const f32x2 ab = *LP(f32x2, pa + 16 * i);
      const f32x2 a3 = zpow<true>(ab, e3); f3[i] = cvtpk(a3.x, -a3.y);
    }
    const f32x4 z4 = {0, 0, 0, 0};
#pragma unroll
    for (int c = 0; c < 2; ++c) {
      kacc[3 * c + 0] = MFMA16(AS8(f1[c]), AS8(fb[c]), z4);
      kacc[3 * c + 1] = MFMA16(AS8(f2[c]), AS8(fb[c]), z4);
      kacc[3 * c + 2] = MFMA16(AS8(f3), AS8(fb[c]), z4);
    }
#pragma unroll
    for (int i = 0; i < 6; ++i) PIN(kacc[i]);
  }
  TS(5);
  BAR();
  TS(6);
  if (w < 4) {
    const int rh = w >> 1, hp = w & 1;
    const unsigned xa = R0 + 1024 * (32 * rh + r32) + 16 * (h ^ r16);
    const int q = r16 >> 2, p = lane & 3;
    unsigned wa = R1 + 128 * (8 * h + q) + 16 * ((4 * hp + 2 * (g & 1) + (p >> 1)) ^ ((q >> 1) << 2)) + 8 * (p & 1); PIN(wa);
    f32x16 acc = {0};
#pragma unroll
    for (int ks = 0; ks < 32; ++ks) {
      const bf16x8 b = *LP(bf16x8, (xa ^ ((ks & 7) << 5)) + 256 * (ks >> 3));
      const bf16x8 a = trd2(wa + 2048 * ks, wa + 2048 * ks + 512);
      acc = MFMA32(a, b, acc);
    }
    acc *= QSCALE;
    float n0 = 0, n1 = 0;
#pragma unroll
    for (int i = 0; i < 8; ++i) { n0 += acc[i] * acc[i]; n1 += acc[8 + i] * acc[8 + i]; }
    float o; n0 = x32(n0, o); n0 += o; n1 = x32(n1, o); n1 += o;
    n0 = __builtin_amdgcn_sqrtf(n0); n1 = __builtin_amdgcn_sqrtf(n1);
    const unsigned qs = R2 + 4096 * (4 + w);
    *LP(u32x4, qs + 2048 + l16) = (u32x4){cvtpk(acc[0], acc[1]), cvtpk(acc[2], acc[3]), cvtpk(acc[4], acc[5]), cvtpk(acc[6], acc[7])};
    *LP(u32x4, qs + 3072 + l16) = (u32x4){cvtpk(acc[8], acc[9]), cvtpk(acc[10], acc[11]), cvtpk(acc[12], acc[13]), cvtpk(acc[14], acc[15])};
    *LP(float, slice + 4 * lane) = n0; *LP(float, slice + 256 + 4 * lane) = n1;
    const float nm = col_max(row_max(fmaxf(n0, n1)));
    if (lane == 0) *LP(float, slice + 1024) = nm;
  } else if (w == 4 && lane < 12) {
    const unsigned j = lane >> 2, o1 = j ? 0u : 0x3f803f80u;
    *LP(u32x4, ONES + 512 * (lane & 3) + (j == 2 ? 64 : 16 * j)) = (u32x4){o1, o1, o1, o1};
  }
  TS(7);
  BAR();
  TS(8);
  const int qt = w & 3, hs = w >> 2;
  const bf16x8 qf = *LP(bf16x8, R2 + 4096 * (4 + qt) + 2048 + 1024 * hs + l16);
  const float kmax = *LP(float, R2 + 1028);
  float sh = *LP(float, R2 + 4096 * qt + 256 * hs + 4 * lane) * kmax;
  const float qmax = fmaxf(fmaxf(*LP(float, R2 + 1024), *LP(float, R2 + 4096 + 1024)), fmaxf(*LP(float, R2 + 8192 + 1024), *LP(float, R2 + 12288 + 1024)));
  const bool unsafe = (unsigned)__builtin_amdgcn_readfirstlane(__float_as_int(qmax * kmax)) > __float_as_uint(SAFE_BOUND);
  {
    const unsigned pb = (w >> 2) * R1 + PART + (w & 3) * 6144 + l16;
#pragma unroll
    for (int i = 0; i < 6; ++i) *LP(f32x4, pb + 1024 * i) = kacc[i];
  }
  BAR();
  TS(9);
  if (unsafe) {
    BAR(); BAR();
    const f32x16 z = {0};
    float m = -INFINITY;
    for (int t = 0; t < 64; ++t) {
      const f32x16 s = MFMA32(*LP(bf16x8, R0 + 1024 * t + l16), qf, z);
#pragma unroll
      for (int i = 0; i < 16; ++i) m = fmaxf(m, s[i]);
    }
    float o; m = x32(m, o); sh = fmaxf(m, o);
  }
  f32x16 oacc = {0};
  {
    f32x16 mneg;
#pragma unroll
    for (int i = 0; i < 16; ++i) mneg[i] = -sh;
    PIN(mneg);
    const bool lo = (lane & 16) == 0;
    unsigned kp = R0 + l16;
    unsigned vp = lo ? R1 + 256 * h + 16 * r16 : ONES + (r32 == 16 ? 0 : (r32 >= 20 && r32 < 28) ? 64 : 16);
    const unsigned vinc = lo ? 2048 : 0;
    const int tb = unsafe ? -1 : 4;
    f32x16 sA, sB; u32x4 pA0, pA1, pB0 = {0, 0, 0, 0}, pB1;
    bf16x8 kf = *LP(bf16x8, kp), v0, v1;
    sA = MFMA32(kf, qf, mneg);
    kf = *LP(bf16x8, kp + 1024);
    v0 = *LP(bf16x8, vp); v1 = v0;
#pragma unroll
    for (int i = 0; i < 16; ++i) sB[i] = 0.f;
#define CV(P, j, S, i) P[j] = cvtpk(S[i], S[i + 1])
#define E4(S, i) do { S[i] = EX2(S[i]); S[i + 1] = EX2(S[i + 1]); S[i + 2] = EX2(S[i + 2]); S[i + 3] = EX2(S[i + 3]); \
                      asm volatile("" : "+v"(S[i]), "+v"(S[i + 1]), "+v"(S[i + 2]), "+v"(S[i + 3])); SBAR(); } while (0)
#define UNIT(S, P0, T, Q0, Q1, KOFF, VOFF) do { \
      E4(S, 0); CV(Q1, 0, T, 8); CV(Q1, 1, T, 10); CV(Q1, 2, T, 12); CV(Q1, 3, T, 14); PIN(Q1); SBAR(); \
      T = MFMA32(kf, qf, mneg); kf = *LP(bf16x8, kp + KOFF); SBAR(); E4(S, 4); \
      oacc = MFMA32(v0, AS8(Q0), oacc); v0 = *LP(bf16x8, vp + VOFF); SBAR(); E4(S, 8); \
      oacc = MFMA32(v1, AS8(Q1), oacc); v1 = *LP(bf16x8, vp + VOFF + 512); SBAR(); \
      CV(P0, 0, S, 0); CV(P0, 1, S, 2); CV(P0, 2, S, 4); CV(P0, 3, S, 6); PIN(P0); SBAR(); E4(S, 12); } while (0)
    for (int t = 0; t < 64; t += 4) {
      if (t == tb || t == 32) BAR();
      UNIT(sA, pA0, sB, pB0, pB1, 2048, 0);
      UNIT(sB, pB0, sA, pA0, pA1, 3072, 1024);
      vp += vinc;
      UNIT(sA, pA0, sB, pB0, pB1, 4096, 0);
      UNIT(sB, pB0, sA, pA0, pA1, 5120, 1024);
      kp += 4096; vp += vinc;
    }
    CV(pB1, 0, sB, 8); CV(pB1, 1, sB, 10); CV(pB1, 2, sB, 12); CV(pB1, 3, sB, 14);
    oacc = MFMA32(v0, AS8(pB0), oacc); oacc = MFMA32(v1, AS8(pB1), oacc);
  }
  TS(10);
  {
    const float rl = __builtin_amdgcn_rcpf(oacc[8]);
    float o; const float r0 = x32(rl, o);
    u32x4 of_ = {cvtpk(oacc[0] * r0, oacc[1] * r0), cvtpk(oacc[2] * r0, oacc[3] * r0), cvtpk(oacc[4] * r0, oacc[5] * r0), cvtpk(oacc[6] * r0, oacc[7] * r0)};
#pragma unroll
    for (int i = 0; i < 4; ++i) of_[i] = min(of_[i], SENT - 1);
    gst16_sc1<0>(ws + WS_O + ((rg * 2 + (qt >> 1)) * 32 + 4 * hg + 2 * (qt & 1) + hs) * 1024, l16, of_);
  }
  BAR();
  TS(11);
  {
    const int rh = w >> 2, kq = w & 3;
    const char* osrc = ws + WS_O + ((rg * 2 + rh) * 32 + 8 * kq) * 1024;
    _Pragma("unroll 1") for (unsigned spins = 0; spins < (1u << 20); ++spins) {
      unsigned t; gld4_sc1<1020>(t, osrc, (lane & 7) * 1024);
      asm volatile("s_waitcnt vmcnt(0)" : "+v"(t) :: "memory");
      if (!__any(t == SENT)) break;
    }
    TS(12);
    u32x4 of[8];
    _Pragma("unroll 1") for (unsigned spins = 0; spins < (1u << 20); ++spins) {
      gld16_sc1<0>(of[0], osrc, l16); gld16_sc1<1024>(of[1], osrc, l16); gld16_sc1<2048>(of[2], osrc, l16); gld16_sc1<3072>(of[3], osrc, l16);
      gld16_sc1<0>(of[4], osrc + 4096, l16); gld16_sc1<1024>(of[5], osrc + 4096, l16); gld16_sc1<2048>(of[6], osrc + 4096, l16); gld16_sc1<3072>(of[7], osrc + 4096, l16);
      asm volatile("s_waitcnt vmcnt(0)" : "+v"(of[0]), "+v"(of[1]), "+v"(of[2]), "+v"(of[3]), "+v"(of[4]), "+v"(of[5]), "+v"(of[6]), "+v"(of[7]) :: "memory");
      unsigned mx = 0;
#pragma unroll
      for (int i = 0; i < 8; ++i) mx = max(mx, max4(of[i]));
      if (!__any(mx == SENT)) break;
    }
    TS(13);
    const int q = r16 >> 2, p = lane & 3;
    unsigned wa = (kq >> 1) * R1 + (kq & 1) * 16384 + 128 * (4 * h + q) + 16 * ((2 * (g & 1) + (p >> 1)) ^ ((q >> 1) << 2)) + 8 * (p & 1); PIN(wa);
    const unsigned wb = wa ^ 64;
    f32x16 c0 = {0}, c1 = {0};
#pragma unroll
    for (int ks = 0; ks < 8; ++ks) {
      c0 = MFMA32(trd2(wa + 2048 * ks, wa + 2048 * ks + 1024), AS8(of[ks]), c0);
      c1 = MFMA32(trd2(wb + 2048 * ks, wb + 2048 * ks + 1024), AS8(of[ks]), c1);
    }
    const unsigned rbase = 32768 + rh * R1;
    const f32x4 G[8] = {{c0[0], c0[1], c0[2], c0[3]}, {c0[4], c0[5], c0[6], c0[7]}, {c0[8], c0[9], c0[10], c0[11]}, {c0[12], c0[13], c0[14], c0[15]},
                        {c1[0], c1[1], c1[2], c1[3]}, {c1[4], c1[5], c1[6], c1[7]}, {c1[8], c1[9], c1[10], c1[11]}, {c1[12], c1[13], c1[14], c1[15]}};
    unsigned wr = rbase + 2048 * kq + l16; PIN(wr);
#pragma unroll
    for (int j = 0; j < 4; ++j) { *LP(f32x4, wr + 8192 * j) = G[2 * j]; *LP(f32x4, wr + 8192 * j + 1024) = G[2 * j + 1]; }
    BAR();
    unsigned rd = rbase + 8192 * kq + l16; PIN(rd);
    f32x4 a = *LP(f32x4, rd), b = *LP(f32x4, rd + 1024);
#pragma unroll
    for (int s = 1; s < 4; ++s) { a += *LP(f32x4, rd + 2048 * s); b += *LP(f32x4, rd + 2048 * s + 1024); }
    auto s0 = __builtin_amdgcn_permlane32_swap(cvtpk(a[0], a[1]), cvtpk(b[0], b[1]), false, false);
    auto s1 = __builtin_amdgcn_permlane32_swap(cvtpk(a[2], a[3]), cvtpk(b[2], b[3]), false, false);
    gst16(out, (row0 + 32 * rh + r32) * 2048 + 1024 + 128 * hg + 32 * kq + 16 * h, (u32x4){s0[0], s1[0], s0[1], s1[1]});
  }
  TS_FLUSH();
}

extern "C" void kernel_launch(void* const* d_in, const int* in_sizes, int n_in, void* d_out, int out_size, void* d_ws, size_t ws_size, hipStream_t stream) {
  (void)in_sizes; (void)n_in; (void)out_size; (void)ws_size;
  (void)hipFuncSetAttribute((const void*)fused_kernel, hipFuncAttributeMaxDynamicSharedMemorySize, LDS_BYTES);
  fused_kernel<<<256, 768, LDS_BYTES, stream>>>((const u16*)d_in[0], (const u16*)d_in[1], (const u16*)d_in[2], (const u16*)d_in[3], (const u16*)d_in[4],
      (const u16*)d_in[5], (const u16*)d_in[6], (const u16*)d_in[7], (const u16*)d_in[8], (const u16*)d_in[9], (const u16*)d_in[10], (const u16*)d_in[11],
      (const u16*)d_in[12], (const u16*)d_in[13], (u16*)d_out, (char*)d_ws);
}
```

```cpp
#include <hip/hip_runtime.h>
#include <hip/hip_bf16.h>
#pragma clang diagnostic ignored "-Wint-to-pointer-cast"

typedef unsigned short u16;
typedef unsigned long long u64;
typedef __attribute__((ext_vector_type(8))) short bf16x8;
typedef __attribute__((ext_vector_type(4))) short s16x4;
typedef __attribute__((ext_vector_type(16))) float f32x16;
typedef __attribute__((ext_vector_type(4))) float f32x4;
typedef __attribute__((ext_vector_type(2))) float f32x2;
typedef __attribute__((ext_vector_type(4))) unsigned u32x4;
typedef __attribute__((ext_vector_type(2))) unsigned u32x2;
typedef __attribute__((ext_vector_type(2))) __bf16 bf16x2;
#define LAS __attribute__((address_space(3)))
#define LP(T, off) ((LAS T*)(unsigned)(off))
#define SBAR() __builtin_amdgcn_sched_barrier(0)
#define PIN(x) asm volatile("" : "+v"(x))
#define MFMA32(a, b, c) __builtin_amdgcn_mfma_f32_32x32x16_bf16(a, b, c, 0, 0, 0)
#define MFMA16(a, b, c) __builtin_amdgcn_mfma_f32_16x16x32_bf16(a, b, c, 0, 0, 0)
#define AS8(x) __builtin_bit_cast(bf16x8, x)
#define EX2(v) __builtin_amdgcn_exp2f(v)
#define BAR() asm volatile("s_waitcnt lgkmcnt(0)\n\ts_barrier" ::: "memory")
#define WAITV(n) asm volatile("s_waitcnt vmcnt(" #n ")" ::: "memory")

#define STAMP 0
#define TS(i) do {} while (0)
#define TSR(i) do {} while (0)
#define TS_FLUSH() do {} while (0)

constexpr unsigned WS_EPOCH = 0, WS_FLAG1 = 32768, WS_FLAG2 = 65536, WS_FLAG3 = 98304, WS_K = 131072, WS_V = WS_K + 65536, WS_O = 1u << 18, WS_UT = WS_O + (2u << 20), WS_YT = WS_UT + (2u << 20), WS_DBG = 8u << 20;
constexpr unsigned R0 = 0, R1 = 65536, R2 = 131072, LDS_BYTES = 163840;
constexpr unsigned KA = R2, KA_STRIDE = 4608;
constexpr unsigned UC = R2 + 9216, UC_STRIDE = 4160;
constexpr unsigned ONES = R2 + 28672;
constexpr unsigned FL = ONES + 128;
constexpr unsigned PART = 40960;
constexpr unsigned YS = R2, US = R2 + 8192;
constexpr float QSCALE = 0.25f * 1.4426950408889634f;
constexpr float SAFE_BOUND = 40.f;
constexpr unsigned SENT = 0xffffffffu;
#define TOUCH_WQ 1
#define AUX_DELAY 8
#define AUX_DELAY2 4
#define CONV_SLEEP 1
constexpr int CODE_KB = 15;

__device__ __forceinline__ unsigned cvtpk(float lo, float hi) { f32x2 v = {lo, hi}; return __builtin_bit_cast(unsigned, __builtin_convertvector(v, bf16x2)); }
__device__ __forceinline__ float bflo(unsigned x) { return __uint_as_float(x << 16); }
__device__ __forceinline__ float bfhi(unsigned x) { return __uint_as_float(x & 0xffff0000u); }
template <int C> __device__ __forceinline__ float dpp(float v) { return __int_as_float(__builtin_amdgcn_update_dpp(0, __float_as_int(v), C, 0xf, 0xf, true)); }
__device__ __forceinline__ float x16(float v, float& o) { auto r = __builtin_amdgcn_permlane16_swap(__float_as_uint(v), __float_as_uint(v), false, false); o = __uint_as_float(r[1]); return __uint_as_float(r[0]); }
__device__ __forceinline__ float x32(float v, float& o) { auto r = __builtin_amdgcn_permlane32_swap(__float_as_uint(v), __float_as_uint(v), false, false); o = __uint_as_float(r[1]); return __uint_as_float(r[0]); }
__device__ __forceinline__ float row_sum(float v) { v += dpp<0x128>(v); v += dpp<0x124>(v); v += dpp<0x122>(v); v += dpp<0x121>(v); return v; }
__device__ __forceinline__ float row_max(float v) { v = fmaxf(v, dpp<0x128>(v)); v = fmaxf(v, dpp<0x124>(v)); v = fmaxf(v, dpp<0x122>(v)); v = fmaxf(v, dpp<0x121>(v)); return v; }
__device__ __forceinline__ float col_sum(float v) { float o; v = x16(v, o); v += o; v = x32(v, o); return v + o; }
__device__ __forceinline__ float col_max(float v) { float o; v = x16(v, o); v = fmaxf(v, o); v = x32(v, o); return fmaxf(v, o); }

template <int C> __device__ __forceinline__ unsigned dppu(unsigned v) { return (unsigned)__builtin_amdgcn_update_dpp(0, (int)v, C, 0xf, 0xf, true); }
__device__ __forceinline__ unsigned wave_umax(unsigned v) {
  v = max(v, dppu<0x128>(v)); v = max(v, dppu<0x124>(v)); v = max(v, dppu<0x122>(v)); v = max(v, dppu<0x121>(v));
  auto r = __builtin_amdgcn_permlane16_swap(v, v, false, false); v = max(r[0], r[1]);
  r = __builtin_amdgcn_permlane32_swap(v, v, false, false); return max(r[0], r[1]);
}
__device__ __forceinline__ void dma16(const void* base, unsigned voff, unsigned ldst) {
  asm volatile("s_mov_b32 m0, %2\n\ts_nop 0\n\tglobal_load_lds_dwordx4 %0, %1" :: "v"(voff), "s"(base), "s"(ldst) : "memory");
}
__device__ __forceinline__ void dma16_sc1(const void* base, unsigned voff, unsigned ldst) {
  asm volatile("s_mov_b32 m0, %2\n\ts_nop 0\n\tglobal_load_lds_dwordx4 %0, %1 sc1" :: "v"(voff), "s"(base), "s"(ldst) : "memory");
}
__device__ __forceinline__ void dma16x2(const void* base, unsigned voff, unsigned ldst) {
  asm volatile("s_mov_b32 m0, %2\n\ts_nop 0\n\tglobal_load_lds_dwordx4 %0, %1\n\tglobal_load_lds_dwordx4 %0, %1 offset:1024" :: "v"(voff), "s"(base), "s"(ldst) : "memory");
}
__device__ __forceinline__ void dma16x4v(const void* base, unsigned v0, unsigned v1, unsigned v2, unsigned v3, unsigned ldst) {
  asm volatile("s_mov_b32 m0, %5\n\ts_nop 0\n\tglobal_load_lds_dwordx4 %0, %4\n\tglobal_load_lds_dwordx4 %1, %4 offset:1024\n\t"
               "global_load_lds_dwordx4 %2, %4 offset:2048\n\tglobal_load_lds_dwordx4 %3, %4 offset:3072" :: "v"(v0), "v"(v1), "v"(v2), "v"(v3), "s"(base), "s"(ldst) : "memory");
}
__device__ __forceinline__ void dma16x4_sc1(const void* base, unsigned voff, unsigned ldst) {
  asm volatile("s_mov_b32 m0, %2\n\ts_nop 0\n\tglobal_load_lds_dwordx4 %0, %1 sc1\n\tglobal_load_lds_dwordx4 %0, %1 offset:1024 sc1\n\t"
               "global_load_lds_dwordx4 %0, %1 offset:2048 sc1\n\tglobal_load_lds_dwordx4 %0, %1 offset:3072 sc1" :: "v"(voff), "s"(base), "s"(ldst) : "memory");
}
template <int OFF> __device__ __forceinline__ void gld16(u32x4& d, const void* base, unsigned voff) { asm volatile("global_load_dwordx4 %0, %1, %2 offset:%3" : "=v"(d) : "v"(voff), "s"(base), "i"(OFF) : "memory"); }
template <int OFF> __device__ __forceinline__ void gld16_sc1(u32x4& d, const void* base, unsigned voff) { asm volatile("global_load_dwordx4 %0, %1, %2 offset:%3 sc1" : "=v"(d) : "v"(voff), "s"(base), "i"(OFF) : "memory"); }
template <int OFF> __device__ __forceinline__ void gld4_sc1(unsigned& d, const void* base, unsigned voff) { asm volatile("global_load_dword %0, %1, %2 offset:%3 sc1" : "=v"(d) : "v"(voff), "s"(base), "i"(OFF) : "memory"); }
__device__ __forceinline__ void gld2(unsigned& d, const void* base, unsigned voff) { asm volatile("global_load_ushort %0, %1, %2" : "=v"(d) : "v"(voff), "s"(base) : "memory"); }
__device__ __forceinline__ void gld8_sc1(u32x2& d, const void* base, unsigned voff) { asm volatile("global_load_dwordx2 %0, %1, %2 sc1" : "=v"(d) : "v"(voff), "s"(base) : "memory"); }
template <int OFF> __device__ __forceinline__ void gst16_sc1(void* base, unsigned voff, u32x4 d) { asm volatile("global_store_dwordx4 %0, %1, %2 offset:%3 sc1\n\ts_nop 1" :: "v"(voff), "v"(d), "s"(base), "i"(OFF) : "memory"); }
__device__ __forceinline__ void gst16(void* base, unsigned voff, u32x4 d) { asm volatile("global_store_dwordx4 %0, %1, %2\n\ts_nop 1" :: "v"(voff), "v"(d), "s"(base) : "memory"); }
__device__ __forceinline__ void gst8_sc1(void* base, unsigned voff, u32x2 d) { asm volatile("global_store_dwordx2 %0, %1, %2 sc1" :: "v"(voff), "v"(d), "s"(base) : "memory"); }
__device__ __forceinline__ void gst4_sc1(void* base, unsigned voff, unsigned d) { asm volatile("global_store_dword %0, %1, %2 sc1" :: "v"(voff), "v"(d), "s"(base) : "memory"); }
__device__ __forceinline__ s16x4 trd(unsigned a) { return __builtin_bit_cast(s16x4, __builtin_amdgcn_ds_read_tr16_b64_v4i16(LP(s16x4, a))); }
__device__ __forceinline__ bf16x8 trd2(unsigned a0, unsigned a1) { s16x4 x = trd(a0), y = trd(a1); return (bf16x8){x[0], x[1], x[2], x[3], y[0], y[1], y[2], y[3]}; }
__device__ __forceinline__ unsigned max4(u32x4 v) { return max(max(v[0], v[1]), max(v[2], v[3])); }

__device__ __forceinline__ void touch(const void* base, unsigned voff, unsigned& d) { asm volatile("global_load_dword %0, %1, %2" : "=v"(d) : "v"(voff), "s"(base) : "memory"); }
__device__ __forceinline__ void put_flag(char* f, int bid, int lane, u64 tag, unsigned val) {
  const unsigned lo = (unsigned)tag, hi = (unsigned)(tag >> 32);
  if (lane < 2) gst8_sc1(f, 128 * bid + 8 * lane, lane ? (u32x2){hi, ~lo} : (u32x2){lo, val});
}
__device__ __forceinline__ unsigned poll_flags(const char* f, int lane, u64 tag) {
  const unsigned lo = (unsigned)tag, hi = (unsigned)(tag >> 32);
  unsigned mx = 0; bool done = false;
  _Pragma("unroll 1") for (unsigned spins = 0; spins < (1u << 20); ++spins) {
    if (!done) {
      u32x4 f0, f1, f2, f3;
      asm volatile("global_load_dwordx4 %0, %4, %5 sc1\n\tglobal_load_dwordx4 %1, %4, %6 sc1\n\tglobal_load_dwordx4 %2, %4, %7 sc1\n\tglobal_load_dwordx4 %3, %4, %8 sc1\n\ts_waitcnt vmcnt(0)"
                   : "=&v"(f0), "=&v"(f1), "=&v"(f2), "=&v"(f3) : "v"(128 * lane), "s"(f), "s"(f + 8192), "s"(f + 16384), "s"(f + 24576) : "memory");
      done = ((f0[0] ^ lo) | (f0[2] ^ hi) | (f0[3] ^ ~lo) | (f1[0] ^ lo) | (f1[2] ^ hi) | (f1[3] ^ ~lo) | (f2[0] ^ lo) | (f2[2] ^ hi) | (f2[3] ^ ~lo) | (f3[0] ^ lo) | (f3[2] ^ hi) | (f3[3] ^ ~lo)) == 0;
      mx = max(max(f0[1], f1[1]), max(f2[1], f3[1]));
    }
    if (!__any(!done)) break;
  }
  return mx;
}
__device__ __forceinline__ void lds_arrive(unsigned addr, int lane) { asm volatile("s_waitcnt lgkmcnt(0)" ::: "memory"); if (lane == 0) __hip_atomic_fetch_add(LP(unsigned, addr), 1u, __ATOMIC_RELAXED, __HIP_MEMORY_SCOPE_WORKGROUP); asm volatile("" ::: "memory"); }
__device__ __forceinline__ void lds_wait(unsigned addr, unsigned target) {
  while ((unsigned)__builtin_amdgcn_readfirstlane(__hip_atomic_load(LP(unsigned, addr), __ATOMIC_RELAXED, __HIP_MEMORY_SCOPE_WORKGROUP)) < target) __builtin_amdgcn_s_sleep(2);
  asm volatile("" ::: "memory");
}
__device__ __forceinline__ f32x2 cmul(f32x2 c, f32x2 w) {
  f32x2 t, r;
  asm("v_pk_mul_f32 %0, %1, %2 op_sel_hi:[0,1]" : "=v"(t) : "v"(c), "v"(w));
  asm("v_pk_fma_f32 %0, %1, %2, %3 op_sel:[1,1,0] op_sel_hi:[1,0,1] neg_lo:[1,0,0]" : "=v"(r) : "v"(c), "v"(w), "v"(t));
  return r;
}
template <bool FR> __device__ __forceinline__ f32x2 zpow(f32x2 ab, f32x2 e) {
  f32x2 t = ab * e;
  float m = EX2(t.x);
  float a = FR ? __builtin_amdgcn_fractf(t.y) : t.y;
  f32x2 cs = {__builtin_amdgcn_cosf(a), __builtin_amdgcn_sinf(a)};
  return cs * m;
}

#define ATT_LOOP \
  "10:\n\tds_read_b32 %9, %16 offset:0\n\ts_waitcnt lgkmcnt(0)\n\tv_readfirstlane_b32 %12, %9\n\ts_nop 0\n\ts_cmp_ge_u32 %12, 4\n\ts_cbranch_scc0 10b\n\t" \
  "ds_read_b128 %3, %1\n\tds_read_b128 %4, %1 offset:1024\n\tds_read_b128 %7, %2\n\tds_read_b128 %8, %2 offset:512\n\tv_mov_b32 v120, 0\n\tv_mov_b32 v121, 0\n\t" \
  "v_mov_b32 v122, 0\n\tv_mov_b32 v123, 0\n\tv_mov_b32 v124, 0\n\tv_mov_b32 v125, 0\n\tv_mov_b32 v126, 0\n\tv_mov_b32 v127, 0\n\tv_mov_b32 v136, 0\n\tv_mov_b32 v137, 0\n\t" \
  "v_mov_b32 v138, 0\n\tv_mov_b32 v139, 0\n\ts_mov_b32 %10, 1\n\ts_mov_b32 %11, 0\n\ts_waitcnt lgkmcnt(3)\n\tv_mfma_f32_32x32x16_bf16 v[96:111], %3, %13, %14\n\t" \
  "s_nop 11\n\t.p2align 6\n\t1:\n\ts_setprio 1\n\tv_exp_f32 v96, v96\n\tv_exp_f32 v97, v97\n\tv_exp_f32 v98, v98\n\tv_exp_f32 v99, v99\n\tv_cvt_pk_bf16_f32 v140, v120, v121\n\t" \
  "v_cvt_pk_bf16_f32 v141, v122, v123\n\tv_cvt_pk_bf16_f32 v142, v124, v125\n\tv_cvt_pk_bf16_f32 v143, v126, v127\n\ts_waitcnt lgkmcnt(0)\n\tv_mfma_f32_32x32x16_bf16 v[112:127], %4, %13, %14\n\t" \
  "ds_read_b128 %3, %1 offset:2048\n\tds_read_b128 %5, %2 offset:0\n\tds_read_b128 %6, %2 offset:512\n\tv_exp_f32 v100, v100\n\tv_exp_f32 v101, v101\n\t" \
  "v_exp_f32 v102, v102\n\tv_exp_f32 v103, v103\n\tv_mfma_f32_32x32x16_bf16 %0, %7, v[136:139], %0\n\tv_exp_f32 v104, v104\n\tv_exp_f32 v105, v105\n\tv_exp_f32 v106, v106\n\t" \
  "v_exp_f32 v107, v107\n\tv_mfma_f32_32x32x16_bf16 %0, %8, v[140:143], %0\n\tv_cvt_pk_bf16_f32 v128, v96, v97\n\tv_cvt_pk_bf16_f32 v129, v98, v99\n\tv_cvt_pk_bf16_f32 v130, v100, v101\n\t" \
  "v_cvt_pk_bf16_f32 v131, v102, v103\n\tv_exp_f32 v108, v108\n\tv_exp_f32 v109, v109\n\tv_exp_f32 v110, v110\n\tv_exp_f32 v111, v111\n\tv_exp_f32 v112, v112\n\t" \
  "v_exp_f32 v113, v113\n\tv_exp_f32 v114, v114\n\tv_exp_f32 v115, v115\n\tv_cvt_pk_bf16_f32 v132, v104, v105\n\tv_cvt_pk_bf16_f32 v133, v106, v107\n\t" \
  "v_cvt_pk_bf16_f32 v134, v108, v109\n\tv_cvt_pk_bf16_f32 v135, v110, v111\n\ts_waitcnt lgkmcnt(0)\n\tv_mfma_f32_32x32x16_bf16 v[96:111], %3, %13, %14\n\t" \
  "ds_read_b128 %4, %1 offset:3072\n\tds_read_b128 %7, %2 offset:1024\n\tds_read_b128 %8, %2 offset:1536\n\tv_exp_f32 v116, v116\n\tv_exp_f32 v117, v117\n\t" \
  "v_exp_f32 v118, v118\n\tv_exp_f32 v119, v119\n\tv_mfma_f32_32x32x16_bf16 %0, %5, v[128:131], %0\n\tv_add_u32 %2, %15, %2\n\tv_exp_f32 v120, v120\n\t" \
  "v_exp_f32 v121, v121\n\tv_exp_f32 v122, v122\n\tv_exp_f32 v123, v123\n\tv_mfma_f32_32x32x16_bf16 %0, %6, v[132:135], %0\n\tv_cvt_pk_bf16_f32 v136, v112, v113\n\t" \
  "v_cvt_pk_bf16_f32 v137, v114, v115\n\tv_cvt_pk_bf16_f32 v138, v116, v117\n\tv_cvt_pk_bf16_f32 v139, v118, v119\n\tv_exp_f32 v124, v124\n\tv_exp_f32 v125, v125\n\t" \
  "v_exp_f32 v126, v126\n\tv_exp_f32 v127, v127\n\tv_exp_f32 v96, v96\n\tv_exp_f32 v97, v97\n\tv_exp_f32 v98, v98\n\tv_exp_f32 v99, v99\n\tv_cvt_pk_bf16_f32 v140, v120, v121\n\t" \
  "v_cvt_pk_bf16_f32 v141, v122, v123\n\tv_cvt_pk_bf16_f32 v142, v124, v125\n\tv_cvt_pk_bf16_f32 v143, v126, v127\n\ts_waitcnt lgkmcnt(0)\n\tv_mfma_f32_32x32x16_bf16 v[112:127], %4, %13, %14\n\t" \
  "ds_read_b128 %3, %1 offset:4096\n\tds_read_b128 %5, %2 offset:0\n\tds_read_b128 %6, %2 offset:512\n\tv_exp_f32 v100, v100\n\tv_exp_f32 v101, v101\n\t" \
  "v_exp_f32 v102, v102\n\tv_exp_f32 v103, v103\n\tv_mfma_f32_32x32x16_bf16 %0, %7, v[136:139], %0\n\tv_exp_f32 v104, v104\n\tv_exp_f32 v105, v105\n\tv_exp_f32 v106, v106\n\t" \
  "v_exp_f32 v107, v107\n\tv_mfma_f32_32x32x16_bf16 %0, %8, v[140:143], %0\n\tv_cvt_pk_bf16_f32 v128, v96, v97\n\tv_cvt_pk_bf16_f32 v129, v98, v99\n\tv_cvt_pk_bf16_f32 v130, v100, v101\n\t" \
  "v_cvt_pk_bf16_f32 v131, v102, v103\n\tv_exp_f32 v108, v108\n\tv_exp_f32 v109, v109\n\tv_exp_f32 v110, v110\n\tv_exp_f32 v111, v111\n\tv_exp_f32 v112, v112\n\t" \
  "v_exp_f32 v113, v113\n\tv_exp_f32 v114, v114\n\tv_exp_f32 v115, v115\n\tv_cvt_pk_bf16_f32 v132, v104, v105\n\tv_cvt_pk_bf16_f32 v133, v106, v107\n\t" \
  "v_cvt_pk_bf16_f32 v134, v108, v109\n\tv_cvt_pk_bf16_f32 v135, v110, v111\n\ts_waitcnt lgkmcnt(0)\n\tv_mfma_f32_32x32x16_bf16 v[96:111], %3, %13, %14\n\t" \
  "ds_read_b128 %4, %1 offset:5120\n\tds_read_b128 %7, %2 offset:1024\n\tds_read_b128 %8, %2 offset:1536\n\tv_exp_f32 v116, v116\n\tv_exp_f32 v117, v117\n\t" \
  "v_exp_f32 v118, v118\n\tv_exp_f32 v119, v119\n\ts_setprio 0\n\tv_mfma_f32_32x32x16_bf16 %0, %5, v[128:131], %0\n\tv_add_u32 %2, %15, %2\n\tv_add_u32 %1, 0x1000, %1\n\t" \
  "v_exp_f32 v120, v120\n\tv_exp_f32 v121, v121\n\tv_exp_f32 v122, v122\n\tv_exp_f32 v123, v123\n\tv_mfma_f32_32x32x16_bf16 %0, %6, v[132:135], %0\n\tv_cvt_pk_bf16_f32 v136, v112, v113\n\t" \
  "v_cvt_pk_bf16_f32 v137, v114, v115\n\tv_cvt_pk_bf16_f32 v138, v116, v117\n\tv_cvt_pk_bf16_f32 v139, v118, v119\n\tv_exp_f32 v124, v124\n\tv_exp_f32 v125, v125\n\t" \
  "v_exp_f32 v126, v126\n\tv_exp_f32 v127, v127\n\ts_sub_u32 %10, %10, 1\n\ts_cmp_lg_u32 %10, 0\n\ts_cbranch_scc1 1b\n\ts_cmp_eq_u32 %11, 0\n\ts_cbranch_scc0 2f\n\t" \
  "11:\n\tds_read_b32 %9, %16 offset:4\n\ts_waitcnt lgkmcnt(0)\n\tv_readfirstlane_b32 %12, %9\n\ts_nop 0\n\ts_cmp_ge_u32 %12, 4\n\ts_cbranch_scc0 11b\n\t" \
  "s_mov_b32 %10, 8\n\ts_mov_b32 %11, 1\n\ts_branch 1b\n\t2:\n\ts_cmp_eq_u32 %11, 1\n\ts_cbranch_scc0 3f\n\t12:\n\tds_read_b32 %9, %16 offset:8\n\ts_waitcnt lgkmcnt(0)\n\t" \
  "v_readfirstlane_b32 %12, %9\n\ts_nop 0\n\ts_cmp_ge_u32 %12, 4\n\ts_cbranch_scc0 12b\n\tv_mov_b32 %9, 1\n\ts_mov_b32 %10, 7\n\ts_mov_b32 %11, 2\n\tds_add_u32 %16, %9 offset:12\n\t" \
  "s_branch 1b\n\t3:\n\ts_nop 0\n\tv_cvt_pk_bf16_f32 v140, v120, v121\n\tv_cvt_pk_bf16_f32 v141, v122, v123\n\tv_cvt_pk_bf16_f32 v142, v124, v125\n\tv_cvt_pk_bf16_f32 v143, v126, v127\n\t" \
  "s_waitcnt lgkmcnt(0)\n\tv_mfma_f32_32x32x16_bf16 %0, %7, v[136:139], %0\n\ts_nop 1\n\tv_mfma_f32_32x32x16_bf16 %0, %8, v[140:143], %0\n\ts_nop 15\n\t"
#define ATT_CLOB "v96", "v97", "v98", "v99", "v100", "v101", "v102", "v103", "v104", "v105", "v106", "v107", "v108", "v109", "v110", "v111", "v112", "v113", "v114", "v115", "v116", "v117", "v118", "v119", "v120", "v121", "v122", "v123", "v124", "v125", "v126", "v127", "v128", "v129", "v130", "v131", "v132", "v133", "v134", "v135", "v136", "v137", "v138", "v139", "v140", "v141", "v142", "v143"

__global__ void __launch_bounds__(768, 3)
fused_kernel(const u16* u_, const u16* Are_, const u16* Aim_, const u16* Bre_, const u16* Bim_, const u16* Cre_, const u16* Cim_, const u16* logdt_, const u16* Dsk_,
             const u16* x_, const u16* wq_, const u16* wk_, const u16* wv_, const u16* wo_, u16* out_, char* ws_) {
  typedef __attribute__((ext_vector_type(8))) u64 u64x8;
  u64x8 ka_, kb_;
  asm volatile("s_load_dwordx16 %0, %2, 0\n\ts_load_dwordx16 %1, %2, 64\n\ts_waitcnt lgkmcnt(0)" : "=&s"(ka_), "=&s"(kb_) : "s"(__builtin_amdgcn_kernarg_segment_ptr()) : "memory");
  const u16 *u = (const u16*)ka_[0], *Are = (const u16*)ka_[1], *Aim = (const u16*)ka_[2], *Bre = (const u16*)ka_[3], *Bim = (const u16*)ka_[4], *Cre = (const u16*)ka_[5], *Cim = (const u16*)ka_[6], *logdt = (const u16*)ka_[7];
  const u16 *Dsk = (const u16*)kb_[0], *x = (const u16*)kb_[1], *wq = (const u16*)kb_[2], *wk = (const u16*)kb_[3], *wv = (const u16*)kb_[4], *wo = (const u16*)kb_[5];
  u16* out = (u16*)kb_[6]; char* ws = (char*)kb_[7];
  u64 pc0; asm volatile("s_getpc_b64 %0" : "=s"(pc0));
  const int tid = threadIdx.x, lane = tid & 63;
  const int w = __builtin_amdgcn_readfirstlane(tid >> 6);
  const int bid = blockIdx.x, xcd = bid & 7, loc = bid >> 3;
  const int rg = xcd * 4 + (loc >> 3), hg = loc & 7, row0 = rg * 64, ch0 = xcd * 64 + loc * 2;
  const int r16 = lane & 15, g = lane >> 4, r32 = lane & 31, h = lane >> 5;
  const unsigned l16 = 16 * lane;
  const unsigned wvo = (lane >> 3) * 1024 + 16 * ((lane & 7) ^ (((lane >> 4) & 1) << 2));
  TSR(0); TS(1);

  if (w >= 8) {
    const int a = w - 8;
    u32x2 ep; gld8_sc1(ep, ws + WS_EPOCH, 0);
    unsigned t0_ = 0, t1_ = 0, t2_ = 0;
    if (a == 3) {
      touch((const void*)pc0, min(loc, CODE_KB - 1) * 1024 + l16, t0_);
      touch(wq + 64 * hg, (128 * (loc >> 3) + lane) * 1024, t1_); touch(wq + 64 * hg, (128 * (loc >> 3) + 64 + lane) * 1024, t2_);
    }
    auto dma_x8 = [&](int r0) {
      const u16* xb = x + (row0 + r0) * 512; const unsigned xv = l16 ^ (16 * (r0 & 8));
      dma16x4v(xb, xv, xv ^ 16, xv ^ 32, xv ^ 48, R0 + 1024 * r0); dma16x4v(xb + 2048, xv ^ 64, xv ^ 80, xv ^ 96, xv ^ 112, R0 + 1024 * r0 + 4096);
    };
    auto dma_w8 = [&](const u16* wm, int p0, unsigned dst) {
      const u16* wb = wm + p0 * 4096 + 64 * hg;
      dma16x4v(wb, wvo, wvo + 7168, wvo + 2 * 7168, wvo + 3 * 7168, dst); dma16x4v(wb + 16384, wvo, wvo + 7168, wvo + 2 * 7168, wvo + 3 * 7168, dst + 4096);
    };
    unsigned ko_, vo_;
    { const int kk0 = row0 + 8 * hg, t = kk0 >> 5, c = (kk0 >> 3) & 3;
      ko_ = WS_K + t * 1024 + ((g & 1) * 32 + 8 * c + r16) * 16 + 8 * (g >> 1); vo_ = WS_V + t * 1024 + 512 * (c >> 1) + 256 * g + 16 * r16 + 8 * (c & 1); PIN(ko_); PIN(vo_); }
    unsigned rb = R2 + l16; PIN(rb);
    __builtin_amdgcn_s_sleep(AUX_DELAY);
    __builtin_amdgcn_s_setprio(2);
    if (a == 1) dma_x8(0); else if (a) dma_w8(wq, 8 * a - 16, R1 + 1024 * (8 * a - 16));
    BAR();
    TS(3);
    u64 tag; float k2 = 0.f;
    u32x4 ua0, ua1, ub0, ub1;
    if (a == 0) {
      __builtin_amdgcn_s_setprio(3);
      f32x4 dk = *LP(f32x4, rb), dv = *LP(f32x4, rb + 1024);
#pragma unroll
      for (int s = 1; s < 8; ++s) { dk += *LP(f32x4, rb + 4096 * s); dv += *LP(f32x4, rb + 4096 * s + 1024); }
      dk *= QSCALE;
      const u32x2 kp = {cvtpk(dk[0], dk[1]), cvtpk(dk[2], dk[3])}, vp = {cvtpk(dv[0], dv[1]), cvtpk(dv[2], dv[3])};
      if (r16 < 8) gst8_sc1(ws, ko_, kp);
      if (g < 2) gst8_sc1(ws, vo_, vp);
      BAR();
      k2 = row_max(col_sum(dk[0] * dk[0] + dk[1] * dk[1] + dk[2] * dk[2] + dk[3] * dk[3]));
    } else {
      BAR();
      _Pragma("unroll 1") for (int st = 0; st < 3; ++st) { if (a == 1) dma_x8(40 + 8 * st); else dma_w8(wq, 24 * a - 32 + 8 * st, R1 + 1024 * (24 * a - 32 + 8 * st)); }
    }
    { const unsigned uvo = (lane >> 3) * 1024 + 16 * (lane & 7); const u16* ub = u + (row0 + 16 * ((a - 1) & 3)) * 512 + 64 * hg;
      asm volatile("s_cmp_eq_u32 %9, 0\n\ts_cbranch_scc1 1f\n\tglobal_load_dwordx4 %0, %4, %5\n\tglobal_load_dwordx4 %1, %4, %6\n\t"
                   "s_cmp_lg_u32 %9, 3\n\ts_cbranch_scc1 1f\n\tglobal_load_dwordx4 %2, %4, %7\n\tglobal_load_dwordx4 %3, %4, %8\n\t1:"
                   : "=&v"(ua0), "=&v"(ua1), "=&v"(ub0), "=&v"(ub1) : "v"(uvo), "s"(ub), "s"(ub + 4096), "s"(ub + 8192), "s"(ub + 12288), "s"(a) : "memory", "scc"); }
    TS(4);
    asm volatile("s_waitcnt vmcnt(0)" : "+v"(ep), "+v"(t0_), "+v"(t1_), "+v"(t2_), "+v"(ua0), "+v"(ua1), "+v"(ub0), "+v"(ub1) :: "memory");
    tag = (((u64)(unsigned)__builtin_amdgcn_readfirstlane(ep[1]) << 32) | (unsigned)__builtin_amdgcn_readfirstlane(ep[0])) + 1;
    if (a == 0) put_flag(ws + WS_FLAG1, bid, lane, tag, __float_as_uint(k2));
    TS(5);
    BAR();
    TS(6);
    if (a == 0) {
      const unsigned m = wave_umax(poll_flags(ws + WS_FLAG1, lane, tag));
      if (lane == 0) *LP(unsigned, R2 + 1028) = m;
    } else {
      const unsigned us = R2 + 4096 * (3 + a);
      unsigned ta = us + 1024 * (g & 1) + 128 * (r16 >> 2) + 32 * (g >> 1) + 8 * (lane & 3); PIN(ta);
      char* ut = ws + WS_UT + (64 * hg) * 4096 + (row0 + 16 * (a - 1)) * 2;
      const unsigned so = (16 * (g >> 1) + r16) * 4096 + 16 * (g & 1);
      *LP(u32x4, us + l16) = ua0; *LP(u32x4, us + 1024 + l16) = ua1;
      gst16_sc1<0>(ut, so, __builtin_bit_cast(u32x4, trd2(ta, ta + 512)));
      gst16_sc1<0>(ut + 32 * 4096, so, __builtin_bit_cast(u32x4, trd2(ta + 64, ta + 64 + 512)));
      if (a == 3) {
        *LP(u32x4, us + l16) = ub0; *LP(u32x4, us + 1024 + l16) = ub1;
        gst16_sc1<32>(ut, so, __builtin_bit_cast(u32x4, trd2(ta, ta + 512)));
        gst16_sc1<32>(ut + 32 * 4096, so, __builtin_bit_cast(u32x4, trd2(ta + 64, ta + 64 + 512)));
      }
    }
    if (a == 1) { touch(wo + 64 * hg, (128 * (loc >> 3) + lane) * 1024, t1_); touch(wo + 64 * hg, (128 * (loc >> 3) + 64 + lane) * 1024, t2_); }
    TS(7);
    BAR();
    TS(8);
    const unsigned kvo = (a & 1) * 65536 + (a >> 1) * 4096;
    const int ch = a >> 1;
    dma16x4_sc1(ws + WS_K + kvo, l16, kvo); dma16x4_sc1(ws + WS_K + kvo + 8192, l16, kvo + 8192);
    asm volatile("s_waitcnt vmcnt(4)" : "+v"(t1_), "+v"(t2_) :: "memory");
    lds_arrive(FL, lane);
    TS(9);
#pragma unroll
    for (int r = 2; r < 5; ++r) dma16x4_sc1(ws + WS_K + kvo + 8192 * r, l16, kvo + 8192 * r);
    WAITV(0); lds_arrive(FL + 4, lane);
    if (a == 0) { lds_wait(FL, 4);
      put_flag(ws + WS_FLAG2, bid, lane, tag, 0); if (bid == 0 && lane == 0) gst8_sc1(ws, WS_EPOCH, (u32x2){(unsigned)tag, (unsigned)(tag >> 32)}); }
    lds_wait(FL + 24, 8);
    _Pragma("unroll 1") for (int task = a; task < 6; task += 4) {
      const int c = task >= 3, tau = task - 3 * c;
      unsigned pb = c * R1 + PART + 1024 * tau + l16; PIN(pb);
      f32x4 v = *LP(f32x4, pb);
#pragma unroll
      for (int s = 1; s < 4; ++s) v += *LP(f32x4, pb + 3072 * s);
      if (c == 0) {
#pragma unroll
        for (int s = 4; s < 8; ++s) v += *LP(f32x4, pb + 3072 * s);
      }
      const unsigned kb = KA + KA_STRIDE * c + 2 * 2055;
      const unsigned c01 = cvtpk(v[0], v[1]), c23 = cvtpk(v[2], v[3]);
      const u16 b0 = (u16)c01, b1 = (u16)(c01 >> 16), b2 = (u16)c23, b3 = (u16)(c23 >> 16);
      if (tau == 0) {
        const unsigned p = kb - 2 * (4 * g + 4 + 16 * r16);
        *LP(u16, p + 6) = b0; *LP(u16, p + 4) = b1; *LP(u16, p + 2) = b2; *LP(u16, p) = b3;
        if (lane == 63) *LP(u16, p - 2) = b3;
      } else if (tau == 1) {
        const bool up = g >= 2;
        const unsigned a0 = kb - 2 * ((up ? 516 + 256 * (g - 2) : 258 + 8 * g) + 16 * r16) - 8, st = up ? 8 : 4;
        const unsigned a1 = a0 - st, a2 = a1 - st, a3 = a2 - st;
        *LP(u16, a0 + 8) = b0; *LP(u16, a2 + 8) = b2;
        *LP(u16, a1 + 6) = b1; *LP(u16, a1 + 8) = b1; *LP(u16, a1 + 10) = b1;
        *LP(u16, a3 + 6) = b3; *LP(u16, a3 + 8) = b3; *LP(u16, a3 + 10) = b3;
        if (up) {
          *LP(u16, a0 + 6) = b0; *LP(u16, a0 + 10) = b0; *LP(u16, a2 + 6) = b2; *LP(u16, a2 + 10) = b2;
          *LP(u16, a1 + 4) = b1; *LP(u16, a1 + 12) = b1; *LP(u16, a3 + 4) = b3; *LP(u16, a3 + 12) = b3;
        }
        if (lane == 31) *LP(u16, a3 + 4) = b3;
        if (lane == 63) { *LP(u16, a3 + 2) = b3; *LP(u16, a3) = b3; }
      } else if (g < 2) {
        const unsigned p = kb - 2 * (1032 + 512 * (g & 1) + 16 * r16) - 536;
        const unsigned d0 = cvtpk(v[0], v[0]), d1 = cvtpk(v[1], v[1]), d2 = cvtpk(v[2], v[2]), d3 = cvtpk(v[3], v[3]);
        *LP(u32x2, p + 536 - 6) = (u32x2){d0, d0}; *LP(unsigned, p + 536 + 2) = d0; *LP(u16, p + 536 + 6) = b0;
        *LP(u32x2, p + 520 - 6) = (u32x2){d1, d1}; *LP(u32x2, p + 520 + 2) = (u32x2){d1, d1}; *LP(u16, p + 520 - 8) = b1;
        *LP(u32x2, p + 24 - 6) = (u32x2){d2, d2}; *LP(unsigned, p + 24 + 2) = d2; *LP(u16, p + 24 + 6) = b2;
        *LP(u32x2, p + 8 - 6) = (u32x2){d3, d3}; *LP(u32x2, p + 8 + 2) = (u32x2){d3, d3}; *LP(u16, p) = b3;
      } else if (lane == 32) *LP(u16, kb) = b0;
    }
    if (a >= 2) {
      if (lane < 60) *LP(u32x2, KA + KA_STRIDE * (a & 1) + 2 * 2056 + 8 * lane) = (u32x2){0, 0};
      if (lane < 4) *LP(u32x4, UC + UC_STRIDE * (2 * (a & 1) + (lane >> 1)) + 16 * (lane & 1)) = (u32x4){0, 0, 0, 0};
    }
    lds_arrive(FL + 16, lane);
    TS(10);
    lds_wait(FL + 16, 4);
#pragma unroll
    for (int r = 5; r < 8; ++r) dma16x4_sc1(ws + WS_K + kvo + 8192 * r, l16, kvo + 8192 * r);
    { char* os = ws + WS_O + (rg * 64 + 32 * (a >> 1) + 4 * hg + 2 * (a & 1)) * 1024; const u32x4 sent = {SENT, SENT, SENT, SENT};
      gst16_sc1<0>(os, l16, sent); gst16_sc1<1024>(os, l16, sent); }
    WAITV(2); lds_arrive(FL + 8, lane);
    poll_flags(ws + WS_FLAG2, lane, tag);
    {
      const char* us = ws + WS_UT + (ch0 + ch) * 4096;
      u32x4 uv[4]; unsigned up[4];
      gld16_sc1<0>(uv[0], us, l16); gld16_sc1<1024>(uv[1], us, l16); gld16_sc1<2048>(uv[2], us, l16); gld16_sc1<3072>(uv[3], us, l16);
      gld4_sc1<-4>(up[0], us, l16); gld4_sc1<1020>(up[1], us, l16); gld4_sc1<2044>(up[2], us, l16); gld4_sc1<3068>(up[3], us, l16);
      asm volatile("s_waitcnt vmcnt(0)" : "+v"(uv[0]), "+v"(uv[1]), "+v"(uv[2]), "+v"(uv[3]), "+v"(up[0]), "+v"(up[1]), "+v"(up[2]), "+v"(up[3]) :: "memory");
      if (lane == 0) up[0] = 0;
      unsigned ub = UC + UC_STRIDE * 2 * ch + 32 + l16; PIN(ub);
#pragma unroll
      for (int i = 0; i < 4; ++i) {
        *LP(u32x4, ub + 1024 * i) = uv[i];
        *LP(u32x4, ub + UC_STRIDE + 1024 * i) = (u32x4){__builtin_amdgcn_alignbit(uv[i][0], up[i], 16), __builtin_amdgcn_alignbit(uv[i][1], uv[i][0], 16),
                                                        __builtin_amdgcn_alignbit(uv[i][2], uv[i][1], 16), __builtin_amdgcn_alignbit(uv[i][3], uv[i][2], 16)};
      }
    }
    TS(2);
    {
      const int cs = a & 1;
      unsigned ba = KA + KA_STRIDE * ch + 2 * (2040 - 16 * r16 + 8 * g) - 3584;
      unsigned aa = UC + UC_STRIDE * (2 * ch + 1 - (r16 & 1)) + 2 * (2 + (r16 & 14) + 8 * g);
      f32x4 y0 = {0, 0, 0, 0}, y1 = y0, y2 = y0, y3 = y0;
      for (int rr = 0; rr < 8; ++rr) {
        bf16x8 A[8], B[8];
#pragma unroll
        for (int i = 0; i < 8; ++i) {
          const LAS unsigned* p = LP(unsigned, aa + 1024 * (i >> 1)) + 128 * (i & 1);
          A[i] = AS8(((u32x4){p[0], p[1], p[2], p[3]}));
          B[i] = LP(bf16x8, ba)[32 * (7 - i)];
        }
#define PACE() do { SBAR(); __builtin_amdgcn_s_sleep(CONV_SLEEP); SBAR(); } while (0)
#define M2(y, i, j, z, k, l) do { y = MFMA16(A[i], B[j], y); z = MFMA16(A[k], B[l], z); PIN(y); PIN(z); PACE(); } while (0)
        if (cs == 0) {
          M2(y3, 0, 7, y2, 0, 4); M2(y3, 1, 6, y2, 1, 3); M2(y3, 2, 5, y2, 2, 2); M2(y3, 3, 4, y2, 3, 1); M2(y3, 4, 3, y2, 4, 0);
          M2(y3, 5, 2, y1, 0, 3); M2(y3, 6, 1, y1, 1, 2); M2(y3, 7, 0, y1, 2, 1); M2(y0, 0, 0, y1, 3, 0);
        } else {
          M2(y3, 0, 6, y2, 0, 5); M2(y3, 1, 5, y2, 1, 4); M2(y3, 2, 4, y2, 2, 3); M2(y3, 3, 3, y2, 3, 2); M2(y3, 4, 2, y2, 4, 1);
          M2(y3, 5, 1, y2, 5, 0); M2(y3, 6, 0, y1, 0, 2); M2(y0, 0, 1, y1, 1, 1); M2(y0, 1, 0, y1, 2, 0);
        }
        ba += 64; aa += 64;
      }
      char* yb = ws + WS_YT + (ch0 + ch) * 4096; const unsigned yo = 32 * r16 + 8 * g;
      gst8_sc1(yb, yo + 512 * (cs ? 1 : 0), (u32x2){cvtpk(y0[0], y0[1]), cvtpk(y0[2], y0[3])});
      gst8_sc1(yb, yo + 512 * (cs ? 2 : 3), (u32x2){cvtpk(y1[0], y1[1]), cvtpk(y1[2], y1[3])});
      gst8_sc1(yb, yo + 512 * (cs ? 5 : 4), (u32x2){cvtpk(y2[0], y2[1]), cvtpk(y2[2], y2[3])});
      gst8_sc1(yb, yo + 512 * (cs ? 6 : 7), (u32x2){cvtpk(y3[0], y3[1]), cvtpk(y3[2], y3[3])});
    }
    TS(11);
    WAITV(0); lds_arrive(FL + 20, lane);
    if (a == 0) { lds_wait(FL + 20, 4); put_flag(ws + WS_FLAG3, bid, lane, tag, 0); }
    TS(12);
    poll_flags(ws + WS_FLAG3, lane, tag);
    {
      const char* ys = ws + WS_YT + (64 * hg) * 4096 + (row0 + 16 * a) * 2; const unsigned yo = (lane >> 1) * 4096 + 16 * (lane & 1);
      dma16_sc1(ys, yo, YS + 2048 * a); dma16_sc1(ys + 32 * 4096, yo, YS + 2048 * a + 1024);
      const unsigned uvo = (lane >> 3) * 1024 + 16 * (lane & 7); const u16* ub = u + (row0 + 16 * a) * 512 + 64 * hg;
      dma16(ub, uvo, US + 2048 * a); dma16(ub + 4096, uvo, US + 2048 * a + 1024);
    }
    lds_wait(FL + 12, 512);
    dma_w8(wo, 16 * a, (a >> 1) * 65536 + (a & 1) * 16384); dma_w8(wo, 16 * a + 8, (a >> 1) * 65536 + (a & 1) * 16384 + 8192);
    TS(13);
    WAITV(0);
    TS_FLUSH();
    return;
  }

  const unsigned slice = R2 + 4096 * w;
  u32x4 xk0, xk1;
  { const unsigned vo = (row0 + 8 * hg + (r16 & 7)) * 1024 + 128 * w + 16 * g;
    gld16<0>(xk0, x, vo); gld16<64>(xk1, x, vo); }
  dma16x2(wk + 1024 * w, l16, slice); dma16x2(wv + 1024 * w, l16, slice + 2048);
  unsigned p_ar, p_ai, p_br, p_bi, p_cr, p_ci, p_dt;
  { const unsigned t = tid & 255, d = ch0 + (t >> 7), vo = (d * 128 + (t & 127)) * 2;
    asm volatile("s_cmp_lt_u32 %16, 4\n\ts_cbranch_scc1 1f\n\t"
                 "global_load_ushort %0, %7, %9\n\tglobal_load_ushort %1, %7, %10\n\tglobal_load_ushort %2, %7, %11\n\tglobal_load_ushort %3, %7, %12\n\t"
                 "global_load_ushort %4, %7, %13\n\tglobal_load_ushort %5, %7, %14\n\tglobal_load_ushort %6, %8, %15\n\ts_waitcnt vmcnt(7)\n\ts_branch 2f\n\t"
                 "1:\n\ts_waitcnt vmcnt(0)\n\t2:"
                 : "=&v"(p_ar), "=&v"(p_ai), "=&v"(p_br), "=&v"(p_bi), "=&v"(p_cr), "=&v"(p_ci), "=&v"(p_dt)
                 : "v"(vo), "v"(d * 2), "s"(Are), "s"(Aim), "s"(Bre), "s"(Bim), "s"(Cre), "s"(Cim), "s"(logdt), "s"(w) : "memory", "scc"); }
  asm volatile("" : "+v"(xk0), "+v"(xk1) :: "memory");
  TS(2);
  {
    const unsigned ta = slice + 256 * g + 8 * r16;
    bf16x8 wk0 = trd2(ta, ta + 128), wk1 = trd2(ta + 1024, ta + 1152), wv0 = trd2(ta + 2048, ta + 2176), wv1 = trd2(ta + 3072, ta + 3200);
    f32x4 dk = {0, 0, 0, 0}, dv = {0, 0, 0, 0};
    dk = MFMA16(wk0, AS8(xk0), dk); dv = MFMA16(AS8(xk0), wv0, dv);
    dk = MFMA16(wk1, AS8(xk1), dk); dv = MFMA16(AS8(xk1), wv1, dv);
    PIN(dk); PIN(dv);
    *LP(f32x4, slice + l16) = dk; *LP(f32x4, slice + 1024 + l16) = dv;
  }
  BAR();
  asm volatile("s_waitcnt vmcnt(0)" : "+v"(p_ar), "+v"(p_ai), "+v"(p_br), "+v"(p_bi), "+v"(p_cr), "+v"(p_ci), "+v"(p_dt) :: "memory");
  if (w >= 4) {
    const float dt = bflo(cvtpk(expf(bflo(p_dt)), 0.f));
    const float al = (dt * bflo(p_ar)) * 1.4426950408889634f;
    float be = (dt * bflo(p_ai)) * 0.15915494309189535f; be -= __builtin_rintf(be);
    const float br = dt * bflo(p_br), bi = dt * bflo(p_bi), cr = bflo(p_cr), ci = bflo(p_ci);
    *LP(f32x4, slice + 2048 + l16) = (f32x4){al, be, cr * br - ci * bi, -(cr * bi + ci * br)};
  }
  BAR();
  TS(3);
  { const int r0 = 8 + 4 * w; const unsigned xv = l16 ^ (16 * (r0 & 12));
    dma16x4v(x + (row0 + r0) * 512, xv, xv ^ 16, xv ^ 32, xv ^ 48, R0 + 1024 * r0); }
  auto kstep = [&](unsigned pa, f32x4* acc) {
    const float eB = (float)(16 * r16), e1 = (float)(r16 + 1);
    const float e2 = (float)(r16 < 8 ? 258 + 2 * r16 : 516 + 256 * ((r16 - 8) >> 2) + 4 * (r16 & 3));
    const float e3 = (float)(r16 < 8 ? 1032 + 256 * (r16 >> 1) + 8 * (r16 & 1) : 0);
    u32x4 fb, f1, f2, f3;
#pragma unroll
    for (int i = 0; i < 4; ++i) {
      const f32x4 P = *LP(f32x4, pa + 16 * i);
      const f32x2 ab = {P[0], P[1]};
      const f32x2 xb = cmul((f32x2){P[2], P[3]}, zpow<false>(ab, (f32x2){eB, -eB})); fb[i] = cvtpk(xb.x, xb.y);
      const f32x2 a1 = zpow<false>(ab, (f32x2){e1, e1}); f1[i] = cvtpk(a1.x, a1.y);
      const f32x2 a2 = zpow<true>(ab, (f32x2){e2, e2}); f2[i] = cvtpk(a2.x, a2.y);
      const f32x2 a3 = zpow<true>(ab, (f32x2){e3, e3}); f3[i] = cvtpk(a3.x, a3.y);
    }
    acc[0] = MFMA16(AS8(f1), AS8(fb), acc[0]); acc[1] = MFMA16(AS8(f2), AS8(fb), acc[1]); acc[2] = MFMA16(AS8(f3), AS8(fb), acc[2]);
    PIN(acc[0]); PIN(acc[1]); PIN(acc[2]);
  };
  f32x4 kc0[3] = {{0, 0, 0, 0}, {0, 0, 0, 0}, {0, 0, 0, 0}}, kc1[3] = {{0, 0, 0, 0}, {0, 0, 0, 0}, {0, 0, 0, 0}};
  { unsigned pa = R2 + 4096 * (4 + (w >> 2)) + 2048 + 256 * (w & 3) + 64 * g; PIN(pa); kstep(pa, kc0); }
  TS(5);
  WAITV(0);
  BAR();
  TS(6);
  if (w < 4) {
    const int rh = w >> 1, hp = w & 1;
    const unsigned xa = R0 + 1024 * (32 * rh + r32) + 16 * (h ^ r16);
    const int q = r16 >> 2, p = lane & 3;
    unsigned wa = R1 + 128 * (8 * h + q) + 16 * ((4 * hp + 2 * (g & 1) + (p >> 1)) ^ ((q >> 1) << 2)) + 8 * (p & 1); PIN(wa);
    f32x16 acc = {0};
    bf16x8 fa[4], fx[4];
#define QLD(ks) do { fx[(ks) & 3] = *LP(bf16x8, (xa ^ (((ks) & 7) << 5)) + 256 * ((ks) >> 3)); fa[(ks) & 3] = trd2(wa + 2048 * (ks), wa + 2048 * (ks) + 512); } while (0)
    QLD(0); QLD(1); QLD(2); QLD(3); SBAR();
#pragma unroll
    for (int ks = 0; ks < 32; ++ks) { acc = MFMA32(fa[ks & 3], fx[ks & 3], acc); if (ks + 4 < 32) QLD(ks + 4); SBAR(); }
    *LP(u32x4, slice + 2048 + l16) = (u32x4){cvtpk(acc[0], acc[1]), cvtpk(acc[2], acc[3]), cvtpk(acc[4], acc[5]), cvtpk(acc[6], acc[7])};
    *LP(u32x4, slice + 3072 + l16) = (u32x4){cvtpk(acc[8], acc[9]), cvtpk(acc[10], acc[11]), cvtpk(acc[12], acc[13]), cvtpk(acc[14], acc[15])};
    f32x2 n = {0, 0};
#pragma unroll
    for (int i = 0; i < 8; ++i) { const f32x2 t = {acc[i], acc[8 + i]}; n += t * t; }
    const unsigned nm = wave_umax(max(__float_as_uint(n.x), __float_as_uint(n.y)));
    if (lane == 0) *LP(unsigned, slice + 1024) = nm;
  } else {
    unsigned pa = R2 + 4096 * (6 + ((w - 4) >> 1)) + 2048 + 512 * (w & 1) + 64 * g; PIN(pa);
    _Pragma("unroll 1") for (int it = 0; it < 2; ++it) { kstep(pa, kc1); pa += 256; }
  }
  if (w == 0 && lane < 14) {
    const unsigned j = lane >> 2, o1 = j ? 0u : 0x3f803f80u;
    *LP(u32x4, j == 3 ? FL + 16 * (lane & 1) : ONES + 512 * (lane & 3) + (j == 2 ? 64 : 16 * j)) = (u32x4){o1, o1, o1, o1};
  }
  TS(7);
  BAR();
  TS(8);
  const int qt = w & 3, hs = w >> 2;
  {
    const unsigned pb = PART + 3072 * w + l16;
    *LP(f32x4, pb) = kc0[0]; *LP(f32x4, pb + 1024) = kc0[1]; *LP(f32x4, pb + 2048) = kc0[2];
    if (w >= 4) { const unsigned pc = pb + R1 - 4 * 3072; *LP(f32x4, pc) = kc1[0]; *LP(f32x4, pc + 1024) = kc1[1]; *LP(f32x4, pc + 2048) = kc1[2]; }
  }
  const bf16x8 qf = *LP(bf16x8, R2 + 4096 * qt + 2048 + 1024 * hs + l16);
  const unsigned q2 = max(max(*LP(unsigned, R2 + 1024), *LP(unsigned, R2 + 4096 + 1024)), max(*LP(unsigned, R2 + 8192 + 1024), *LP(unsigned, R2 + 12288 + 1024)));
  float sh = __builtin_amdgcn_sqrtf(2.04f * __uint_as_float(q2) * *LP(float, R2 + 1028));
  const bool unsafe = (unsigned)__builtin_amdgcn_readfirstlane(__float_as_int(sh)) > __float_as_uint(SAFE_BOUND);
  lds_arrive(FL + 24, lane);
  u32x4 dsk; gld16<0>(dsk, Dsk, 128 * hg + 64 * (w & 1) + 16 * g);
  TS(9);
  if (unsafe) {
    lds_wait(FL + 8, 4);
    const f32x16 z = {0};
    float m = -INFINITY;
    for (int t = 0; t < 64; ++t) {
      const f32x16 s = MFMA32(*LP(bf16x8, R0 + 1024 * t + l16), qf, z);
#pragma unroll
      for (int i = 0; i < 16; ++i) m = fmaxf(m, s[i]);
    }
    float o; m = x32(m, o); sh = fmaxf(m, o);
  }
  f32x16 oacc = {0};
  {
    f32x16 mneg;
#pragma unroll
    for (int i = 0; i < 16; ++i) mneg[i] = -sh;
    const bool lo = (lane & 16) == 0;
    unsigned kp = R0 + l16;
    unsigned vp = lo ? R1 + 256 * h + 16 * r16 : ONES + (r32 == 16 ? 0 : (r32 >= 20 && r32 < 28) ? 64 : 16);
    const unsigned vinc = lo ? 2048 : 0;
    bf16x8 kA, kB, vA0, vA1, vB0, vB1; unsigned tmp, scnt, sstg, stmp;
    asm volatile("s_nop 1\n\t" ATT_LOOP
                 : "+v"(oacc), "+v"(kp), "+v"(vp), "=&v"(kA), "=&v"(kB), "=&v"(vA0), "=&v"(vA1), "=&v"(vB0), "=&v"(vB1), "=&v"(tmp), "=&s"(scnt), "=&s"(sstg), "=&s"(stmp)
                 : "v"(qf), "v"(mneg), "v"(vinc), "v"(FL) : "memory", "scc", ATT_CLOB);
  }
  TS(10);
  asm volatile("s_waitcnt vmcnt(0)" : "+v"(dsk) :: "memory");
  {
    const float rl = __builtin_amdgcn_rcpf(oacc[8]);
    float o; const float r0 = x32(rl, o);
    u32x4 of_ = {cvtpk(oacc[0] * r0, oacc[1] * r0), cvtpk(oacc[2] * r0, oacc[3] * r0), cvtpk(oacc[4] * r0, oacc[5] * r0), cvtpk(oacc[6] * r0, oacc[7] * r0)};
#pragma unroll
    for (int i = 0; i < 4; ++i) of_[i] = min(of_[i], SENT - 1);
    gst16_sc1<0>(ws + WS_O + ((rg * 2 + (qt >> 1)) * 32 + 4 * hg + 2 * (qt & 1) + hs) * 1024, l16, of_);
  }
  BAR();
  TS(11);
  {
    const unsigned ta = YS + 1024 * w + 256 * g + 8 * r16;
    const u32x4 t = __builtin_bit_cast(u32x4, trd2(ta, ta + 128)), uu = *LP(u32x4, US + 2048 * (w >> 1) + 128 * r16 + 64 * (w & 1) + 16 * g);
    u32x4 o;
#pragma unroll
    for (int i = 0; i < 4; ++i) {
      const unsigned s0 = cvtpk(bflo(uu[i]) * bflo(dsk[i]), bfhi(uu[i]) * bfhi(dsk[i]));
      o[i] = cvtpk(bflo(t[i]) + bflo(s0), bfhi(t[i]) + bfhi(s0));
    }
    gst16(out, (row0 + 16 * (w >> 1) + r16) * 2048 + 128 * hg + 64 * (w & 1) + 16 * g, o);
  }
  {
    const int rh = w >> 2, kq = w & 3;
    const char* osrc = ws + WS_O + ((rg * 2 + rh) * 32 + 8 * kq) * 1024;
    const int q = r16 >> 2, p = lane & 3;
    unsigned wa = (kq >> 1) * R1 + (kq & 1) * 16384 + 128 * (4 * h + q) + 16 * ((2 * (g & 1) + (p >> 1)) ^ ((q >> 1) << 2)) + 8 * (p & 1); PIN(wa);
    const unsigned wb = wa ^ 64;
    bf16x8 fa[8], fb[8];
#pragma unroll
    for (int ks = 0; ks < 8; ++ks) { fa[ks] = trd2(wa + 2048 * ks, wa + 2048 * ks + 1024); fb[ks] = trd2(wb + 2048 * ks, wb + 2048 * ks + 1024); PIN(fa[ks]); PIN(fb[ks]); }
    _Pragma("unroll 1") for (unsigned spins = 0; spins < (1u << 20); ++spins) {
      unsigned t; gld4_sc1<1020>(t, osrc, (lane & 7) * 1024);
      asm volatile("s_waitcnt vmcnt(0)" : "+v"(t) :: "memory");
      if (!__any(t == SENT)) break;
    }
    TS(12);
    f32x16 c0, c1;
    _Pragma("unroll 1") for (unsigned spins = 0; spins < (1u << 20); ++spins) {
      u32x4 of[8];
      gld16_sc1<0>(of[0], osrc, l16); gld16_sc1<1024>(of[1], osrc, l16); gld16_sc1<2048>(of[2], osrc, l16); gld16_sc1<3072>(of[3], osrc, l16);
      gld16_sc1<0>(of[4], osrc + 4096, l16); gld16_sc1<1024>(of[5], osrc + 4096, l16); gld16_sc1<2048>(of[6], osrc + 4096, l16); gld16_sc1<3072>(of[7], osrc + 4096, l16);
      unsigned mx = 0;
      const f32x16 z = {0};
#define OSTEP(ks, n) do { asm volatile("s_waitcnt vmcnt(" #n ")" : "+v"(of[ks]) :: "memory"); \
        c0 = MFMA32(fa[ks], AS8(of[ks]), ks ? c0 : z); c1 = MFMA32(fb[ks], AS8(of[ks]), ks ? c1 : z); mx = max(mx, max4(of[ks])); SBAR(); } while (0)
      OSTEP(0, 7); OSTEP(1, 6); OSTEP(2, 5); OSTEP(3, 4); OSTEP(4, 3); OSTEP(5, 2); OSTEP(6, 1); OSTEP(7, 0);
      if (!__any(mx == SENT)) break;
    }
    TS(13);
    const unsigned rbase = 32768 + rh * R1;
    const f32x4 G[8] = {{c0[0], c0[1], c0[2], c0[3]}, {c0[4], c0[5], c0[6], c0[7]}, {c0[8], c0[9], c0[10], c0[11]}, {c0[12], c0[13], c0[14], c0[15]},
                        {c1[0], c1[1], c1[2], c1[3]}, {c1[4], c1[5], c1[6], c1[7]}, {c1[8], c1[9], c1[10], c1[11]}, {c1[12], c1[13], c1[14], c1[15]}};
    unsigned wr = rbase + 2048 * kq + l16; PIN(wr);
#pragma unroll
    for (int j = 0; j < 4; ++j) { *LP(f32x4, wr + 8192 * j) = G[2 * j]; *LP(f32x4, wr + 8192 * j + 1024) = G[2 * j + 1]; }
    BAR();
    unsigned rd = rbase + 8192 * kq + l16; PIN(rd);
    f32x4 a = *LP(f32x4, rd), b = *LP(f32x4, rd + 1024);
#pragma unroll
    for (int s = 1; s < 4; ++s) { a += *LP(f32x4, rd + 2048 * s); b += *LP(f32x4, rd + 2048 * s + 1024); }
    auto s0 = __builtin_amdgcn_permlane32_swap(cvtpk(a[0], a[1]), cvtpk(b[0], b[1]), false, false);
    auto s1 = __builtin_amdgcn_permlane32_swap(cvtpk(a[2], a[3]), cvtpk(b[2], b[3]), false, false);
    gst16(out, (row0 + 32 * rh + r32) * 2048 + 1024 + 128 * hg + 32 * kq + 16 * h, (u32x4){s0[0], s1[0], s0[1], s1[1]});
  }
  TS_FLUSH();
}

extern "C" void kernel_launch(void* const* d_in, const int* in_sizes, int n_in, void* d_out, int out_size, void* d_ws, size_t ws_size, hipStream_t stream) {
  (void)in_sizes; (void)n_in; (void)out_size; (void)ws_size;
  (void)hipFuncSetAttribute((const void*)fused_kernel, hipFuncAttributeMaxDynamicSharedMemorySize, LDS_BYTES);
  fused_kernel<<<256, 768, LDS_BYTES, stream>>>((const u16*)d_in[0], (const u16*)d_in[1], (const u16*)d_in[2], (const u16*)d_in[3], (const u16*)d_in[4],
      (const u16*)d_in[5], (const u16*)d_in[6], (const u16*)d_in[7], (const u16*)d_in[8], (const u16*)d_in[9], (const u16*)d_in[10], (const u16*)d_in[11],
      (const u16*)d_in[12], (const u16*)d_in[13], (u16*)d_out, (char*)d_ws);
}
```

```cpp
#include <hip/hip_runtime.h>
#include <hip/hip_bf16.h>
#pragma clang diagnostic ignored "-Wint-to-pointer-cast"

typedef unsigned short u16;
typedef unsigned long long u64;
typedef __attribute__((ext_vector_type(8))) short bf16x8;
typedef __attribute__((ext_vector_type(4))) short s16x4;
typedef __attribute__((ext_vector_type(16))) float f32x16;
typedef __attribute__((ext_vector_type(4))) float f32x4;
typedef __attribute__((ext_vector_type(2))) float f32x2;
typedef __attribute__((ext_vector_type(4))) unsigned u32x4;
typedef __attribute__((ext_vector_type(2))) unsigned u32x2;
typedef __attribute__((ext_vector_type(2))) __bf16 bf16x2;
#define LAS __attribute__((address_space(3)))
#define LP(T, off) ((LAS T*)(unsigned)(off))
#define SBAR() __builtin_amdgcn_sched_barrier(0)
#define PIN(x) asm volatile("" : "+v"(x))
#define MFMA32(a, b, c) __builtin_amdgcn_mfma_f32_32x32x16_bf16(a, b, c, 0, 0, 0)
#define MFMA16(a, b, c) __builtin_amdgcn_mfma_f32_16x16x32_bf16(a, b, c, 0, 0, 0)
typedef __attribute__((ext_vector_type(8))) float f32x8;
typedef __attribute__((ext_vector_type(8))) __bf16 bf16v8;
typedef __attribute__((ext_vector_type(16))) __bf16 bf16v16;
#define PV(v, p, o) __builtin_amdgcn_smfmac_f32_32x32x32_bf16(__builtin_bit_cast(bf16v8, v), __builtin_bit_cast(bf16v16, p), o, 0xdddd, 0, 0)
#define AS8(x) __builtin_bit_cast(bf16x8, x)
#define EX2(v) __builtin_amdgcn_exp2f(v)
#define BAR() asm volatile("s_waitcnt lgkmcnt(0)\n\ts_barrier" ::: "memory")
#define WAITV(n) asm volatile("s_waitcnt vmcnt(" #n ")" ::: "memory")

#define DMA_PACE 0
#define FXCD 1
#define WGATE 1
#define WCNT 8
#define R_PACE 4
#define AUX_SLP 2
#define STAMP 0
#define TS(i) do {} while (0)
#define TSR(i) do {} while (0)
#define TS_FLUSH() do {} while (0)

constexpr unsigned WS_EPOCH = 0, WS_FLAG1 = 32768, WS_FLAG2 = 65536, WS_FLAG3 = 98304, WS_K = 131072, WS_V = WS_K + 65536, WS_O = 1u << 18, WS_UT = WS_O + (2u << 20), WS_YT = WS_UT + (2u << 20), WS_DBG = 8u << 20;
constexpr unsigned R0 = 0, R1 = 65536, R2 = 131072, LDS_BYTES = 163840;
constexpr unsigned KA = R2, KA_STRIDE = 4608;
constexpr unsigned UC = R2 + 9216, UC_STRIDE = 4160;
constexpr unsigned ONES = R2 + 28672;
constexpr unsigned FL = R2 + 32000;
constexpr unsigned C_R0 = FL, C_PART = FL + 4, C_KTAB = FL + 8, C_PROG = FL + 12, C_R1 = FL + 16, C_R4 = FL + 20, C_R7 = FL + 24, C_Y = FL + 28, C_F3 = FL + 32,
                   C_KV = FL + 36, C_KVRD = FL + 40, C_PAR = FL + 44, C_XW = FL + 48, C_STG = FL + 52, C_UC = FL + 56, C_END = FL + 64, C_UT = FL + 68, C_X = FL + 72, C_W1 = FL + 76, C_QRD = FL + 80, C_Q = FL + 84, C_F1 = FL + 88, C_F2 = FL + 92, C_CMB = FL + 96;
constexpr unsigned PART = 20480;
constexpr unsigned WST = R0 + 32768;
constexpr unsigned YS = R2;
constexpr float QSCALE = 0.25f * 1.4426950408889634f;
constexpr float SAFE_BOUND = 40.f;
constexpr unsigned SENT = 0xffffffffu;
#define AUX_DELAY 8
#define KPRIO 0
#define XGATE 0
#define O_DELAY 13
#define QPRIO 0
#define FSP 128
#define POLL_GATE C_XW
#define POLL_CNT 0
#define POLL_DELAY 14
#define CONV_SINGLE 0
#define WO_AT 7
#define CONV_SLEEP 1
constexpr int CODE_KB = 22;

__device__ __forceinline__ unsigned cvtpk(float lo, float hi) { f32x2 v = {lo, hi}; return __builtin_bit_cast(unsigned, __builtin_convertvector(v, bf16x2)); }
__device__ __forceinline__ float bflo(unsigned x) { return __uint_as_float(x << 16); }
__device__ __forceinline__ float bfhi(unsigned x) { return __uint_as_float(x & 0xffff0000u); }
template <int C> __device__ __forceinline__ float dpp(float v) { return __int_as_float(__builtin_amdgcn_update_dpp(0, __float_as_int(v), C, 0xf, 0xf, true)); }
__device__ __forceinline__ float x16(float v, float& o) { auto r = __builtin_amdgcn_permlane16_swap(__float_as_uint(v), __float_as_uint(v), false, false); o = __uint_as_float(r[1]); return __uint_as_float(r[0]); }
__device__ __forceinline__ float x32(float v, float& o) { auto r = __builtin_amdgcn_permlane32_swap(__float_as_uint(v), __float_as_uint(v), false, false); o = __uint_as_float(r[1]); return __uint_as_float(r[0]); }
__device__ __forceinline__ float row_sum(float v) { v += dpp<0x128>(v); v += dpp<0x124>(v); v += dpp<0x122>(v); v += dpp<0x121>(v); return v; }
__device__ __forceinline__ float row_max(float v) { v = fmaxf(v, dpp<0x128>(v)); v = fmaxf(v, dpp<0x124>(v)); v = fmaxf(v, dpp<0x122>(v)); v = fmaxf(v, dpp<0x121>(v)); return v; }
__device__ __forceinline__ float col_sum(float v) { float o; v = x16(v, o); v += o; v = x32(v, o); return v + o; }
__device__ __forceinline__ float col_max(float v) { float o; v = x16(v, o); v = fmaxf(v, o); v = x32(v, o); return fmaxf(v, o); }

template <int C> __device__ __forceinline__ unsigned dppu(unsigned v) { return (unsigned)__builtin_amdgcn_update_dpp(0, (int)v, C, 0xf, 0xf, true); }
__device__ __forceinline__ unsigned wave_umax(unsigned v) {
  v = max(v, dppu<0x128>(v)); v = max(v, dppu<0x124>(v)); v = max(v, dppu<0x122>(v)); v = max(v, dppu<0x121>(v));
  auto r = __builtin_amdgcn_permlane16_swap(v, v, false, false); v = max(r[0], r[1]);
  r = __builtin_amdgcn_permlane32_swap(v, v, false, false); return max(r[0], r[1]);
}
__device__ __forceinline__ void dma16(const void* base, unsigned voff, unsigned ldst) {
  asm volatile("s_mov_b32 m0, %2\n\ts_nop 0\n\tglobal_load_lds_dwordx4 %0, %1" :: "v"(voff), "s"(base), "s"(ldst) : "memory");
}
__device__ __forceinline__ void dma16_sc1(const void* base, unsigned voff, unsigned ldst) {
  asm volatile("s_mov_b32 m0, %2\n\ts_nop 0\n\tglobal_load_lds_dwordx4 %0, %1 sc1" :: "v"(voff), "s"(base), "s"(ldst) : "memory");
}
__device__ __forceinline__ void dma16x2(const void* base, unsigned voff, unsigned ldst) {
  asm volatile("s_mov_b32 m0, %2\n\ts_nop 0\n\tglobal_load_lds_dwordx4 %0, %1\n\tglobal_load_lds_dwordx4 %0, %1 offset:1024" :: "v"(voff), "s"(base), "s"(ldst) : "memory");
}
__device__ __forceinline__ void dma16x4v(const void* base, unsigned v0, unsigned v1, unsigned v2, unsigned v3, unsigned ldst) {
  asm volatile("s_mov_b32 m0, %5\n\ts_nop 0\n\tglobal_load_lds_dwordx4 %0, %4\n\tglobal_load_lds_dwordx4 %1, %4 offset:1024\n\t"
               "global_load_lds_dwordx4 %2, %4 offset:2048\n\tglobal_load_lds_dwordx4 %3, %4 offset:3072" :: "v"(v0), "v"(v1), "v"(v2), "v"(v3), "s"(base), "s"(ldst) : "memory");
}
__device__ __forceinline__ void dma16x4_sc1(const void* base, unsigned voff, unsigned ldst) {
  asm volatile("s_mov_b32 m0, %2\n\ts_nop 0\n\tglobal_load_lds_dwordx4 %0, %1 sc1\n\tglobal_load_lds_dwordx4 %0, %1 offset:1024 sc1\n\t"
               "global_load_lds_dwordx4 %0, %1 offset:2048 sc1\n\tglobal_load_lds_dwordx4 %0, %1 offset:3072 sc1" :: "v"(voff), "s"(base), "s"(ldst) : "memory");
}
#define DMA_PACE_STR_(n) #n
#define DMA_PACE_STR(n) DMA_PACE_STR_(n)
__device__ __forceinline__ void dma16x4_sc1_slow(const void* base, unsigned voff, unsigned ldst) {
  asm volatile("s_mov_b32 m0, %2\n\ts_nop 0\n\tglobal_load_lds_dwordx4 %0, %1 sc1\n\ts_sleep " DMA_PACE_STR(DMA_PACE) "\n\tglobal_load_lds_dwordx4 %0, %1 offset:1024 sc1\n\ts_sleep " DMA_PACE_STR(DMA_PACE) "\n\t"
               "global_load_lds_dwordx4 %0, %1 offset:2048 sc1\n\ts_sleep " DMA_PACE_STR(DMA_PACE) "\n\tglobal_load_lds_dwordx4 %0, %1 offset:3072 sc1\n\ts_sleep " DMA_PACE_STR(DMA_PACE) :: "v"(voff), "s"(base), "s"(ldst) : "memory");
}
__device__ __forceinline__ void dma16x4v_slow(const void* base, unsigned v0, unsigned v1, unsigned v2, unsigned v3, unsigned ldst) {
  asm volatile("s_mov_b32 m0, %5\n\ts_nop 0\n\tglobal_load_lds_dwordx4 %0, %4\n\ts_sleep " DMA_PACE_STR(DMA_PACE) "\n\tglobal_load_lds_dwordx4 %1, %4 offset:1024\n\ts_sleep " DMA_PACE_STR(DMA_PACE) "\n\t"
               "global_load_lds_dwordx4 %2, %4 offset:2048\n\ts_sleep " DMA_PACE_STR(DMA_PACE) "\n\tglobal_load_lds_dwordx4 %3, %4 offset:3072\n\ts_sleep " DMA_PACE_STR(DMA_PACE) :: "v"(v0), "v"(v1), "v"(v2), "v"(v3), "s"(base), "s"(ldst) : "memory");
}
template <int OFF> __device__ __forceinline__ void gld16(u32x4& d, const void* base, unsigned voff) { asm volatile("global_load_dwordx4 %0, %1, %2 offset:%3" : "=v"(d) : "v"(voff), "s"(base), "i"(OFF) : "memory"); }
template <int OFF> __device__ __forceinline__ void gld16_sc1(u32x4& d, const void* base, unsigned voff) { asm volatile("global_load_dwordx4 %0, %1, %2 offset:%3 sc1" : "=v"(d) : "v"(voff), "s"(base), "i"(OFF) : "memory"); }
template <int OFF> __device__ __forceinline__ void gld4_sc1(unsigned& d, const void* base, unsigned voff) { asm volatile("global_load_dword %0, %1, %2 offset:%3 sc1" : "=v"(d) : "v"(voff), "s"(base), "i"(OFF) : "memory"); }
__device__ __forceinline__ void gld2(unsigned& d, const void* base, unsigned voff) { asm volatile("global_load_ushort %0, %1, %2" : "=v"(d) : "v"(voff), "s"(base) : "memory"); }
__device__ __forceinline__ void gld8_sc1(u32x2& d, const void* base, unsigned voff) { asm volatile("global_load_dwordx2 %0, %1, %2 sc1" : "=v"(d) : "v"(voff), "s"(base) : "memory"); }
template <int OFF> __device__ __forceinline__ void gst16_sc1(void* base, unsigned voff, u32x4 d) { asm volatile("global_store_dwordx4 %0, %1, %2 offset:%3 sc1\n\ts_nop 1" :: "v"(voff), "v"(d), "s"(base), "i"(OFF) : "memory"); }
__device__ __forceinline__ void gst16(void* base, unsigned voff, u32x4 d) { asm volatile("global_store_dwordx4 %0, %1, %2\n\ts_nop 1" :: "v"(voff), "v"(d), "s"(base) : "memory"); }
__device__ __forceinline__ void gst8_sc1(void* base, unsigned voff, u32x2 d) { asm volatile("global_store_dwordx2 %0, %1, %2 sc1" :: "v"(voff), "v"(d), "s"(base) : "memory"); }
__device__ __forceinline__ void gst4_sc1(void* base, unsigned voff, unsigned d) { asm volatile("global_store_dword %0, %1, %2 sc1" :: "v"(voff), "v"(d), "s"(base) : "memory"); }
__device__ __forceinline__ s16x4 trd(unsigned a) { return __builtin_bit_cast(s16x4, __builtin_amdgcn_ds_read_tr16_b64_v4i16(LP(s16x4, a))); }
__device__ __forceinline__ bf16x8 trd2(unsigned a0, unsigned a1) { s16x4 x = trd(a0), y = trd(a1); return (bf16x8){x[0], x[1], x[2], x[3], y[0], y[1], y[2], y[3]}; }
__device__ __forceinline__ unsigned max4(u32x4 v) { return max(max(v[0], v[1]), max(v[2], v[3])); }

__device__ __forceinline__ void touch(const void* base, unsigned voff, unsigned& d) { asm volatile("global_load_dword %0, %1, %2" : "=v"(d) : "v"(voff), "s"(base) : "memory"); }
__device__ __forceinline__ void put_flag(char* f, int sp, int bid, int lane, u64 tag, unsigned val) {
  const unsigned lo = (unsigned)tag, hi = (unsigned)(tag >> 32);
  if (lane < 2) gst8_sc1(f, sp * bid + 8 * lane, lane ? (u32x2){hi, ~lo} : (u32x2){lo, val});
}
__device__ __forceinline__ unsigned poll_flags(const char* f, int lane, u64 tag) {
  const unsigned lo = (unsigned)tag, hi = (unsigned)(tag >> 32);
  unsigned mx = 0; bool done = false;
  _Pragma("unroll 1") for (unsigned spins = 0; spins < (1u << 20); ++spins) {
    if (!done) {
      u32x4 f0, f1, f2, f3;
      asm volatile("global_load_dwordx4 %0, %4, %5 sc1\n\tglobal_load_dwordx4 %1, %4, %6 sc1\n\tglobal_load_dwordx4 %2, %4, %7 sc1\n\tglobal_load_dwordx4 %3, %4, %8 sc1\n\ts_waitcnt vmcnt(0)"
                   : "=&v"(f0), "=&v"(f1), "=&v"(f2), "=&v"(f3) : "v"(FSP * lane), "s"(f), "s"(f + 64 * FSP), "s"(f + 128 * FSP), "s"(f + 192 * FSP) : "memory");
      done = ((f0[0] ^ lo) | (f0[2] ^ hi) | (f0[3] ^ ~lo) | (f1[0] ^ lo) | (f1[2] ^ hi) | (f1[3] ^ ~lo) | (f2[0] ^ lo) | (f2[2] ^ hi) | (f2[3] ^ ~lo) | (f3[0] ^ lo) | (f3[2] ^ hi) | (f3[3] ^ ~lo)) == 0;
      mx = max(max(f0[1], f1[1]), max(f2[1], f3[1]));
    }
    if (!__any(!done)) break;
  }
  return mx;
}
__device__ __forceinline__ void poll_some(const char* f, int who, u64 tag) {
  const unsigned lo = (unsigned)tag, hi = (unsigned)(tag >> 32);
  bool done = false;
  _Pragma("unroll 1") for (unsigned spins = 0; spins < (1u << 20); ++spins) {
    if (!done) {
      u32x4 f0;
      asm volatile("global_load_dwordx4 %0, %1, %2 sc1\n\ts_waitcnt vmcnt(0)" : "=&v"(f0) : "v"(128 * who), "s"(f) : "memory");
      done = ((f0[0] ^ lo) | (f0[2] ^ hi) | (f0[3] ^ ~lo)) == 0;
    }
    if (!__any(!done)) break;
    __builtin_amdgcn_s_sleep(4);
  }
}
__device__ __forceinline__ void lds_arrive(unsigned addr, int) { asm volatile("s_mov_b64 exec, 1\n\tds_add_u32 %0, %1\n\ts_mov_b64 exec, -1" :: "v"(addr), "v"(1u) : "memory"); }
template <int SLP> __device__ __forceinline__ void lds_poll(unsigned addr, unsigned target) {
  unsigned t, c;
  asm volatile("1:\n\tds_read_b32 %0, %2\n\ts_waitcnt lgkmcnt(0)\n\tv_readfirstlane_b32 %1, %0\n\ts_nop 0\n\ts_cmp_ge_u32 %1, %3\n\ts_cbranch_scc1 2f\n\ts_sleep %4\n\ts_branch 1b\n\t2:"
               : "=&v"(t), "=&s"(c) : "v"(addr), "s"(target), "i"(SLP) : "memory", "scc");
}
__device__ __forceinline__ void lds_wait(unsigned addr, unsigned target) { lds_poll<2>(addr, target); }
__device__ __forceinline__ void lds_spin(unsigned addr, unsigned target) { lds_poll<1>(addr, target); }
__device__ __forceinline__ f32x2 cmul(f32x2 c, f32x2 w) {
  f32x2 t, r;
  asm("v_pk_mul_f32 %0, %1, %2 op_sel_hi:[0,1]" : "=v"(t) : "v"(c), "v"(w));
  asm("v_pk_fma_f32 %0, %1, %2, %3 op_sel:[1,1,0] op_sel_hi:[1,0,1] neg_lo:[1,0,0]" : "=v"(r) : "v"(c), "v"(w), "v"(t));
  return r;
}
template <bool FR> __device__ __forceinline__ f32x2 zpow(f32x2 ab, f32x2 e) {
  f32x2 t = ab * e;
  float m = EX2(t.x);
  float a = FR ? __builtin_amdgcn_fractf(t.y) : t.y;
  f32x2 cs = {__builtin_amdgcn_cosf(a), __builtin_amdgcn_sinf(a)};
  return cs * m;
}

#define CONV_0 \
  "ds_read_b128 v[100:103], %8 offset:0\n\tds_read2_b32 v[40:41], %4 offset0:0 offset1:1\n\tds_read2_b32 v[42:43], %4 offset0:2 offset1:3\n\ts_sleep 1\n\t" \
  "ds_read_b128 v[96:99], %8 offset:512\n\tds_read2_b32 v[44:45], %4 offset0:128 offset1:129\n\tds_read2_b32 v[46:47], %4 offset0:130 offset1:131\n\ts_sleep 1\n\t" \
  "ds_read_b128 v[92:95], %8 offset:1024\n\tds_read2_b32 v[48:49], %5 offset0:0 offset1:1\n\tds_read2_b32 v[50:51], %5 offset0:2 offset1:3\n\ts_sleep 1\n\t" \
  "ds_read_b128 v[88:91], %8 offset:1536\n\tds_read2_b32 v[52:53], %5 offset0:128 offset1:129\n\tds_read2_b32 v[54:55], %5 offset0:130 offset1:131\n\ts_sleep 1\n\t" \
  "ds_read_b128 v[84:87], %8 offset:2048\n\tds_read2_b32 v[56:57], %6 offset0:0 offset1:1\n\tds_read2_b32 v[58:59], %6 offset0:2 offset1:3\n\ts_sleep 1\n\t" \
  "ds_read2_b32 v[60:61], %6 offset0:128 offset1:129\n\tds_read2_b32 v[62:63], %6 offset0:130 offset1:131\n\tds_read_b128 v[80:83], %8 offset:2560\n\ts_sleep 1\n\t" \
  "ds_read2_b32 v[64:65], %7 offset0:0 offset1:1\n\tds_read2_b32 v[66:67], %7 offset0:2 offset1:3\n\tds_read2_b32 v[68:69], %7 offset0:128 offset1:129\n\t" \
  "s_sleep 1\n\tds_read2_b32 v[70:71], %7 offset0:130 offset1:131\n\tds_read_b128 v[72:75], %8 offset:3584\n\tds_read_b128 v[76:79], %8 offset:3072\n\t" \
  "s_sleep 1\n\ts_mov_b32 %9, 8\n\t1:\n\tv_add_u32 %4, 64, %4\n\tv_add_u32 %5, 64, %5\n\tv_add_u32 %6, 64, %6\n\tv_add_u32 %7, 64, %7\n\tv_add_u32 %8, 64, %8\n\t" \
  "s_waitcnt lgkmcnt(15)\n\tv_mfma_f32_16x16x32_bf16 %3, v[40:43], v[100:103], %3\n\ts_waitcnt lgkmcnt(14)\n\tv_mfma_f32_16x16x32_bf16 %2, v[40:43], v[88:91], %2\n\t" \
  "ds_read_b128 v[100:103], %8 offset:0\n\ts_sleep 1\n\ts_waitcnt lgkmcnt(12)\n\tv_mfma_f32_16x16x32_bf16 %1, v[40:43], v[84:87], %1\n\ts_waitcnt lgkmcnt(2)\n\t" \
  "v_mfma_f32_16x16x32_bf16 %0, v[40:43], v[72:75], %0\n\tds_read2_b32 v[40:41], %4 offset0:0 offset1:1\n\tds_read2_b32 v[42:43], %4 offset0:2 offset1:3\n\t" \
  "s_sleep 2\n\tv_mfma_f32_16x16x32_bf16 %3, v[44:47], v[96:99], %3\n\tv_mfma_f32_16x16x32_bf16 %2, v[44:47], v[84:87], %2\n\tds_read_b128 v[96:99], %8 offset:512\n\t" \
  "s_sleep 1\n\tv_mfma_f32_16x16x32_bf16 %1, v[44:47], v[80:83], %1\n\tv_mfma_f32_16x16x32_bf16 %3, v[48:51], v[92:95], %3\n\tds_read2_b32 v[44:45], %4 offset0:128 offset1:129\n\t" \
  "ds_read2_b32 v[46:47], %4 offset0:130 offset1:131\n\tds_read_b128 v[92:95], %8 offset:1024\n\ts_sleep 2\n\tv_mfma_f32_16x16x32_bf16 %2, v[48:51], v[80:83], %2\n\t" \
  "s_waitcnt lgkmcnt(7)\n\tv_mfma_f32_16x16x32_bf16 %1, v[48:51], v[76:79], %1\n\tds_read2_b32 v[48:49], %5 offset0:0 offset1:1\n\tds_read2_b32 v[50:51], %5 offset0:2 offset1:3\n\t" \
  "s_sleep 1\n\tv_mfma_f32_16x16x32_bf16 %3, v[52:55], v[88:91], %3\n\tv_mfma_f32_16x16x32_bf16 %2, v[52:55], v[76:79], %2\n\tds_read_b128 v[88:91], %8 offset:1536\n\t" \
  "s_sleep 2\n\tv_mfma_f32_16x16x32_bf16 %1, v[52:55], v[72:75], %1\n\tv_mfma_f32_16x16x32_bf16 %3, v[56:59], v[84:87], %3\n\tds_read2_b32 v[52:53], %5 offset0:128 offset1:129\n\t" \
  "ds_read2_b32 v[54:55], %5 offset0:130 offset1:131\n\tds_read_b128 v[84:87], %8 offset:2048\n\ts_sleep 1\n\tv_mfma_f32_16x16x32_bf16 %2, v[56:59], v[72:75], %2\n\t" \
  "v_mfma_f32_16x16x32_bf16 %3, v[60:63], v[80:83], %3\n\tds_read2_b32 v[56:57], %6 offset0:0 offset1:1\n\tds_read2_b32 v[58:59], %6 offset0:2 offset1:3\n\t" \
  "ds_read2_b32 v[60:61], %6 offset0:128 offset1:129\n\tds_read2_b32 v[62:63], %6 offset0:130 offset1:131\n\tds_read_b128 v[80:83], %8 offset:2560\n\ts_sleep 2\n\t" \
  "v_mfma_f32_16x16x32_bf16 %3, v[64:67], v[76:79], %3\n\tv_mfma_f32_16x16x32_bf16 %3, v[68:71], v[72:75], %3\n\tds_read2_b32 v[64:65], %7 offset0:0 offset1:1\n\t" \
  "ds_read2_b32 v[66:67], %7 offset0:2 offset1:3\n\tds_read2_b32 v[68:69], %7 offset0:128 offset1:129\n\tds_read2_b32 v[70:71], %7 offset0:130 offset1:131\n\t" \
  "ds_read_b128 v[72:75], %8 offset:3584\n\tds_read_b128 v[76:79], %8 offset:3072\n\ts_sleep 1\n\ts_sub_u32 %9, %9, 1\n\ts_cmp_lg_u32 %9, 0\n\ts_cbranch_scc1 1b\n\t" \
  "s_waitcnt lgkmcnt(0)\n\ts_nop 7\n\t"
#define CONV_1 \
  "ds_read_b128 v[96:99], %8 offset:512\n\tds_read2_b32 v[40:41], %4 offset0:0 offset1:1\n\tds_read2_b32 v[42:43], %4 offset0:2 offset1:3\n\ts_sleep 1\n\t" \
  "ds_read_b128 v[92:95], %8 offset:1024\n\tds_read2_b32 v[44:45], %4 offset0:128 offset1:129\n\tds_read2_b32 v[46:47], %4 offset0:130 offset1:131\n\ts_sleep 1\n\t" \
  "ds_read_b128 v[88:91], %8 offset:1536\n\tds_read2_b32 v[48:49], %5 offset0:0 offset1:1\n\tds_read2_b32 v[50:51], %5 offset0:2 offset1:3\n\ts_sleep 1\n\t" \
  "ds_read_b128 v[84:87], %8 offset:2048\n\tds_read2_b32 v[52:53], %5 offset0:128 offset1:129\n\tds_read2_b32 v[54:55], %5 offset0:130 offset1:131\n\ts_sleep 1\n\t" \
  "ds_read_b128 v[80:83], %8 offset:2560\n\tds_read2_b32 v[56:57], %6 offset0:0 offset1:1\n\tds_read2_b32 v[58:59], %6 offset0:2 offset1:3\n\ts_sleep 1\n\t" \
  "ds_read_b128 v[76:79], %8 offset:3072\n\tds_read2_b32 v[60:61], %6 offset0:128 offset1:129\n\tds_read2_b32 v[62:63], %6 offset0:130 offset1:131\n\ts_sleep 1\n\t" \
  "ds_read2_b32 v[64:65], %7 offset0:0 offset1:1\n\tds_read2_b32 v[66:67], %7 offset0:2 offset1:3\n\tds_read_b128 v[72:75], %8 offset:3584\n\ts_sleep 1\n\t" \
  "s_mov_b32 %9, 8\n\t1:\n\tv_add_u32 %4, 64, %4\n\tv_add_u32 %5, 64, %5\n\tv_add_u32 %6, 64, %6\n\tv_add_u32 %7, 64, %7\n\tv_add_u32 %8, 64, %8\n\ts_waitcnt lgkmcnt(15)\n\t" \
  "v_mfma_f32_16x16x32_bf16 %3, v[40:43], v[96:99], %3\n\tv_mfma_f32_16x16x32_bf16 %2, v[40:43], v[92:95], %2\n\tds_read_b128 v[96:99], %8 offset:512\n\t" \
  "s_sleep 1\n\ts_waitcnt lgkmcnt(9)\n\tv_mfma_f32_16x16x32_bf16 %1, v[40:43], v[80:83], %1\n\ts_waitcnt lgkmcnt(6)\n\tv_mfma_f32_16x16x32_bf16 %0, v[40:43], v[76:79], %0\n\t" \
  "ds_read2_b32 v[40:41], %4 offset0:0 offset1:1\n\tds_read2_b32 v[42:43], %4 offset0:2 offset1:3\n\ts_sleep 2\n\tv_mfma_f32_16x16x32_bf16 %3, v[44:47], v[92:95], %3\n\t" \
  "v_mfma_f32_16x16x32_bf16 %2, v[44:47], v[88:91], %2\n\tds_read_b128 v[92:95], %8 offset:1024\n\ts_sleep 1\n\tv_mfma_f32_16x16x32_bf16 %1, v[44:47], v[76:79], %1\n\t" \
  "s_waitcnt lgkmcnt(4)\n\tv_mfma_f32_16x16x32_bf16 %0, v[44:47], v[72:75], %0\n\tds_read2_b32 v[44:45], %4 offset0:128 offset1:129\n\tds_read2_b32 v[46:47], %4 offset0:130 offset1:131\n\t" \
  "s_sleep 2\n\tv_mfma_f32_16x16x32_bf16 %3, v[48:51], v[88:91], %3\n\tv_mfma_f32_16x16x32_bf16 %2, v[48:51], v[84:87], %2\n\tds_read_b128 v[88:91], %8 offset:1536\n\t" \
  "s_sleep 1\n\tv_mfma_f32_16x16x32_bf16 %1, v[48:51], v[72:75], %1\n\tv_mfma_f32_16x16x32_bf16 %3, v[52:55], v[84:87], %3\n\tds_read2_b32 v[48:49], %5 offset0:0 offset1:1\n\t" \
  "ds_read2_b32 v[50:51], %5 offset0:2 offset1:3\n\tds_read_b128 v[84:87], %8 offset:2048\n\ts_sleep 2\n\tv_mfma_f32_16x16x32_bf16 %2, v[52:55], v[80:83], %2\n\t" \
  "v_mfma_f32_16x16x32_bf16 %3, v[56:59], v[80:83], %3\n\tds_read2_b32 v[52:53], %5 offset0:128 offset1:129\n\tds_read2_b32 v[54:55], %5 offset0:130 offset1:131\n\t" \
  "ds_read_b128 v[80:83], %8 offset:2560\n\ts_sleep 1\n\tv_mfma_f32_16x16x32_bf16 %2, v[56:59], v[76:79], %2\n\tv_mfma_f32_16x16x32_bf16 %3, v[60:63], v[76:79], %3\n\t" \
  "ds_read2_b32 v[56:57], %6 offset0:0 offset1:1\n\tds_read2_b32 v[58:59], %6 offset0:2 offset1:3\n\tds_read_b128 v[76:79], %8 offset:3072\n\ts_sleep 2\n\t" \
  "v_mfma_f32_16x16x32_bf16 %2, v[60:63], v[72:75], %2\n\tv_mfma_f32_16x16x32_bf16 %3, v[64:67], v[72:75], %3\n\tds_read2_b32 v[60:61], %6 offset0:128 offset1:129\n\t" \
  "ds_read2_b32 v[62:63], %6 offset0:130 offset1:131\n\tds_read2_b32 v[64:65], %7 offset0:0 offset1:1\n\tds_read2_b32 v[66:67], %7 offset0:2 offset1:3\n\t" \
  "ds_read_b128 v[72:75], %8 offset:3584\n\ts_sleep 1\n\ts_sub_u32 %9, %9, 1\n\ts_cmp_lg_u32 %9, 0\n\ts_cbranch_scc1 1b\n\ts_waitcnt lgkmcnt(0)\n\ts_nop 7\n\t"
#define CONV_CLOB "v40", "v41", "v42", "v43", "v44", "v45", "v46", "v47", "v48", "v49", "v50", "v51", "v52", "v53", "v54", "v55", "v56", "v57", "v58", "v59", "v60", "v61", "v62", "v63", "v64", "v65", "v66", "v67", "v68", "v69", "v70", "v71", "v72", "v73", "v74", "v75", "v76", "v77", "v78", "v79", "v80", "v81", "v82", "v83", "v84", "v85", "v86", "v87", "v88", "v89", "v90", "v91", "v92", "v93", "v94", "v95", "v96", "v97", "v98", "v99", "v100", "v101", "v102", "v103"
#define ATT_LOOP \
  "v_mov_b32 v88, 0\n\tv_mov_b32 v89, 0\n\tv_mov_b32 v90, 0\n\tv_mov_b32 v91, 0\n\tv_mov_b32 v92, 0\n\tv_mov_b32 v93, 0\n\tv_mov_b32 v94, 0\n\tv_mov_b32 v95, 0\n\t" \
  "v_mov_b32 v96, 0\n\tv_mov_b32 v97, 0\n\tv_mov_b32 v98, 0\n\tv_mov_b32 v99, 0\n\tv_mov_b32 v100, 0\n\tv_mov_b32 v101, 0\n\tv_mov_b32 v102, 0\n\tv_mov_b32 v103, 0\n\t" \
  "s_cmp_lt_u32 %28, 4\n\ts_cbranch_scc0 50f\n\t10:\n\tds_read_b32 %9, %22 offset:0\n\ts_waitcnt lgkmcnt(0)\n\tv_readfirstlane_b32 %12, %9\n\ts_nop 0\n\t" \
  "s_cmp_ge_u32 %12, 4\n\ts_cbranch_scc1 108f\n\ts_sleep 1\n\ts_branch 10b\n\t108:\n\tds_read_b128 %3, %1\n\ts_waitcnt lgkmcnt(0)\n\tv_mfma_f32_32x32x16_bf16 v[104:119], %3, %19, 0\n\t" \
  "s_nop 11\n\ts_setprio 2\n\tv_exp_f32 v104, v104\n\tv_exp_f32 v105, v105\n\tv_mfma_f32_32x32x16_bf16 v[120:135], %3, %20, 0\n\tds_read_b128 %4, %1 offset:1024\n\t" \
  "ds_read_b128 %5, %2 offset:0\n\tds_read_b128 %6, %2 offset:512\n\tds_write_b128 %23, %13\n\tds_write_b128 %23, %14 offset:1024\n\tds_write_b128 %23, %15 offset:2048\n\t" \
  "v_exp_f32 v106, v106\n\tv_exp_f32 v107, v107\n\tv_exp_f32 v108, v108\n\tv_exp_f32 v109, v109\n\tv_exp_f32 v110, v110\n\tv_exp_f32 v111, v111\n\tv_exp_f32 v112, v112\n\t" \
  "v_exp_f32 v113, v113\n\tv_exp_f32 v114, v114\n\tv_exp_f32 v115, v115\n\tv_exp_f32 v116, v116\n\tv_exp_f32 v117, v117\n\tv_exp_f32 v118, v118\n\tv_exp_f32 v119, v119\n\t" \
  "v_exp_f32 v120, v120\n\tv_exp_f32 v121, v121\n\ts_waitcnt lgkmcnt(3)\n\tv_mfma_f32_32x32x16_bf16 v[136:151], %4, %19, 0\n\tv_mov_b32 %9, 1\n\ts_mov_b64 exec, 1\n\t" \
  "ds_add_u32 %22, %9 offset:4\n\ts_mov_b64 exec, -1\n\tv_exp_f32 v122, v122\n\tv_exp_f32 v123, v123\n\tv_exp_f32 v124, v124\n\tv_exp_f32 v125, v125\n\t" \
  "v_exp_f32 v126, v126\n\tv_smfmac_f32_32x32x32_bf16 v[88:103], %5, v[104:111], %27\n\tv_exp_f32 v127, v127\n\tv_exp_f32 v128, v128\n\tv_exp_f32 v129, v129\n\t" \
  "v_exp_f32 v130, v130\n\tv_exp_f32 v131, v131\n\tv_smfmac_f32_32x32x32_bf16 v[88:103], %6, v[112:119], %27\n\tv_exp_f32 v132, v132\n\tv_exp_f32 v133, v133\n\t" \
  "v_exp_f32 v134, v134\n\tv_exp_f32 v135, v135\n\tv_exp_f32 v136, v136\n\tv_exp_f32 v137, v137\n\tv_mfma_f32_32x32x16_bf16 v[152:167], %4, %20, 0\n\tds_read_b128 %3, %1 offset:2048\n\t" \
  "ds_read_b128 %7, %2 offset:1024\n\tds_read_b128 %8, %2 offset:1536\n\tds_read_b128 v[84:87], %22 offset:16\n\tv_exp_f32 v138, v138\n\tv_exp_f32 v139, v139\n\t" \
  "v_exp_f32 v140, v140\n\tv_exp_f32 v141, v141\n\tv_exp_f32 v142, v142\n\tv_smfmac_f32_32x32x32_bf16 %0, %5, v[120:127], %27\n\tv_add_u32 %2, %21, %2\n\t" \
  "v_add_u32 %1, 0x800, %1\n\tv_exp_f32 v143, v143\n\tv_exp_f32 v144, v144\n\tv_exp_f32 v145, v145\n\tv_exp_f32 v146, v146\n\tv_exp_f32 v147, v147\n\tv_smfmac_f32_32x32x32_bf16 %0, %6, v[128:135], %27\n\t" \
  "v_exp_f32 v148, v148\n\tv_exp_f32 v149, v149\n\tv_exp_f32 v150, v150\n\tv_exp_f32 v151, v151\n\tv_exp_f32 v152, v152\n\tv_exp_f32 v153, v153\n\ts_waitcnt lgkmcnt(0)\n\t" \
  "v_mfma_f32_32x32x16_bf16 v[104:119], %3, %19, 0\n\tv_exp_f32 v154, v154\n\tv_exp_f32 v155, v155\n\tv_exp_f32 v156, v156\n\tv_exp_f32 v157, v157\n\ts_setprio 0\n\t" \
  "v_exp_f32 v158, v158\n\tv_smfmac_f32_32x32x32_bf16 v[88:103], %7, v[136:143], %27\n\tv_exp_f32 v159, v159\n\tv_exp_f32 v160, v160\n\tv_exp_f32 v161, v161\n\t" \
  "v_exp_f32 v162, v162\n\tv_exp_f32 v163, v163\n\tv_smfmac_f32_32x32x32_bf16 v[88:103], %8, v[144:151], %27\n\tv_exp_f32 v164, v164\n\tv_exp_f32 v165, v165\n\t" \
  "v_exp_f32 v166, v166\n\tv_exp_f32 v167, v167\n\tv_readfirstlane_b32 %12, v84\n\ts_nop 0\n\ts_cmp_ge_u32 %12, 4\n\ts_cbranch_scc1 119f\n\t11:\n\tds_read_b32 %9, %22 offset:16\n\t" \
  "s_waitcnt lgkmcnt(0)\n\tv_readfirstlane_b32 %12, %9\n\ts_nop 0\n\ts_cmp_ge_u32 %12, 4\n\ts_cbranch_scc1 118f\n\ts_sleep 1\n\ts_branch 11b\n\t118:\n\t" \
  "119:\n\ts_mov_b32 %10, 2\n\ts_mov_b32 %11, 1\n\t.p2align 6\n\t1:\n\ts_setprio 2\n\tv_exp_f32 v104, v104\n\tv_exp_f32 v105, v105\n\tv_mfma_f32_32x32x16_bf16 v[120:135], %3, %20, 0\n\t" \
  "ds_read_b128 %4, %1 offset:1024\n\tds_read_b128 %5, %2 offset:0\n\tds_read_b128 %6, %2 offset:512\n\tv_exp_f32 v106, v106\n\tv_exp_f32 v107, v107\n\t" \
  "v_exp_f32 v108, v108\n\tv_exp_f32 v109, v109\n\tv_exp_f32 v110, v110\n\tv_smfmac_f32_32x32x32_bf16 %0, %7, v[152:159], %27\n\tv_exp_f32 v111, v111\n\t" \
  "v_exp_f32 v112, v112\n\tv_exp_f32 v113, v113\n\tv_exp_f32 v114, v114\n\tv_exp_f32 v115, v115\n\tv_smfmac_f32_32x32x32_bf16 %0, %8, v[160:167], %27\n\t" \
  "v_exp_f32 v116, v116\n\tv_exp_f32 v117, v117\n\tv_exp_f32 v118, v118\n\tv_exp_f32 v119, v119\n\tv_exp_f32 v120, v120\n\tv_exp_f32 v121, v121\n\ts_waitcnt lgkmcnt(0)\n\t" \
  "v_mfma_f32_32x32x16_bf16 v[136:151], %4, %19, 0\n\tv_exp_f32 v122, v122\n\tv_exp_f32 v123, v123\n\tv_exp_f32 v124, v124\n\tv_exp_f32 v125, v125\n\tv_exp_f32 v126, v126\n\t" \
  "v_smfmac_f32_32x32x32_bf16 v[88:103], %5, v[104:111], %27\n\tv_exp_f32 v127, v127\n\tv_exp_f32 v128, v128\n\tv_exp_f32 v129, v129\n\tv_exp_f32 v130, v130\n\t" \
  "v_exp_f32 v131, v131\n\tv_smfmac_f32_32x32x32_bf16 v[88:103], %6, v[112:119], %27\n\tv_exp_f32 v132, v132\n\tv_exp_f32 v133, v133\n\tv_exp_f32 v134, v134\n\t" \
  "v_exp_f32 v135, v135\n\tv_exp_f32 v136, v136\n\tv_exp_f32 v137, v137\n\tv_mfma_f32_32x32x16_bf16 v[152:167], %4, %20, 0\n\tds_read_b128 %3, %1 offset:2048\n\t" \
  "ds_read_b128 %7, %2 offset:1024\n\tds_read_b128 %8, %2 offset:1536\n\tds_read_b128 v[84:87], %22 offset:16\n\tv_exp_f32 v138, v138\n\tv_exp_f32 v139, v139\n\t" \
  "v_exp_f32 v140, v140\n\tv_exp_f32 v141, v141\n\tv_exp_f32 v142, v142\n\tv_smfmac_f32_32x32x32_bf16 %0, %5, v[120:127], %27\n\tv_add_u32 %2, %21, %2\n\t" \
  "v_add_u32 %1, 0x800, %1\n\tv_exp_f32 v143, v143\n\tv_exp_f32 v144, v144\n\tv_exp_f32 v145, v145\n\tv_exp_f32 v146, v146\n\tv_exp_f32 v147, v147\n\tv_smfmac_f32_32x32x32_bf16 %0, %6, v[128:135], %27\n\t" \
  "v_exp_f32 v148, v148\n\tv_exp_f32 v149, v149\n\tv_exp_f32 v150, v150\n\tv_exp_f32 v151, v151\n\tv_exp_f32 v152, v152\n\tv_exp_f32 v153, v153\n\ts_waitcnt lgkmcnt(0)\n\t" \
  "v_mfma_f32_32x32x16_bf16 v[104:119], %3, %19, 0\n\tv_exp_f32 v154, v154\n\tv_exp_f32 v155, v155\n\tv_exp_f32 v156, v156\n\tv_exp_f32 v157, v157\n\ts_setprio 0\n\t" \
  "v_exp_f32 v158, v158\n\tv_smfmac_f32_32x32x32_bf16 v[88:103], %7, v[136:143], %27\n\tv_exp_f32 v159, v159\n\tv_exp_f32 v160, v160\n\tv_exp_f32 v161, v161\n\t" \
  "v_exp_f32 v162, v162\n\tv_exp_f32 v163, v163\n\tv_smfmac_f32_32x32x32_bf16 v[88:103], %8, v[144:151], %27\n\tv_exp_f32 v164, v164\n\tv_exp_f32 v165, v165\n\t" \
  "v_exp_f32 v166, v166\n\tv_exp_f32 v167, v167\n\ts_sub_u32 %10, %10, 1\n\ts_cmp_lg_u32 %10, 0\n\ts_cbranch_scc1 1b\n\ts_cmp_eq_u32 %11, 1\n\ts_cbranch_scc0 4f\n\t" \
  "v_readfirstlane_b32 %12, v85\n\ts_nop 0\n\ts_cmp_ge_u32 %12, 4\n\ts_cbranch_scc1 139f\n\t13:\n\tds_read_b32 %9, %22 offset:20\n\ts_waitcnt lgkmcnt(0)\n\t" \
  "v_readfirstlane_b32 %12, %9\n\ts_nop 0\n\ts_cmp_ge_u32 %12, 4\n\ts_cbranch_scc1 138f\n\ts_sleep 1\n\ts_branch 13b\n\t138:\n\t139:\n\ts_mov_b32 %10, 6\n\t" \
  "s_mov_b32 %11, 2\n\ts_branch 1b\n\t4:\n\ts_cmp_eq_u32 %11, 2\n\ts_cbranch_scc0 3f\n\tv_mov_b32 %9, 1\n\ts_mov_b64 exec, 1\n\tds_add_u32 %22, %9 offset:12\n\t" \
  "s_mov_b64 exec, -1\n\tv_readfirstlane_b32 %12, v86\n\ts_nop 0\n\ts_cmp_ge_u32 %12, 4\n\ts_cbranch_scc1 129f\n\t12:\n\tds_read_b32 %9, %22 offset:24\n\t" \
  "s_waitcnt lgkmcnt(0)\n\tv_readfirstlane_b32 %12, %9\n\ts_nop 0\n\ts_cmp_ge_u32 %12, 4\n\ts_cbranch_scc1 128f\n\ts_sleep 1\n\ts_branch 12b\n\t128:\n\t" \
  "129:\n\ts_mov_b32 %10, 7\n\ts_mov_b32 %11, 3\n\ts_branch 1b\n\t50:\n\ts_setprio 1\n\t10:\n\tds_read_b32 %9, %22 offset:0\n\ts_waitcnt lgkmcnt(0)\n\t" \
  "v_readfirstlane_b32 %12, %9\n\ts_nop 0\n\ts_cmp_ge_u32 %12, 4\n\ts_cbranch_scc1 108f\n\ts_sleep 1\n\ts_branch 10b\n\t108:\n\tds_read_b128 %3, %1\n\t" \
  "s_waitcnt lgkmcnt(0)\n\tv_mfma_f32_32x32x16_bf16 v[104:119], %3, %19, 0\n\ts_nop 11\n\tv_exp_f32 v104, v104\n\tv_exp_f32 v105, v105\n\tv_mfma_f32_32x32x16_bf16 v[120:135], %3, %20, 0\n\t" \
  "ds_read_b128 %4, %1 offset:1024\n\tds_read_b128 %5, %2 offset:0\n\tds_read_b128 %6, %2 offset:512\n\tds_write_b128 %23, %13\n\tds_write_b128 %23, %14 offset:1024\n\t" \
  "ds_write_b128 %23, %15 offset:2048\n\tds_write_b128 %23, %16 offset:32768\n\tv_exp_f32 v106, v106\n\tv_exp_f32 v107, v107\n\tv_exp_f32 v108, v108\n\t" \
  "v_exp_f32 v109, v109\n\tv_exp_f32 v110, v110\n\tv_exp_f32 v111, v111\n\tv_exp_f32 v112, v112\n\tv_exp_f32 v113, v113\n\tv_exp_f32 v114, v114\n\tv_exp_f32 v115, v115\n\t" \
  "v_exp_f32 v116, v116\n\tv_exp_f32 v117, v117\n\tv_exp_f32 v118, v118\n\tv_exp_f32 v119, v119\n\tv_exp_f32 v120, v120\n\tv_exp_f32 v121, v121\n\ts_waitcnt lgkmcnt(4)\n\t" \
  "v_mfma_f32_32x32x16_bf16 v[136:151], %4, %19, 0\n\tds_write_b128 %23, %17 offset:33792\n\tds_write_b128 %23, %18 offset:34816\n\tv_mov_b32 %9, 1\n\t" \
  "s_mov_b64 exec, 1\n\tds_add_u32 %22, %9 offset:4\n\ts_mov_b64 exec, -1\n\tv_exp_f32 v122, v122\n\tv_exp_f32 v123, v123\n\tv_exp_f32 v124, v124\n\tv_exp_f32 v125, v125\n\t" \
  "v_exp_f32 v126, v126\n\tv_smfmac_f32_32x32x32_bf16 v[88:103], %5, v[104:111], %27\n\tv_exp_f32 v127, v127\n\tv_exp_f32 v128, v128\n\tv_exp_f32 v129, v129\n\t" \
  "v_exp_f32 v130, v130\n\tv_exp_f32 v131, v131\n\tv_smfmac_f32_32x32x32_bf16 v[88:103], %6, v[112:119], %27\n\tv_exp_f32 v132, v132\n\tv_exp_f32 v133, v133\n\t" \
  "v_exp_f32 v134, v134\n\tv_exp_f32 v135, v135\n\tv_exp_f32 v136, v136\n\tv_exp_f32 v137, v137\n\tv_mfma_f32_32x32x16_bf16 v[152:167], %4, %20, 0\n\tds_read_b128 %3, %1 offset:2048\n\t" \
  "ds_read_b128 %7, %2 offset:1024\n\tds_read_b128 %8, %2 offset:1536\n\tds_read_b128 v[84:87], %22 offset:16\n\tv_exp_f32 v138, v138\n\tv_exp_f32 v139, v139\n\t" \
  "v_exp_f32 v140, v140\n\tv_exp_f32 v141, v141\n\tv_exp_f32 v142, v142\n\tv_smfmac_f32_32x32x32_bf16 %0, %5, v[120:127], %27\n\tv_add_u32 %2, %21, %2\n\t" \
  "v_add_u32 %1, 0x800, %1\n\tv_exp_f32 v143, v143\n\tv_exp_f32 v144, v144\n\tv_exp_f32 v145, v145\n\tv_exp_f32 v146, v146\n\tv_exp_f32 v147, v147\n\tv_smfmac_f32_32x32x32_bf16 %0, %6, v[128:135], %27\n\t" \
  "v_exp_f32 v148, v148\n\tv_exp_f32 v149, v149\n\tv_exp_f32 v150, v150\n\tv_exp_f32 v151, v151\n\tv_exp_f32 v152, v152\n\tv_exp_f32 v153, v153\n\ts_waitcnt lgkmcnt(0)\n\t" \
  "v_mfma_f32_32x32x16_bf16 v[104:119], %3, %19, 0\n\tv_exp_f32 v154, v154\n\tv_exp_f32 v155, v155\n\tv_exp_f32 v156, v156\n\tv_exp_f32 v157, v157\n\tv_exp_f32 v158, v158\n\t" \
  "v_smfmac_f32_32x32x32_bf16 v[88:103], %7, v[136:143], %27\n\tv_exp_f32 v159, v159\n\tv_exp_f32 v160, v160\n\tv_exp_f32 v161, v161\n\tv_exp_f32 v162, v162\n\t" \
  "v_exp_f32 v163, v163\n\tv_smfmac_f32_32x32x32_bf16 v[88:103], %8, v[144:151], %27\n\tv_exp_f32 v164, v164\n\tv_exp_f32 v165, v165\n\tv_exp_f32 v166, v166\n\t" \
  "v_exp_f32 v167, v167\n\tv_readfirstlane_b32 %12, v84\n\ts_nop 0\n\ts_cmp_ge_u32 %12, 4\n\ts_cbranch_scc1 119f\n\t11:\n\tds_read_b32 %9, %22 offset:16\n\t" \
  "s_waitcnt lgkmcnt(0)\n\tv_readfirstlane_b32 %12, %9\n\ts_nop 0\n\ts_cmp_ge_u32 %12, 4\n\ts_cbranch_scc1 118f\n\ts_sleep 1\n\ts_branch 11b\n\t118:\n\t" \
  "119:\n\ts_mov_b32 %10, 2\n\ts_mov_b32 %11, 1\n\t.p2align 6\n\t1:\n\tv_exp_f32 v104, v104\n\tv_exp_f32 v105, v105\n\tv_mfma_f32_32x32x16_bf16 v[120:135], %3, %20, 0\n\t" \
  "ds_read_b128 %4, %1 offset:1024\n\tds_read_b128 %5, %2 offset:0\n\tds_read_b128 %6, %2 offset:512\n\tv_exp_f32 v106, v106\n\tv_exp_f32 v107, v107\n\t" \
  "v_exp_f32 v108, v108\n\tv_exp_f32 v109, v109\n\tv_exp_f32 v110, v110\n\tv_smfmac_f32_32x32x32_bf16 %0, %7, v[152:159], %27\n\tv_exp_f32 v111, v111\n\t" \
  "v_exp_f32 v112, v112\n\tv_exp_f32 v113, v113\n\tv_exp_f32 v114, v114\n\tv_exp_f32 v115, v115\n\tv_smfmac_f32_32x32x32_bf16 %0, %8, v[160:167], %27\n\t" \
  "v_exp_f32 v116, v116\n\tv_exp_f32 v117, v117\n\tv_exp_f32 v118, v118\n\tv_exp_f32 v119, v119\n\tv_exp_f32 v120, v120\n\tv_exp_f32 v121, v121\n\ts_waitcnt lgkmcnt(0)\n\t" \
  "v_mfma_f32_32x32x16_bf16 v[136:151], %4, %19, 0\n\tv_exp_f32 v122, v122\n\tv_exp_f32 v123, v123\n\tv_exp_f32 v124, v124\n\tv_exp_f32 v125, v125\n\tv_exp_f32 v126, v126\n\t" \
  "v_smfmac_f32_32x32x32_bf16 v[88:103], %5, v[104:111], %27\n\tv_exp_f32 v127, v127\n\tv_exp_f32 v128, v128\n\tv_exp_f32 v129, v129\n\tv_exp_f32 v130, v130\n\t" \
  "v_exp_f32 v131, v131\n\tv_smfmac_f32_32x32x32_bf16 v[88:103], %6, v[112:119], %27\n\tv_exp_f32 v132, v132\n\tv_exp_f32 v133, v133\n\tv_exp_f32 v134, v134\n\t" \
  "v_exp_f32 v135, v135\n\tv_exp_f32 v136, v136\n\tv_exp_f32 v137, v137\n\tv_mfma_f32_32x32x16_bf16 v[152:167], %4, %20, 0\n\tds_read_b128 %3, %1 offset:2048\n\t" \
  "ds_read_b128 %7, %2 offset:1024\n\tds_read_b128 %8, %2 offset:1536\n\tds_read_b128 v[84:87], %22 offset:16\n\tv_exp_f32 v138, v138\n\tv_exp_f32 v139, v139\n\t" \
  "v_exp_f32 v140, v140\n\tv_exp_f32 v141, v141\n\tv_exp_f32 v142, v142\n\tv_smfmac_f32_32x32x32_bf16 %0, %5, v[120:127], %27\n\tv_add_u32 %2, %21, %2\n\t" \
  "v_add_u32 %1, 0x800, %1\n\tv_exp_f32 v143, v143\n\tv_exp_f32 v144, v144\n\tv_exp_f32 v145, v145\n\tv_exp_f32 v146, v146\n\tv_exp_f32 v147, v147\n\tv_smfmac_f32_32x32x32_bf16 %0, %6, v[128:135], %27\n\t" \
  "v_exp_f32 v148, v148\n\tv_exp_f32 v149, v149\n\tv_exp_f32 v150, v150\n\tv_exp_f32 v151, v151\n\tv_exp_f32 v152, v152\n\tv_exp_f32 v153, v153\n\ts_waitcnt lgkmcnt(0)\n\t" \
  "v_mfma_f32_32x32x16_bf16 v[104:119], %3, %19, 0\n\tv_exp_f32 v154, v154\n\tv_exp_f32 v155, v155\n\tv_exp_f32 v156, v156\n\tv_exp_f32 v157, v157\n\tv_exp_f32 v158, v158\n\t" \
  "v_smfmac_f32_32x32x32_bf16 v[88:103], %7, v[136:143], %27\n\tv_exp_f32 v159, v159\n\tv_exp_f32 v160, v160\n\tv_exp_f32 v161, v161\n\tv_exp_f32 v162, v162\n\t" \
  "v_exp_f32 v163, v163\n\tv_smfmac_f32_32x32x32_bf16 v[88:103], %8, v[144:151], %27\n\tv_exp_f32 v164, v164\n\tv_exp_f32 v165, v165\n\tv_exp_f32 v166, v166\n\t" \
  "v_exp_f32 v167, v167\n\ts_sub_u32 %10, %10, 1\n\ts_cmp_lg_u32 %10, 0\n\ts_cbranch_scc1 1b\n\ts_cmp_eq_u32 %11, 1\n\ts_cbranch_scc0 4f\n\tv_readfirstlane_b32 %12, v85\n\t" \
  "s_nop 0\n\ts_cmp_ge_u32 %12, 4\n\ts_cbranch_scc1 139f\n\t13:\n\tds_read_b32 %9, %22 offset:20\n\ts_waitcnt lgkmcnt(0)\n\tv_readfirstlane_b32 %12, %9\n\t" \
  "s_nop 0\n\ts_cmp_ge_u32 %12, 4\n\ts_cbranch_scc1 138f\n\ts_sleep 1\n\ts_branch 13b\n\t138:\n\t139:\n\ts_mov_b32 %10, 6\n\ts_mov_b32 %11, 2\n\ts_branch 1b\n\t" \
  "4:\n\ts_cmp_eq_u32 %11, 2\n\ts_cbranch_scc0 3f\n\tv_mov_b32 %9, 1\n\ts_mov_b64 exec, 1\n\tds_add_u32 %22, %9 offset:12\n\ts_mov_b64 exec, -1\n\tv_readfirstlane_b32 %12, v86\n\t" \
  "s_nop 0\n\ts_cmp_ge_u32 %12, 4\n\ts_cbranch_scc1 129f\n\t12:\n\tds_read_b32 %9, %22 offset:24\n\ts_waitcnt lgkmcnt(0)\n\tv_readfirstlane_b32 %12, %9\n\t" \
  "s_nop 0\n\ts_cmp_ge_u32 %12, 4\n\ts_cbranch_scc1 128f\n\ts_sleep 1\n\ts_branch 12b\n\t128:\n\t129:\n\ts_mov_b32 %10, 7\n\ts_mov_b32 %11, 3\n\ts_branch 1b\n\t" \
  "3:\n\ts_setprio 0\n\tv_smfmac_f32_32x32x32_bf16 %0, %7, v[152:159], %27\n\tv_smfmac_f32_32x32x32_bf16 %0, %8, v[160:167], %27\n\tv_readfirstlane_b32 %12, v87\n\t" \
  "s_nop 0\n\ts_cmp_ge_u32 %12, 4\n\ts_cbranch_scc1 149f\n\t14:\n\tds_read_b32 %9, %22 offset:28\n\ts_waitcnt lgkmcnt(0)\n\tv_readfirstlane_b32 %12, %9\n\t" \
  "s_nop 0\n\ts_cmp_ge_u32 %12, 4\n\ts_cbranch_scc1 148f\n\ts_sleep 1\n\ts_branch 14b\n\t148:\n\t149:\n\tv_and_b32 %2, 0x3ff, %1\n\tv_lshrrev_b32 %1, 2, %2\n\t" \
  "v_add_u32 v84, %24, %2\n\tv_add_u32 v85, %24, %1\n\tv_mov_b32 v86, %26\n\tv_mov_b32 %9, 1\n\tds_write_b128 v84, v[88:91]\n\tds_write_b128 v84, v[92:95] offset:1024\n\t" \
  "ds_write_b32 v85, v96 offset:2048\n\ts_mov_b64 exec, 1\n\tds_add_u32 v86, %9\n\ts_mov_b64 exec, -1\n\tv_add_u32 v84, %25, %2\n\tv_add_u32 v85, %25, %1\n\t" \
  "15:\n\tds_read_b32 %9, v86\n\ts_waitcnt lgkmcnt(0)\n\tv_readfirstlane_b32 %12, %9\n\ts_nop 0\n\ts_cmp_ge_u32 %12, 2\n\ts_cbranch_scc0 15b\n\tds_read_b128 %3, v84\n\t" \
  "ds_read_b128 %4, v84 offset:1024\n\tds_read_b32 %9, v85 offset:2048\n\ts_waitcnt lgkmcnt(0)\n\t"
#define ATT_CLOB "v84", "v85", "v86", "v87", "v88", "v89", "v90", "v91", "v92", "v93", "v94", "v95", "v96", "v97", "v98", "v99", "v100", "v101", "v102", "v103", "v104", "v105", "v106", "v107", "v108", "v109", "v110", "v111", "v112", "v113", "v114", "v115", "v116", "v117", "v118", "v119", "v120", "v121", "v122", "v123", "v124", "v125", "v126", "v127", "v128", "v129", "v130", "v131", "v132", "v133", "v134", "v135", "v160", "v161", "v162", "v163", "v164", "v165", "v166", "v167"

__global__ void __launch_bounds__(768, 3)
fused_kernel(const u16* u_, const u16* Are_, const u16* Aim_, const u16* Bre_, const u16* Bim_, const u16* Cre_, const u16* Cim_, const u16* logdt_, const u16* Dsk_,
             const u16* x_, const u16* wq_, const u16* wk_, const u16* wv_, const u16* wo_, u16* out_, char* ws_) {
  typedef __attribute__((ext_vector_type(8))) u64 u64x8;
  u64x8 ka_, kb_;
  asm volatile("s_load_dwordx16 %0, %2, 0\n\ts_load_dwordx16 %1, %2, 64" : "=&s"(ka_), "=&s"(kb_) : "s"(__builtin_amdgcn_kernarg_segment_ptr()) : "memory");
  u64 pc0; asm volatile("s_getpc_b64 %0" : "=s"(pc0));
  const int tid = threadIdx.x, lane = tid & 63;
  const int w = __builtin_amdgcn_readfirstlane(tid >> 6);
  const int bid = blockIdx.x, xcd = bid & 7, loc = bid >> 3;
  const int rg = xcd * 4 + (loc >> 3), hg = loc & 7, row0 = rg * 64, ch0 = xcd * 64 + loc * 2;
  const int r16 = lane & 15, g = lane >> 4, r32 = lane & 31, h = lane >> 5;
  const unsigned l16 = 16 * lane;
  asm volatile("s_cmp_lt_u32 %0, 8\n\ts_cbranch_scc1 1f\n\ts_cmp_eq_u32 %0, 9\n\ts_cbranch_scc0 2f\n\t"
               "s_mov_b32 m0, %5\n\ts_nop 0\n\tglobal_load_lds_dword %1, %2\n\t2:\n\ts_cmp_eq_u32 %0, 8\n\ts_cbranch_scc0 3f\n\t"
               "s_mov_b64 exec, 0xffffffff\n\tds_write_b32 %3, %4\n\ts_mov_b64 exec, -1\n\ts_waitcnt lgkmcnt(0)\n\t3:\n\ts_barrier\n\t1:"
               :: "s"(w), "v"(min(loc, CODE_KB - 1) * 1024 + l16), "s"(pc0), "v"(FL + 4 * lane), "v"(0u), "s"(R1) : "memory", "scc");
  unsigned xo_ = (row0 + 8 * hg + (r16 & 7)) * 1024 + 128 * w + 64 * (r16 >> 3) + 16 * g, po_ = ((ch0 + ((tid & 255) >> 7)) * 128 + (tid & 127)) * 2;
  asm volatile("s_waitcnt lgkmcnt(0)" : "+s"(ka_), "+s"(kb_), "+v"(xo_), "+v"(po_) :: "memory");
  const u16 *u = (const u16*)ka_[0], *Are = (const u16*)ka_[1], *Aim = (const u16*)ka_[2], *Bre = (const u16*)ka_[3], *Bim = (const u16*)ka_[4], *Cre = (const u16*)ka_[5], *Cim = (const u16*)ka_[6], *logdt = (const u16*)ka_[7];
  const u16 *Dsk = (const u16*)kb_[0], *x = (const u16*)kb_[1], *wq = (const u16*)kb_[2], *wk = (const u16*)kb_[3], *wv = (const u16*)kb_[4], *wo = (const u16*)kb_[5];
  u16* out = (u16*)kb_[6]; char* ws = (char*)kb_[7];
  TSR(0); TS(1);

  if (w >= 8) {
    const int a = w - 8;
    int lane = threadIdx.x & 63; PIN(lane);
    const int r16 = lane & 15, g = lane >> 4;
    const unsigned l16 = 16 * lane;
    const unsigned wvo = (lane >> 3) * 1024 + 16 * ((lane & 7) ^ (((lane >> 4) & 1) << 2));
    u32x2 ep; gld8_sc1(ep, ws + WS_EPOCH, 0);
    unsigned dsk; touch(Dsk, 2 * ch0, dsk);
    unsigned t0_ = 0, t1_;
    const int fid = FXCD ? 32 * xcd + loc : bid;
    __builtin_amdgcn_s_setprio(3);
    u64 tag; float k2 = 0.f;
    u32x4 ua0, ua1, ub0, ub1;
    if (a == 0) {
      const int kk0 = row0 + 8 * hg, t = kk0 >> 5, c = (kk0 >> 3) & 3;
      unsigned ko = WS_K + t * 1024 + ((g & 1) * 32 + 8 * c + r16) * 16 + 8 * (g >> 1), vo = WS_V + t * 1024 + 256 * c + 16 * r16 + 8 * (g & 1), rb = R2 + l16;
      PIN(ko); PIN(vo); PIN(rb);
      lds_spin(C_KV, 8);
      TS(3);
      f32x4 pk[8], pv[8];
#pragma unroll
      for (int s = 0; s < 8; ++s) { pk[s] = *LP(f32x4, rb + 4096 * s); pv[s] = *LP(f32x4, rb + 4096 * s + 1024); }
      SBAR();
      f32x4 dk = ((pk[0] + pk[1]) + (pk[2] + pk[3])) + ((pk[4] + pk[5]) + (pk[6] + pk[7])), dv = ((pv[0] + pv[1]) + (pv[2] + pv[3])) + ((pv[4] + pv[5]) + (pv[6] + pv[7]));
#pragma unroll
      for (int i = 0; i < 4; ++i) { dk[i] += dpp<0x128>(dk[i]); float o; dv[i] = x32(dv[i], o); dv[i] += o; }
      dk *= QSCALE;
      const u32x2 kp = {cvtpk(dk[0], dk[1]), cvtpk(dk[2], dk[3])}, vp = {cvtpk(dv[0], dv[1]), cvtpk(dv[2], dv[3])};
      if (r16 < 8) gst8_sc1(ws, ko, kp);
      if (g < 2) gst8_sc1(ws, vo, vp);
      lds_arrive(C_KVRD, lane);
      k2 = row_max(col_sum(dk[0] * dk[0] + dk[1] * dk[1] + dk[2] * dk[2] + dk[3] * dk[3]));
    } else {
      __builtin_amdgcn_s_sleep(AUX_DELAY);
      if (a < 3) touch(wq + 64 * hg, (128 * (loc >> 3) + 64 * (a - 1) + lane) * 1024, t0_);
      if (WGATE == 2) lds_spin(C_KVRD, 1); else if (WGATE == 1) lds_spin(C_KV, WCNT);
      _Pragma("unroll 1") for (int p0 = 4 * (a - 1); p0 < 64; p0 += 12) dma16x4v(wq + p0 * 4096 + 64 * hg, wvo, wvo + 7168, wvo + 2 * 7168, wvo + 3 * 7168, R1 + 1024 * p0);
    }
    if (a) { if (a == 1) WAITV(12); else WAITV(8); lds_arrive(C_W1, lane); }
    asm volatile("s_waitcnt vmcnt(0)" : "+v"(ep), "+v"(t0_), "+v"(dsk) :: "memory");
    TS(4);
    tag = (((u64)(unsigned)__builtin_amdgcn_readfirstlane(ep[1]) << 32) | (unsigned)__builtin_amdgcn_readfirstlane(ep[0])) + 1;
    if (a == 0) {
      put_flag(ws + WS_FLAG1, FSP, fid, lane, tag, __float_as_uint(k2));
      TS(5);
      if (POLL_CNT) lds_wait(POLL_GATE, POLL_CNT);
      __builtin_amdgcn_s_sleep(POLL_DELAY);
      const unsigned m = wave_umax(poll_flags(ws + WS_FLAG1, lane, tag));
      if (lane == 0) *LP(unsigned, R2 + 1028) = m;
      lds_arrive(C_F1, lane);
    } else lds_arrive(C_XW, lane);
    { const unsigned uvo = (lane >> 3) * 1024 + 16 * (lane & 7); const u16* ub = u + (row0 + 16 * ((a - 1) & 3)) * 512 + 64 * hg;
      asm volatile("s_cmp_eq_u32 %9, 0\n\ts_cbranch_scc1 1f\n\tglobal_load_dwordx4 %0, %4, %5\n\tglobal_load_dwordx4 %1, %4, %6\n\t"
                   "s_cmp_lg_u32 %9, 3\n\ts_cbranch_scc1 1f\n\tglobal_load_dwordx4 %2, %4, %7\n\tglobal_load_dwordx4 %3, %4, %8\n\t1:"
                   : "=&v"(ua0), "=&v"(ua1), "=&v"(ub0), "=&v"(ub1) : "v"(uvo), "s"(ub), "s"(ub + 4096), "s"(ub + 8192), "s"(ub + 12288), "s"(a) : "memory", "scc"); }
    asm volatile("s_waitcnt vmcnt(0)" : "+v"(ua0), "+v"(ua1), "+v"(ub0), "+v"(ub1) :: "memory");
    if (a) {
      const unsigned us = R2 + 4096 * (3 + a);
      unsigned ta = us + 1024 * (g & 1) + 128 * (r16 >> 2) + 32 * (g >> 1) + 8 * (lane & 3); PIN(ta);
      if (WGATE != 2) lds_spin(C_KVRD, 1);
      *LP(u32x4, us + l16) = ua0; *LP(u32x4, us + 1024 + l16) = ua1;
      ua0 = __builtin_bit_cast(u32x4, trd2(ta, ta + 512)); ua1 = __builtin_bit_cast(u32x4, trd2(ta + 64, ta + 64 + 512));
      *LP(u32x4, us + l16) = ub0; *LP(u32x4, us + 1024 + l16) = ub1;
      ub0 = __builtin_bit_cast(u32x4, trd2(ta, ta + 512)); ub1 = __builtin_bit_cast(u32x4, trd2(ta + 64, ta + 64 + 512));
      PIN(ua0); PIN(ua1); PIN(ub0); PIN(ub1);
    }
    const unsigned kvo = (a & 1) * 65536 + (a >> 1) * 32768;
    TS(7);
    lds_spin(C_F1, 1); lds_spin(C_QRD, 4);
    TS(8);
    const int ch = a >> 1;
    dma16x4_sc1(ws + WS_K + kvo, l16, kvo); dma16x4_sc1(ws + WS_K + kvo + 4096, l16, kvo + 4096);
    WAITV(4); lds_arrive(C_R0, lane);
    TS(9);
    if (a) {
      char* ut = ws + WS_UT + (64 * hg) * 4096 + (row0 + 16 * (a - 1)) * 2;
      const unsigned so = (16 * (g >> 1) + r16) * 4096 + 16 * (g & 1);
      gst16_sc1<0>(ut, so, ua0); gst16_sc1<0>(ut + 32 * 4096, so, ua1);
      if (a == 3) { gst16_sc1<32>(ut, so, ub0); gst16_sc1<32>(ut + 32 * 4096, so, ub1); WAITV(4); } else WAITV(2);
    } else WAITV(0);
    lds_arrive(C_R1, lane);
    _Pragma("unroll 1") for (int r = 2; r < 5; ++r) { __builtin_amdgcn_s_sleep(R_PACE); dma16x4_sc1_slow(ws + WS_K + kvo + 4096 * r, l16, kvo + 4096 * r); }
    WAITV(0); lds_arrive(C_R4, lane);
    if (a == 0) {
      lds_poll<1>(C_R4, 4);
      put_flag(ws + WS_FLAG2, 128, bid, lane, tag, 0); if (bid == 0 && lane == 0) gst8_sc1(ws, WS_EPOCH, (u32x2){(unsigned)tag, (unsigned)(tag >> 32)});
    }
    lds_poll<AUX_SLP>(C_PART, 8);
    _Pragma("unroll 1") for (int c = a == 3; c < (a == 2 ? 1 : 2); ++c) {
      const int tau = a < 2 ? 1 - a : 2;
      unsigned pb = c * R1 + PART + 1024 * tau + l16; PIN(pb);
      f32x4 v = *LP(f32x4, pb);
#pragma unroll
      for (int s = 1; s < 4; ++s) v += *LP(f32x4, pb + 3072 * s);
      if (c == 0) {
#pragma unroll
        for (int s = 0; s < 4; ++s) v += *LP(f32x4, pb + 32768 + 3072 * s);
      }
      const unsigned kb = KA + KA_STRIDE * c + 2 * 2055;
      const unsigned c01 = cvtpk(v[0], v[1]), c23 = cvtpk(v[2], v[3]);
      const u16 b0 = (u16)c01, b1 = (u16)(c01 >> 16), b2 = (u16)c23, b3 = (u16)(c23 >> 16);
      if (tau == 0) {
        const unsigned p = kb - 2 * (4 * g + 4 + 16 * r16);
        *LP(u16, p + 6) = b0; *LP(u16, p + 4) = b1; *LP(u16, p + 2) = b2; *LP(u16, p) = b3;
        if (lane == 63) *LP(u16, p - 2) = b3;
      } else if (tau == 1) {
        const bool up = g >= 2;
        const unsigned a0 = kb - 2 * ((up ? 516 + 256 * (g - 2) : 258 + 8 * g) + 16 * r16) - 8, st = up ? 8 : 4;
        const unsigned a1 = a0 - st, a2 = a1 - st, a3 = a2 - st;
        *LP(u16, a0 + 8) = b0; *LP(u16, a2 + 8) = b2;
        *LP(u16, a1 + 6) = b1; *LP(u16, a1 + 8) = b1; *LP(u16, a1 + 10) = b1;
        *LP(u16, a3 + 6) = b3; *LP(u16, a3 + 8) = b3; *LP(u16, a3 + 10) = b3;
        if (up) {
          *LP(u16, a0 + 6) = b0; *LP(u16, a0 + 10) = b0; *LP(u16, a2 + 6) = b2; *LP(u16, a2 + 10) = b2;
          *LP(u16, a1 + 4) = b1; *LP(u16, a1 + 12) = b1; *LP(u16, a3 + 4) = b3; *LP(u16, a3 + 12) = b3;
        }
        if (lane == 31) *LP(u16, a3 + 4) = b3;
        if (lane == 63) { *LP(u16, a3 + 2) = b3; *LP(u16, a3) = b3; }
      } else if (g < 2) {
        const unsigned p = kb - 2 * (1032 + 512 * (g & 1) + 16 * r16) - 536;
        const unsigned d0 = cvtpk(v[0], v[0]), d1 = cvtpk(v[1], v[1]), d2 = cvtpk(v[2], v[2]), d3 = cvtpk(v[3], v[3]);
        *LP(u32x2, p + 536 - 6) = (u32x2){d0, d0}; *LP(unsigned, p + 536 + 2) = d0; *LP(u16, p + 536 + 6) = b0;
        *LP(u32x2, p + 520 - 6) = (u32x2){d1, d1}; *LP(u32x2, p + 520 + 2) = (u32x2){d1, d1}; *LP(u16, p + 520 - 8) = b1;
        *LP(u32x2, p + 24 - 6) = (u32x2){d2, d2}; *LP(unsigned, p + 24 + 2) = d2; *LP(u16, p + 24 + 6) = b2;
        *LP(u32x2, p + 8 - 6) = (u32x2){d3, d3}; *LP(u32x2, p + 8 + 2) = (u32x2){d3, d3}; *LP(u16, p) = b3;
      } else if (lane == 32) *LP(u16, kb) = (u16)cvtpk(v[0] + (c ? bfhi(dsk) : bflo(dsk)), 0.f);
    }
    if (a >= 2) {
      if (lane < 60) *LP(u32x2, KA + KA_STRIDE * (a & 1) + 2 * 2056 + 8 * lane) = (u32x2){0, 0};
    }
    lds_arrive(C_KTAB, lane);
    TS(10);
    if (a == 0) { poll_some(ws + WS_FLAG2, (lane & 7) + 8 * (8 * ((lane >> 3) & 3) + xcd), tag); lds_arrive(C_F2, lane); }
    else lds_poll<AUX_SLP>(C_F2, 1);
    {
      const char* us = ws + WS_UT + (ch0 + ch) * 4096 + (a & 1) * 2048;
      u32x4 uv[2]; unsigned up[2];
      gld16_sc1<0>(uv[0], us, l16); gld16_sc1<1024>(uv[1], us, l16); gld4_sc1<-4>(up[0], us, l16); gld4_sc1<1020>(up[1], us, l16);
      lds_poll<AUX_SLP>(C_KTAB, 4);
      _Pragma("unroll 1") for (int r = 5; r < 8; ++r) dma16x4_sc1_slow(ws + WS_K + kvo + 4096 * r, l16, kvo + 4096 * r);
      asm volatile("s_waitcnt vmcnt(12)" : "+v"(uv[0]), "+v"(uv[1]), "+v"(up[0]), "+v"(up[1]) :: "memory");
      if (lane == 0 && !(a & 1)) up[0] = 0;
      if (lane < 2) *LP(u32x4, UC + UC_STRIDE * a + 16 * lane) = (u32x4){0, 0, 0, 0};
      unsigned ub = UC + UC_STRIDE * 2 * ch + 32 + (a & 1) * 2048 + l16; PIN(ub);
#pragma unroll
      for (int i = 0; i < 2; ++i) {
        *LP(u32x4, ub + 1024 * i) = uv[i];
        *LP(u32x4, ub + UC_STRIDE + 1024 * i) = (u32x4){__builtin_amdgcn_alignbit(uv[i][0], up[i], 16), __builtin_amdgcn_alignbit(uv[i][1], uv[i][0], 16),
                                                        __builtin_amdgcn_alignbit(uv[i][2], uv[i][1], 16), __builtin_amdgcn_alignbit(uv[i][3], uv[i][2], 16)};
      }
    }
    lds_arrive(C_UC, lane);
    WAITV(0); lds_arrive(C_R7, lane);
    { char* os = ws + WS_O + (rg * 64 + 32 * (a >> 1) + 4 * hg + 2 * (a & 1)) * 1024; const u32x4 sent = {SENT, SENT, SENT, SENT};
      gst16_sc1<0>(os, l16, sent); gst16_sc1<1024>(os, l16, sent); }
    touch(wo + 64 * hg, (128 * (loc >> 3) + 32 * a + (lane & 31)) * 1024, t1_);
    lds_poll<AUX_SLP>(C_UC, 4);
    TS(2);
    {
      const int cs = a & 1;
      unsigned ba = KA + KA_STRIDE * ch + 2 * (2040 - 16 * r16 + 8 * g) - 3584;
      const unsigned aa = UC + UC_STRIDE * (2 * ch + 1 - (r16 & 1)) + 2 * (2 + (r16 & 14) + 8 * g);
      f32x4 y0 = {0, 0, 0, 0}, y1 = y0, y2 = y0, y3 = y0;
      unsigned a0 = aa, a1 = aa + 1024, a2 = aa + 2048, a3 = aa + 3072, cnt;
#define CONV(L) asm volatile(L : "+v"(y0), "+v"(y1), "+v"(y2), "+v"(y3), "+v"(a0), "+v"(a1), "+v"(a2), "+v"(a3), "+v"(ba), "=&s"(cnt) :: "memory", "scc", CONV_CLOB)
      if (cs == 0) CONV(CONV_0); else CONV(CONV_1);
      char* yb = ws + WS_YT + (ch0 + ch) * 4096; const unsigned yo = 32 * r16 + 8 * g;
      gst8_sc1(yb, yo + 512 * (cs ? 1 : 0), (u32x2){cvtpk(y0[0], y0[1]), cvtpk(y0[2], y0[3])});
      gst8_sc1(yb, yo + 512 * (cs ? 2 : 3), (u32x2){cvtpk(y1[0], y1[1]), cvtpk(y1[2], y1[3])});
      gst8_sc1(yb, yo + 512 * (cs ? 5 : 4), (u32x2){cvtpk(y2[0], y2[1]), cvtpk(y2[2], y2[3])});
      gst8_sc1(yb, yo + 512 * (cs ? 6 : 7), (u32x2){cvtpk(y3[0], y3[1]), cvtpk(y3[2], y3[3])});
    }
    TS(11);
    asm volatile("s_waitcnt vmcnt(0)" : "+v"(t1_) :: "memory"); lds_arrive(C_Y, lane);
    if (a == 0) { lds_poll<1>(C_Y, 4); put_flag(ws + WS_FLAG3, 128, bid, lane, tag, 0); }
    TS(12);
    lds_poll<AUX_SLP>(C_PROG, 8);
    _Pragma("unroll 1") for (int j = 0; j < 4; ++j)
      dma16x4v_slow(wo + (16 * a + 4 * j) * 4096 + 64 * hg, wvo, wvo + 7168, wvo + 2 * 7168, wvo + 3 * 7168, 32768 * a + 4096 * j);
    if (a == 0) { poll_some(ws + WS_FLAG3, lane < 32 ? hg + 8 * lane : xcd + 8 * ((loc & 24) + (lane & 7)), tag); lds_arrive(C_F3, lane); }
    else lds_poll<AUX_SLP>(C_F3, 1);
    {
      const char* ys = ws + WS_YT + (64 * hg) * 4096 + (row0 + 16 * a) * 2; const unsigned yo = (lane >> 1) * 4096 + 16 * (lane & 1);
      dma16_sc1(ys, yo, YS + 2048 * a); dma16_sc1(ys + 32 * 4096, yo, YS + 2048 * a + 1024);
      const unsigned ta = YS + 2048 * a + 256 * g + 8 * r16, oo = (row0 + 16 * a + r16) * 2048 + 128 * hg + 16 * g;
      WAITV(0);
      lds_arrive(C_STG, lane);
      gst16_sc1<0>(out, oo, __builtin_bit_cast(u32x4, trd2(ta, ta + 128)));
      gst16_sc1<64>(out, oo, __builtin_bit_cast(u32x4, trd2(ta + 1024, ta + 1152)));
    }
    TS(13);
    TS_FLUSH();
    return;
  }

  const unsigned slice = R2 + 4096 * w;
  u32x4 xk;
  gld16<0>(xk, x, xo_);
  dma16x2(wk + 1024 * w, l16, WST + 4096 * w); dma16x2(wv + 1024 * w, l16, WST + 4096 * w + 2048);
  unsigned p_ar, p_ai, p_br, p_bi, p_cr, p_ci, p_dt;
  { const unsigned vo = po_, d = po_ >> 8;
    asm volatile("s_barrier\n\ts_cmp_lt_u32 %16, 4\n\ts_cbranch_scc1 1f\n\t"
                 "global_load_ushort %0, %7, %9\n\tglobal_load_ushort %1, %7, %10\n\tglobal_load_ushort %2, %7, %11\n\tglobal_load_ushort %3, %7, %12\n\t"
                 "global_load_ushort %4, %7, %13\n\tglobal_load_ushort %5, %7, %14\n\tglobal_load_ushort %6, %8, %15\n\ts_waitcnt vmcnt(7)\n\ts_branch 2f\n\t"
                 "1:\n\ts_waitcnt vmcnt(0)\n\t2:"
                 : "=&v"(p_ar), "=&v"(p_ai), "=&v"(p_br), "=&v"(p_bi), "=&v"(p_cr), "=&v"(p_ci), "=&v"(p_dt)
                 : "v"(vo), "v"(d * 2), "s"(Are), "s"(Aim), "s"(Bre), "s"(Bim), "s"(Cre), "s"(Cim), "s"(logdt), "s"(w) : "memory", "scc"); }
  asm volatile("" : "+v"(xk) :: "memory");
  TS(2);
  {
    const unsigned ta = WST + 4096 * w + 256 * g + 8 * r16;
    bf16x8 wk0 = trd2(ta, ta + 128), wk1 = trd2(ta + 1024, ta + 1152), wv0 = trd2(ta + 2048, ta + 2176), wv1 = trd2(ta + 3072, ta + 3200);
    const f32x4 z4 = {0, 0, 0, 0};
    const f32x4 dk0 = MFMA16(wk0, AS8(xk), z4), dk1 = MFMA16(wk1, AS8(xk), z4), dv0 = MFMA16(AS8(xk), wv0, z4), dv1 = MFMA16(AS8(xk), wv1, z4);
    const f32x4 dk = r16 < 8 ? dk0 : dk1, dv = g < 2 ? dv0 : dv1;
    *LP(f32x4, slice + l16) = dk; *LP(f32x4, slice + 1024 + l16) = dv;
    lds_arrive(C_KV, lane);
  }
  TS(3);
  asm volatile("s_waitcnt vmcnt(0)" : "+v"(p_ar), "+v"(p_ai), "+v"(p_br), "+v"(p_bi), "+v"(p_cr), "+v"(p_ci), "+v"(p_dt) :: "memory");
  if (w < 4) {
    lds_spin(XGATE ? C_KVRD : C_KV, XGATE ? 1 : 8);
    __builtin_amdgcn_s_setprio(3);
    const u16* xb = x + (row0 + 16 * w) * 512;
#pragma unroll
    for (int j = 0; j < 4; ++j) dma16x4v(xb + 2048 * j, l16 ^ (64 * j), l16 ^ (64 * j + 16), l16 ^ (64 * j + 32), l16 ^ (64 * j + 48), R0 + 16384 * w + 4096 * j);
    __builtin_amdgcn_s_setprio(0);
  } else {
    const float dt = bflo(cvtpk(expf(bflo(p_dt)), 0.f));
    const float al = (dt * bflo(p_ar)) * 1.4426950408889634f;
    float be = (dt * bflo(p_ai)) * 0.15915494309189535f; be -= __builtin_rintf(be);
    const float br = dt * bflo(p_br), bi = dt * bflo(p_bi), cr = bflo(p_cr), ci = bflo(p_ci);
    *LP(f32x4, slice + 2048 + l16) = (f32x4){al, be, cr * br - ci * bi, -(cr * bi + ci * br)};
    lds_arrive(C_PAR, lane);
  }
  lds_spin(C_PAR, 4);
  TS(4);
  auto kstep = [&](const f32x4* PP, f32x4* acc) {
    const float eB = (float)(16 * r16), e1 = (float)(r16 + 1);
    const float e2 = (float)(r16 < 8 ? 258 + 2 * r16 : 516 + 256 * ((r16 - 8) >> 2) + 4 * (r16 & 3));
    const float e3 = (float)(r16 < 8 ? 1032 + 256 * (r16 >> 1) + 8 * (r16 & 1) : 0);
    u32x4 fb, f1, f2, f3;
#pragma unroll
    for (int i = 0; i < 4; ++i) {
      const f32x4 P = PP[i];
      const f32x2 ab = {P[0], P[1]};
      const f32x2 xb = cmul((f32x2){P[2], P[3]}, zpow<false>(ab, (f32x2){eB, -eB})); fb[i] = cvtpk(xb.x, xb.y);
      const f32x2 a1 = zpow<false>(ab, (f32x2){e1, e1}); f1[i] = cvtpk(a1.x, a1.y);
      const f32x2 a2 = zpow<true>(ab, (f32x2){e2, e2}); f2[i] = cvtpk(a2.x, a2.y);
      const f32x2 a3 = zpow<true>(ab, (f32x2){e3, e3}); f3[i] = cvtpk(a3.x, a3.y);
    }
    acc[0] = MFMA16(AS8(f1), AS8(fb), acc[0]); acc[1] = MFMA16(AS8(f2), AS8(fb), acc[1]); acc[2] = MFMA16(AS8(f3), AS8(fb), acc[2]);
    PIN(acc[0]); PIN(acc[1]); PIN(acc[2]);
  };
  f32x4 kc0[3] = {{0, 0, 0, 0}, {0, 0, 0, 0}, {0, 0, 0, 0}}, kc1[3] = {{0, 0, 0, 0}, {0, 0, 0, 0}, {0, 0, 0, 0}};
  auto ldp = [&](unsigned pa, f32x4* P) { PIN(pa);
#pragma unroll
    for (int i = 0; i < 4; ++i) P[i] = *LP(f32x4, pa + 16 * i); };
  f32x4 P0[4]; ldp(R2 + 4096 * (4 + (w >> 2)) + 2048 + 256 * (w & 3) + 64 * g, P0);
  if (w < 4) {
    kstep(P0, kc0);
    TS(5);
    WAITV(0);
    lds_arrive(C_X, lane);
    const int rh = w >> 1, hp = w & 1;
    const unsigned xa = R0 + 1024 * (32 * rh + r32) + 16 * (h ^ r16);
    const int q = r16 >> 2, p = lane & 3;
    unsigned wa = R1 + 128 * (8 * h + q) + 16 * ((4 * hp + 2 * (g & 1) + (p >> 1)) ^ ((q >> 1) << 2)) + 8 * (p & 1); PIN(wa);
    f32x16 acc = {0};
    bf16x8 fa[6], fx[6];
    unsigned xs[8];
#pragma unroll
    for (int i = 0; i < 8; ++i) { xs[i] = xa ^ (i << 5); PIN(xs[i]); }
#define QLD(ks) do { fx[(ks) % 6] = *LP(bf16x8, xs[(ks) & 7] + 256 * ((ks) >> 3)); fa[(ks) % 6] = trd2(wa + 2048 * (ks), wa + 2048 * (ks) + 512); } while (0)
    lds_spin(C_X, 4); lds_spin(C_W1, 3);
    __builtin_amdgcn_s_setprio(QPRIO);
    TS(6);
    QLD(0); QLD(1); QLD(2); QLD(3); SBAR();
#pragma unroll
    for (int ks = 0; ks < 32; ++ks) {
      if (ks == 14) lds_spin(C_XW, 3);
      if (ks + 4 < 32) QLD(ks + 4);
      if (ks == 27) lds_arrive(C_QRD, lane);
      SBAR(); acc = MFMA32(fa[ks % 6], fx[ks % 6], acc); SBAR();
    }
    *LP(u32x4, slice + 2048 + l16) = (u32x4){cvtpk(acc[0], acc[1]), cvtpk(acc[2], acc[3]), cvtpk(acc[4], acc[5]), cvtpk(acc[6], acc[7])};
    *LP(u32x4, slice + 3072 + l16) = (u32x4){cvtpk(acc[8], acc[9]), cvtpk(acc[10], acc[11]), cvtpk(acc[12], acc[13]), cvtpk(acc[14], acc[15])};
    f32x2 n0 = {0, 0}, n1 = {0, 0};
#pragma unroll
    for (int i = 0; i < 8; i += 2) { const f32x2 t0 = {acc[i], acc[i + 1]}, t1 = {acc[8 + i], acc[9 + i]}; n0 += t0 * t0; n1 += t1 * t1; }
    const unsigned nm = wave_umax(max(__float_as_uint(n0.x + n0.y), __float_as_uint(n1.x + n1.y)));
    if (WGATE != 2) lds_spin(C_KVRD, 1);
    if (lane == 0) *LP(unsigned, slice + 1024) = nm;
    lds_arrive(C_Q, lane);
  } else {
    f32x4 P1[4], P2[4];
    __builtin_amdgcn_s_setprio(KPRIO);
    ldp(R2 + 4096 * (6 + ((w - 4) >> 1)) + 2048 + 512 * (w & 1) + 64 * g, P1); ldp(R2 + 4096 * (6 + ((w - 4) >> 1)) + 2048 + 512 * (w & 1) + 256 + 64 * g, P2);
    kstep(P0, kc0); SBAR();
    if (w == 4) {
      lds_spin(C_KVRD, 1);
      if (lane < 12) { const unsigned j = lane >> 2, o1 = j ? 0u : 0x3f803f80u; *LP(u32x4, ONES + 512 * (lane & 3) + (j == 2 ? 64 : 16 * j)) = (u32x4){o1, o1, o1, o1}; }
      lds_arrive(C_Q, lane);
    }
    kstep(P1, kc1); SBAR(); kstep(P2, kc1);
  }
  TS(7);
  lds_spin(C_Q, 5); lds_spin(C_F1, 1);
  TS(8);
  const int qt = w & 3, kh = w >> 2;
  const bf16x8 qB = *LP(bf16x8, R2 + 4096 * qt + 2048 + 1024 * kh + l16), qA = *LP(bf16x8, R2 + 4096 * qt + 3072 - 1024 * kh + l16);
  unsigned nb = R2 + 1024; PIN(nb);
  const unsigned q2 = max(max(*LP(unsigned, nb), *LP(unsigned, nb + 4096)), max(*LP(unsigned, nb + 8192), *LP(unsigned, nb + 12288)));
  const float sh = __builtin_amdgcn_sqrtf(2.04f * __uint_as_float(q2) * *LP(float, nb + 4));
  const bool unsafe = (unsigned)__builtin_amdgcn_readfirstlane(__float_as_int(sh)) > __float_as_uint(SAFE_BOUND);
  __builtin_amdgcn_s_setprio(0);
  TS(9);
  f32x16 oB = {0};
  f32x4 pa, pb; float pl;
  const unsigned cbw = R2 + 8192 + 2304 * w, cbr = R2 + 8192 + 2304 * (w ^ 4);
  {
    const bool lo = (lane & 16) == 0;
    unsigned kp = R0 + 32768 * kh + l16;
    unsigned vp = lo ? R1 + 32768 * kh + 256 * h + 16 * r16 : ONES + (r32 == 16 ? 0 : (r32 >= 20 && r32 < 28) ? 64 : 16);
    const unsigned vinc = lo ? 2048 : 0, pba = 32768 * kh + PART + 3072 * qt + l16;
    if (__builtin_expect(unsafe, 0)) {
      lds_wait(C_R0, 4);
#pragma unroll
      for (int i = 0; i < 3; ++i) { *LP(f32x4, pba + 1024 * i) = kc0[i]; if (kh) *LP(f32x4, pba + 32768 + 1024 * i) = kc1[i]; }
      lds_arrive(C_PART, lane);
    }
    asm volatile("" ::: "memory");
    if (__builtin_expect(unsafe, 0)) {
      int lane = threadIdx.x & 63; PIN(lane); const unsigned l16 = 16 * lane;
      lds_wait(C_R7, 4);
      const f32x16 z = {0};
      f32x16 oA = {0};
      float mA = -INFINITY, mB = -INFINITY;
      _Pragma("unroll 1") for (int t = 0; t < 64; ++t) {
        const bf16x8 kf = *LP(bf16x8, R0 + 1024 * t + l16);
        const f32x16 sa = MFMA32(kf, qA, z), sb = MFMA32(kf, qB, z);
#pragma unroll
        for (int i = 0; i < 16; ++i) { mA = fmaxf(mA, sa[i]); mB = fmaxf(mB, sb[i]); }
      }
      float o; mA = x32(mA, o); mA = fmaxf(mA, o); mB = x32(mB, o); mB = fmaxf(mB, o);
      _Pragma("unroll 1") for (int t = 0; t < 64; ++t) {
        const f32x16 sc = MFMA32(*LP(bf16x8, kp), (t & 1) ? qB : qA, z);
        const float m = (t & 1) ? mB : mA;
        f32x8 p0, p1;
#pragma unroll
        for (int i = 0; i < 8; ++i) { p0[i] = EX2(sc[i] - m); p1[i] = EX2(sc[8 + i] - m); }
        const bf16x8 v0 = *LP(bf16x8, vp), v1 = *LP(bf16x8, vp + 512);
        if (t & 1) { oB = PV(v0, p0, oB); oB = PV(v1, p1, oB); kp += 1024; vp += vinc >> 1; }
        else { oA = PV(v0, p0, oA); oA = PV(v1, p1, oA); }
      }
      lds_arrive(C_PROG, lane); lds_wait(C_Y, 4);
      *LP(f32x4, cbw + l16) = (f32x4){oA[0], oA[1], oA[2], oA[3]}; *LP(f32x4, cbw + 1024 + l16) = (f32x4){oA[4], oA[5], oA[6], oA[7]}; *LP(float, cbw + 2048 + 4 * lane) = oA[8];
      lds_arrive(C_CMB + 4 * qt, lane); lds_wait(C_CMB + 4 * qt, 2);
      pa = *LP(f32x4, cbr + l16); pb = *LP(f32x4, cbr + 1024 + l16); pl = *LP(float, cbr + 2048 + 4 * lane);
    } else {
      bf16x8 vA0, vA1, vB0, vB1; unsigned scnt, sstg, stmp;
#define ATT(LOOP) asm volatile("s_nop 1\n\t" LOOP \
                 : "+v"(oB), "+v"(kp), "+v"(vp), "=&v"(pa), "=&v"(pb), "=&v"(vA0), "=&v"(vA1), "=&v"(vB0), "=&v"(vB1), "=&v"(pl), "=&s"(scnt), "=&s"(sstg), "=&s"(stmp), \
                   "+{v[136:139]}"(kc0[0]), "+{v[140:143]}"(kc0[1]), "+{v[144:147]}"(kc0[2]), "+{v[148:151]}"(kc1[0]), "+{v[152:155]}"(kc1[1]), "+{v[156:159]}"(kc1[2]) \
                 : "v"(qA), "v"(qB), "v"(vinc), "v"(FL), "v"(pba), "s"(cbw), "s"(cbr), "s"(C_CMB + 4 * qt), "v"(0xddddu), "s"(w) \
                 : "memory", "scc", ATT_CLOB)
      ATT(ATT_LOOP);
    }
  }
  TS(10);
  int lane2 = threadIdx.x & 63; PIN(lane2);
  {
    const unsigned l16 = 16 * lane2;
    const float rl = __builtin_amdgcn_rcpf(oB[8] + pl);
    float o; const float r0 = x32(rl, o);
    u32x4 of_ = {cvtpk((oB[0] + pa[0]) * r0, (oB[1] + pa[1]) * r0), cvtpk((oB[2] + pa[2]) * r0, (oB[3] + pa[3]) * r0),
                 cvtpk((oB[4] + pb[0]) * r0, (oB[5] + pb[1]) * r0), cvtpk((oB[6] + pb[2]) * r0, (oB[7] + pb[3]) * r0)};
#pragma unroll
    for (int i = 0; i < 4; ++i) of_[i] = min(of_[i], SENT - 1);
    gst16_sc1<0>(ws + WS_O + ((rg * 2 + (qt >> 1)) * 32 + 4 * hg + 2 * (qt & 1) + kh) * 1024, l16, of_);
  }
  lds_arrive(C_END, lane);
  lds_spin(C_STG, 4);
  TS(11);
  {
    const int lane = lane2, r16 = lane & 15, g = lane >> 4, r32 = lane & 31, h = lane >> 5; const unsigned l16 = 16 * lane;
    const int rh = w >> 2, kq = w & 3;
    const char* osrc = ws + WS_O + ((rg * 2 + rh) * 32 + 8 * kq) * 1024;
    const int q = r16 >> 2, p = lane & 3;
    unsigned wa = 32768 * kq + 128 * (4 * h + q) + 16 * ((2 * ((kq + (g & 1)) & 3) + (p >> 1)) ^ ((q >> 1) << 2)) + 8 * (p & 1); PIN(wa);
    const unsigned wb = wa ^ 64;
    bf16x8 fa[8], fb[8];
#pragma unroll
    for (int ks = 0; ks < 8; ++ks) { fa[ks] = trd2(wa + 2048 * ks, wa + 2048 * ks + 1024); fb[ks] = trd2(wb + 2048 * ks, wb + 2048 * ks + 1024); }
    SBAR();
    lds_spin(C_END, 8);
    __builtin_amdgcn_s_sleep(O_DELAY);
    TS(12);
    f32x16 c0, c1;
    u32x4 of[8];
#define LOAD_O() do { gld16_sc1<0>(of[0], osrc, l16); gld16_sc1<1024>(of[1], osrc, l16); gld16_sc1<2048>(of[2], osrc, l16); gld16_sc1<3072>(of[3], osrc, l16); \
      gld16_sc1<0>(of[4], osrc + 4096, l16); gld16_sc1<1024>(of[5], osrc + 4096, l16); gld16_sc1<2048>(of[6], osrc + 4096, l16); gld16_sc1<3072>(of[7], osrc + 4096, l16); } while (0)
#define OSTEP(ks, n) do { asm volatile("s_waitcnt vmcnt(" #n ")" : "+v"(of[ks]) :: "memory"); \
        c0 = MFMA32(fa[ks], AS8(of[ks]), ks ? c0 : z); c1 = MFMA32(fb[ks], AS8(of[ks]), ks ? c1 : z); mx = max(mx, max4(of[ks])); SBAR(); } while (0)
    LOAD_O();
    const f32x16 z = {0};
    unsigned mx = 0;
    OSTEP(0, 7); OSTEP(1, 6); OSTEP(2, 5); OSTEP(3, 4); OSTEP(4, 3); OSTEP(5, 2); OSTEP(6, 1); OSTEP(7, 0);
    _Pragma("unroll 1") for (unsigned spins = 0; spins < (1u << 20) && __any(mx == SENT); ++spins) {
      LOAD_O(); mx = 0;
      OSTEP(0, 7); OSTEP(1, 6); OSTEP(2, 5); OSTEP(3, 4); OSTEP(4, 3); OSTEP(5, 2); OSTEP(6, 1); OSTEP(7, 0);
    }
    TS(13);
    const unsigned rbase = 16384 + 8192 * rh + l16;
#pragma unroll
    for (int j = 1; j < 4; ++j) {
      const unsigned wr = rbase + 32768 * ((kq + j) & 3) + 2048 * j;
      const f32x16& c = j < 2 ? c0 : c1; const int o = 8 * (j & 1);
      *LP(f32x4, wr) = (f32x4){c[o], c[o + 1], c[o + 2], c[o + 3]}; *LP(f32x4, wr + 1024) = (f32x4){c[o + 4], c[o + 5], c[o + 6], c[o + 7]};
    }
    unsigned rd = rbase + 32768 * kq, oo = (row0 + 32 * rh + r32) * 2048 + 1024 + 128 * hg + 32 * kq + 16 * h; PIN(rd); PIN(oo);
    BAR();
    f32x4 ra[3], rb[3];
#pragma unroll
    for (int s = 1; s < 4; ++s) { ra[s - 1] = *LP(f32x4, rd + 2048 * s); rb[s - 1] = *LP(f32x4, rd + 2048 * s + 1024); }
    SBAR();
    const f32x4 a = ((f32x4){c0[0], c0[1], c0[2], c0[3]} + ra[0]) + (ra[1] + ra[2]), b = ((f32x4){c0[4], c0[5], c0[6], c0[7]} + rb[0]) + (rb[1] + rb[2]);
    auto s0 = __builtin_amdgcn_permlane32_swap(cvtpk(a[0], a[1]), cvtpk(b[0], b[1]), false, false);
    auto s1 = __builtin_amdgcn_permlane32_swap(cvtpk(a[2], a[3]), cvtpk(b[2], b[3]), false, false);
    gst16(out, oo, (u32x4){s0[0], s1[0], s0[1], s1[1]});
  }
  TS_FLUSH();
}

extern "C" void kernel_launch(void* const* d_in, const int* in_sizes, int n_in, void* d_out, int out_size, void* d_ws, size_t ws_size, hipStream_t stream) {
  (void)in_sizes; (void)n_in; (void)out_size; (void)ws_size;
  (void)hipFuncSetAttribute((const void*)fused_kernel, hipFuncAttributeMaxDynamicSharedMemorySize, LDS_BYTES);
  fused_kernel<<<256, 768, LDS_BYTES, stream>>>((const u16*)d_in[0], (const u16*)d_in[1], (const u16*)d_in[2], (const u16*)d_in[3], (const u16*)d_in[4],
      (const u16*)d_in[5], (const u16*)d_in[6], (const u16*)d_in[7], (const u16*)d_in[8], (const u16*)d_in[9], (const u16*)d_in[10], (const u16*)d_in[11],
      (const u16*)d_in[12], (const u16*)d_in[13], (u16*)d_out, (char*)d_ws);
}
```
